```python
import torch

def _softmax(x, axis=-1):
    return torch.softmax(x, dim=axis)
def _gelu(x):
    return torch.nn.functional.gelu(x, approximate="tanh")
def _silu(x):
    return torch.nn.functional.silu(x)
def _sigmoid(x):
    return torch.sigmoid(x)
def _layernorm(x, axis=-1, eps=1e-5):
    m = x.mean(dim=axis, keepdim=True)
    v = x.var(dim=axis, keepdim=True, unbiased=False)
    return (x - m) / torch.sqrt(v + eps)
def _scatter_add(tgt, idx, src, axis=0):
    return tgt.clone().index_add_(axis, idx, src)
def _segment_sum(src, seg, n):
    out = torch.zeros((n,) + src.shape[1:], dtype=src.dtype)
    return out.index_add_(0, seg, src)
def _pad(x, pw):
    flat = tuple(v for pair in reversed(pw) for v in pair)
    return torch.nn.functional.pad(x, flat)

kernel_name = 'concat_heads__gqa_block_cached__gqa_block_cached__near_ridge'

def setup_inputs():

    _w = (lambda a: (lambda t: t.to(torch.bfloat16) if t.is_floating_point() else t)(torch.as_tensor(a)))
    def _A_setup():
        B, Sq, D = 16, 32, 1024
        Hq, Hkv, Sk, Dh = 32, 4, 1024, 128
        assert Hq % Hkv == 0, (Hq, Hkv)
        return {
            "x": _w((lambda *s: torch.randn(*s, dtype=torch.bfloat16) * 0.8)(B, Sq, D)),
            "k_cache": _w((lambda *s: torch.randn(*s, dtype=torch.bfloat16) * 0.8)(B, Hkv, Sk, Dh)),
            "v_cache": _w((lambda *s: torch.randn(*s, dtype=torch.bfloat16) * 0.05)(B, Hkv, Sk, Dh)),
            "wq": _w((lambda *s: torch.randn(*s, dtype=torch.bfloat16) * 0.8)(D, Hq * Dh) * (D ** -0.5)),
            "wo": _w((lambda *s: torch.randn(*s, dtype=torch.bfloat16) * 0.05)(Hq * Dh, D) * ((Hq * Dh) ** -0.5)),
        }
    _A_in = _A_setup()

    def _B_setup():
        B, Sq, D = 16, 32, 1024
        Hq, Hkv, Sk, Dh = 32, 4, 1024, 128
        assert Hq % Hkv == 0, (Hq, Hkv)
        return {
            "x": _w((lambda *s: torch.randn(*s, dtype=torch.bfloat16) * 0.8)(B, Sq, D)),
            "k_cache": _w((lambda *s: torch.randn(*s, dtype=torch.bfloat16) * 0.8)(B, Hkv, Sk, Dh)),
            "v_cache": _w((lambda *s: torch.randn(*s, dtype=torch.bfloat16) * 0.05)(B, Hkv, Sk, Dh)),
            "wq": _w((lambda *s: torch.randn(*s, dtype=torch.bfloat16) * 0.8)(D, Hq * Dh) * (D ** -0.5)),
            "wo": _w((lambda *s: torch.randn(*s, dtype=torch.bfloat16) * 0.05)(Hq * Dh, D) * ((Hq * Dh) ** -0.5)),
        }
    _B_in = _B_setup()

    return {
        "A_x": _A_in["x"],
        "A_k_cache": _A_in["k_cache"],
        "A_v_cache": _A_in["v_cache"],
        "A_wq": _A_in["wq"],
        "A_wo": _A_in["wo"],
        "B_x": _B_in["x"],
        "B_k_cache": _B_in["k_cache"],
        "B_v_cache": _B_in["v_cache"],
        "B_wq": _B_in["wq"],
        "B_wo": _B_in["wo"],
    }

def reference(A_x, A_k_cache, A_v_cache, A_wq, A_wo, B_x, B_k_cache, B_v_cache, B_wq, B_wo):
    def _A_ref(x, k_cache, v_cache, wq, wo):
        B, Sq, D = x.shape
        Hq, Dh = 32, 128
        Hkv = k_cache.shape[1]
        rep = Hq // Hkv
        q = (lambda x, *d: x.permute(*d))((x @ wq).reshape(B, Sq, Hq, Dh), 0, 2, 1, 3)
        k = (lambda x, n, axis: torch.repeat_interleave(x, n, dim=axis))(k_cache, rep, axis=1)
        v = (lambda x, n, axis: torch.repeat_interleave(x, n, dim=axis))(v_cache, rep, axis=1)
        scores = torch.einsum("bhqd,bhkd->bhqk", q, k) * (Dh ** -0.5)
        p = _softmax(scores, axis=-1)
        o = torch.einsum("bhqk,bhkd->bhqd", p, v)
        return (lambda x, *d: x.permute(*d))(o, 0, 2, 1, 3).reshape(B, Sq, Hq * Dh) @ wo

    def _B_ref(x, k_cache, v_cache, wq, wo):
        B, Sq, D = x.shape
        Hq, Dh = 32, 128
        Hkv = k_cache.shape[1]
        rep = Hq // Hkv
        q = (lambda x, *d: x.permute(*d))((x @ wq).reshape(B, Sq, Hq, Dh), 0, 2, 1, 3)
        k = (lambda x, n, axis: torch.repeat_interleave(x, n, dim=axis))(k_cache, rep, axis=1)
        v = (lambda x, n, axis: torch.repeat_interleave(x, n, dim=axis))(v_cache, rep, axis=1)
        scores = torch.einsum("bhqd,bhkd->bhqk", q, k) * (Dh ** -0.5)
        p = _softmax(scores, axis=-1)
        o = torch.einsum("bhqk,bhkd->bhqd", p, v)
        return (lambda x, *d: x.permute(*d))(o, 0, 2, 1, 3).reshape(B, Sq, Hq * Dh) @ wo

    a = _A_ref(A_x, A_k_cache, A_v_cache, A_wq, A_wo)
    b = _B_ref(B_x, B_k_cache, B_v_cache, B_wq, B_wo)
    return torch.concatenate([a, b], axis=-1)
```

```cpp
#include <hip/hip_runtime.h>
#include <hip/hip_bf16.h>
typedef short bf16x8 __attribute__((ext_vector_type(8)));
typedef float f32x16 __attribute__((ext_vector_type(16)));
typedef unsigned short u16;
__device__ __forceinline__ float bf2f(u16 v) { return __uint_as_float(((unsigned)v) << 16); }
__device__ __forceinline__ u16 f2bf(float f) { __hip_bfloat16 b = __float2bfloat16(f); return *(u16*)&b; }

__global__ __launch_bounds__(256) void gemm_nn(const u16* A0, const u16* A1, const u16* B0, const u16* B1, u16* C0, u16* C1,
                                               int K, int N, int ldc, float alpha) {
  const u16* A = blockIdx.z ? A1 : A0; const u16* B = blockIdx.z ? B1 : B0; u16* C = blockIdx.z ? C1 : C0;
  __shared__ u16 As[64][40];
  __shared__ u16 Bs[64][40];
  const int tid = threadIdx.x, lane = tid & 63, w = tid >> 6, wr = w >> 1, wc = w & 1;
  const int m0 = blockIdx.y * 64, n0 = blockIdx.x * 64;
  f32x16 acc = {};
  for (int k0 = 0; k0 < K; k0 += 32) {
    { int r = tid >> 2, c = (tid & 3) * 8;
      *(bf16x8*)&As[r][c] = *(const bf16x8*)&A[(size_t)(m0 + r) * K + k0 + c]; }
    { int kk = tid >> 3, c = (tid & 7) * 8;
      bf16x8 v = *(const bf16x8*)&B[(size_t)(k0 + kk) * N + n0 + c];
      for (int j = 0; j < 8; ++j) Bs[c + j][kk] = (u16)v[j]; }
    __syncthreads();
    for (int s = 0; s < 2; ++s) {
      bf16x8 a = *(bf16x8*)&As[wr * 32 + (lane & 31)][s * 16 + (lane >> 5) * 8];
      bf16x8 b = *(bf16x8*)&Bs[wc * 32 + (lane & 31)][s * 16 + (lane >> 5) * 8];
      acc = __builtin_amdgcn_mfma_f32_32x32x16_bf16(a, b, acc, 0, 0, 0);
    }
    __syncthreads();
  }
  for (int r = 0; r < 16; ++r) {
    int row = (r & 3) + 8 * (r >> 2) + 4 * (lane >> 5);
    C[(size_t)(m0 + wr * 32 + row) * ldc + n0 + wc * 32 + (lane & 31)] = f2bf(acc[r] * alpha);
  }
}

__global__ __launch_bounds__(64) void attn(const u16* Q, const u16* K0, const u16* K1, const u16* V0, const u16* V1, u16* O) {
  const int h = blockIdx.x, b = blockIdx.y, br = blockIdx.z, lane = threadIdx.x, r32 = lane & 31, hi = lane >> 5;
  const u16* Kp = (br ? K1 : K0) + ((size_t)b * 4 + (h >> 3)) * 1024 * 128;
  const u16* Vp = (br ? V1 : V0) + ((size_t)b * 4 + (h >> 3)) * 1024 * 128;
  const u16* Qp = Q + ((size_t)br * 512 + b * 32) * 4096 + h * 128;
  u16* Op = O + ((size_t)br * 512 + b * 32) * 4096 + h * 128;
  bf16x8 q[8];
  for (int d = 0; d < 8; ++d) q[d] = *(const bf16x8*)&Qp[(size_t)r32 * 4096 + d * 16 + hi * 8];
  f32x16 o[4] = {};
  float m = -INFINITY, l = 0.f;
  for (int t = 0; t < 32; ++t) {
    f32x16 s = {};
    for (int d = 0; d < 8; ++d) {
      bf16x8 k = *(const bf16x8*)&Kp[(size_t)(t * 32 + r32) * 128 + d * 16 + hi * 8];
      s = __builtin_amdgcn_mfma_f32_32x32x16_bf16(k, q[d], s, 0, 0, 0);
    }
    float mx = s[0];
    for (int r = 1; r < 16; ++r) mx = fmaxf(mx, s[r]);
    mx = fmaxf(mx, __shfl_xor(mx, 32));
    float mn = fmaxf(m, mx), al = exp2f(m - mn); m = mn;
    float ps = 0.f;
    for (int r = 0; r < 16; ++r) { s[r] = exp2f(s[r] - mn); ps += s[r]; }
    ps += __shfl_xor(ps, 32);
    l = l * al + ps;
    for (int d = 0; d < 4; ++d) for (int r = 0; r < 16; ++r) o[d][r] *= al;
    for (int ks = 0; ks < 2; ++ks) {
      bf16x8 p;
      for (int j = 0; j < 8; ++j) p[j] = (short)f2bf(s[8 * ks + j]);
      for (int d = 0; d < 4; ++d) {
        bf16x8 v;
        for (int j = 0; j < 8; ++j) { int key = t * 32 + 16 * ks + 8 * (j >> 2) + 4 * hi + (j & 3); v[j] = (short)Vp[(size_t)key * 128 + d * 32 + r32]; }
        o[d] = __builtin_amdgcn_mfma_f32_32x32x16_bf16(v, p, o[d], 0, 0, 0);
      }
    }
  }
  float il = 1.f / l;
  for (int d = 0; d < 4; ++d) for (int r = 0; r < 16; ++r) {
    int dd = d * 32 + (r & 3) + 8 * (r >> 2) + 4 * hi;
    Op[(size_t)r32 * 4096 + dd] = f2bf(o[d][r] * il);
  }
}

extern "C" void kernel_launch(void* const* d_in, const int* in_sizes, int n_in, void* d_out, int out_size, void* d_ws, size_t ws_size, hipStream_t stream) {
  const u16* xA = (const u16*)d_in[0]; const u16* kA = (const u16*)d_in[1]; const u16* vA = (const u16*)d_in[2];
  const u16* wqA = (const u16*)d_in[3]; const u16* woA = (const u16*)d_in[4];
  const u16* xB = (const u16*)d_in[5]; const u16* kB = (const u16*)d_in[6]; const u16* vB = (const u16*)d_in[7];
  const u16* wqB = (const u16*)d_in[8]; const u16* woB = (const u16*)d_in[9];
  u16* Q = (u16*)d_ws; u16* O = Q + (size_t)2 * 512 * 4096; u16* out = (u16*)d_out;
  const float c = 0.08838834764831845f * 1.4426950408889634f;
  gemm_nn<<<dim3(64, 8, 2), 256, 0, stream>>>(xA, xB, wqA, wqB, Q, Q + (size_t)512 * 4096, 1024, 4096, 4096, c);
  attn<<<dim3(32, 16, 2), 64, 0, stream>>>(Q, kA, kB, vA, vB, O);
  gemm_nn<<<dim3(16, 8, 2), 256, 0, stream>>>(O, O + (size_t)512 * 4096, woA, woB, out, out + 1024, 4096, 1024, 2048, 1.f);
}
```

```cpp
#include <hip/hip_runtime.h>
#include <hip/hip_bf16.h>
typedef short bf16x8 __attribute__((ext_vector_type(8)));
typedef float f32x16 __attribute__((ext_vector_type(16)));
typedef unsigned short u16;
__device__ __forceinline__ float bf2f(u16 v) { return __uint_as_float(((unsigned)v) << 16); }
__device__ __forceinline__ u16 f2bf(float f) { __hip_bfloat16 b = __float2bfloat16(f); return *(u16*)&b; }

__global__ __launch_bounds__(256) void gemm_nn(const u16* A0, const u16* A1, const u16* B0, const u16* B1, u16* C0, u16* C1,
                                               int K, int N, int ldc, float alpha) {
  const u16* A = blockIdx.z ? A1 : A0; const u16* B = blockIdx.z ? B1 : B0; u16* C = blockIdx.z ? C1 : C0;
  __shared__ u16 As[64][40];
  __shared__ u16 Bs[64][40];
  const int tid = threadIdx.x, lane = tid & 63, w = tid >> 6, wr = w >> 1, wc = w & 1;
  const int m0 = blockIdx.y * 64, n0 = blockIdx.x * 64;
  f32x16 acc = {};
  for (int k0 = 0; k0 < K; k0 += 32) {
    { int r = tid >> 2, c = (tid & 3) * 8;
      *(bf16x8*)&As[r][c] = *(const bf16x8*)&A[(size_t)(m0 + r) * K + k0 + c]; }
    { int kk = tid >> 3, c = (tid & 7) * 8;
      bf16x8 v = *(const bf16x8*)&B[(size_t)(k0 + kk) * N + n0 + c];
      for (int j = 0; j < 8; ++j) Bs[c + j][kk] = (u16)v[j]; }
    __syncthreads();
    for (int s = 0; s < 2; ++s) {
      bf16x8 a = *(bf16x8*)&As[wr * 32 + (lane & 31)][s * 16 + (lane >> 5) * 8];
      bf16x8 b = *(bf16x8*)&Bs[wc * 32 + (lane & 31)][s * 16 + (lane >> 5) * 8];
      acc = __builtin_amdgcn_mfma_f32_32x32x16_bf16(a, b, acc, 0, 0, 0);
    }
    __syncthreads();
  }
  for (int r = 0; r < 16; ++r) {
    int row = (r & 3) + 8 * (r >> 2) + 4 * (lane >> 5);
    C[(size_t)(m0 + wr * 32 + row) * ldc + n0 + wc * 32 + (lane & 31)] = f2bf(acc[r] * alpha);
  }
}

__global__ __launch_bounds__(64) void attn(const u16* Q, const u16* K0, const u16* K1, const u16* V0, const u16* V1, u16* O) {
  const int h = blockIdx.x, b = blockIdx.y, br = blockIdx.z, lane = threadIdx.x, r32 = lane & 31, hi = lane >> 5;
  const u16* Kp = (br ? K1 : K0) + ((size_t)b * 4 + (h >> 3)) * 1024 * 128;
  const u16* Vp = (br ? V1 : V0) + ((size_t)b * 4 + (h >> 3)) * 1024 * 128;
  const u16* Qp = Q + ((size_t)br * 512 + b * 32) * 4096 + h * 128;
  u16* Op = O + ((size_t)br * 512 + b * 32) * 4096 + h * 128;
  bf16x8 q[8];
  for (int d = 0; d < 8; ++d) q[d] = *(const bf16x8*)&Qp[(size_t)r32 * 4096 + d * 16 + hi * 8];
  f32x16 o[4] = {};
  float m = -INFINITY, l = 0.f;
  for (int t = 0; t < 32; ++t) {
    f32x16 s = {};
    for (int d = 0; d < 8; ++d) {
      bf16x8 k = *(const bf16x8*)&Kp[(size_t)(t * 32 + r32) * 128 + d * 16 + hi * 8];
      s = __builtin_amdgcn_mfma_f32_32x32x16_bf16(k, q[d], s, 0, 0, 0);
    }
    float mx = s[0];
    for (int r = 1; r < 16; ++r) mx = fmaxf(mx, s[r]);
    mx = fmaxf(mx, __shfl_xor(mx, 32));
    float mn = fmaxf(m, mx), al = exp2f(m - mn); m = mn;
    float ps = 0.f;
    for (int r = 0; r < 16; ++r) { s[r] = exp2f(s[r] - mn); ps += s[r]; }
    ps += __shfl_xor(ps, 32);
    l = l * al + ps;
    for (int d = 0; d < 4; ++d) for (int r = 0; r < 16; ++r) o[d][r] *= al;
    for (int ks = 0; ks < 2; ++ks) {
      bf16x8 p;
      for (int j = 0; j < 8; ++j) p[j] = (short)f2bf(s[8 * ks + j]);
      for (int d = 0; d < 4; ++d) {
        bf16x8 v;
        for (int j = 0; j < 8; ++j) { int key = t * 32 + 16 * ks + 8 * (j >> 2) + 4 * hi + (j & 3); v[j] = (short)Vp[(size_t)key * 128 + d * 32 + r32]; }
        o[d] = __builtin_amdgcn_mfma_f32_32x32x16_bf16(v, p, o[d], 0, 0, 0);
      }
    }
  }
  float il = 1.f / l;
  for (int d = 0; d < 4; ++d) for (int r = 0; r < 16; ++r) {
    int dd = d * 32 + (r & 3) + 8 * (r >> 2) + 4 * hi;
    Op[(size_t)r32 * 4096 + dd] = f2bf(o[d][r] * il);
  }
}

#define NBLK 1024
#define MASK 0

typedef unsigned v4u __attribute__((ext_vector_type(4)));
__global__ __launch_bounds__(512) void rd(const v4u* a0, const v4u* a1, const v4u* a2, const v4u* a3, const v4u* a4,
    const v4u* b0, const v4u* b1, const v4u* b2, const v4u* b3, const v4u* b4, v4u* out) {
  const unsigned long long t0 = __builtin_amdgcn_s_memrealtime();
  const int tid = blockIdx.x * 512 + threadIdx.x; const int nth = NBLK * 512;
  v4u acc = {0, 0, 0, 0};
  const v4u* ps[10] = {a0, a1, a2, a3, a4, b0, b1, b2, b3, b4};
  const int n16[10] = {65536, 1048576, 1048576, 524288, 524288, 65536, 1048576, 1048576, 524288, 524288};
#pragma unroll
  for (int i = 0; i < 10; ++i) {
    if (!((MASK >> i) & 1)) continue;
#pragma unroll 8
    for (int j = tid; j < n16[i]; j += nth) acc ^= ps[i][j];
  }
  out[tid] = acc;
  __syncthreads();
  const unsigned long long t1 = __builtin_amdgcn_s_memrealtime();
  const unsigned long long tend = t1 + 100ull * (t1 - t0);
  while (__builtin_amdgcn_s_memrealtime() < tend) __builtin_amdgcn_s_sleep(8);
}
extern "C" void kernel_launch(void* const* d_in, const int* in_sizes, int n_in, void* d_out, int out_size, void* d_ws, size_t ws_size, hipStream_t stream) {
  const u16* xA = (const u16*)d_in[0]; const u16* kA = (const u16*)d_in[1]; const u16* vA = (const u16*)d_in[2];
  const u16* wqA = (const u16*)d_in[3]; const u16* woA = (const u16*)d_in[4];
  const u16* xB = (const u16*)d_in[5]; const u16* kB = (const u16*)d_in[6]; const u16* vB = (const u16*)d_in[7];
  const u16* wqB = (const u16*)d_in[8]; const u16* woB = (const u16*)d_in[9];
  u16* Q = (u16*)d_ws; u16* O = Q + (size_t)2 * 512 * 4096; u16* out = (u16*)d_out;
  const float c = 0.08838834764831845f * 1.4426950408889634f;

  rd<<<NBLK, 512, 0, stream>>>((const v4u*)d_in[0], (const v4u*)d_in[1], (const v4u*)d_in[2], (const v4u*)d_in[3], (const v4u*)d_in[4],
    (const v4u*)d_in[5], (const v4u*)d_in[6], (const v4u*)d_in[7], (const v4u*)d_in[8], (const v4u*)d_in[9], (v4u*)((char*)d_ws + (64u<<20)));
  gemm_nn<<<dim3(64, 8, 2), 256, 0, stream>>>(xA, xB, wqA, wqB, Q, Q + (size_t)512 * 4096, 1024, 4096, 4096, c);
  attn<<<dim3(32, 16, 2), 64, 0, stream>>>(Q, kA, kB, vA, vB, O);
  gemm_nn<<<dim3(16, 8, 2), 256, 0, stream>>>(O, O + (size_t)512 * 4096, woA, woB, out, out + 1024, 4096, 1024, 2048, 1.f);
}
```

```cpp
#include <hip/hip_runtime.h>
#include <hip/hip_bf16.h>
typedef short bf16x8 __attribute__((ext_vector_type(8)));
typedef float f32x16 __attribute__((ext_vector_type(16)));
typedef unsigned short u16;
__device__ __forceinline__ float bf2f(u16 v) { return __uint_as_float(((unsigned)v) << 16); }
__device__ __forceinline__ u16 f2bf(float f) { __hip_bfloat16 b = __float2bfloat16(f); return *(u16*)&b; }

__global__ __launch_bounds__(256) void gemm_nn(const u16* A0, const u16* A1, const u16* B0, const u16* B1, u16* C0, u16* C1,
                                               int K, int N, int ldc, float alpha) {
  const u16* A = blockIdx.z ? A1 : A0; const u16* B = blockIdx.z ? B1 : B0; u16* C = blockIdx.z ? C1 : C0;
  __shared__ u16 As[64][40];
  __shared__ u16 Bs[64][40];
  const int tid = threadIdx.x, lane = tid & 63, w = tid >> 6, wr = w >> 1, wc = w & 1;
  const int m0 = blockIdx.y * 64, n0 = blockIdx.x * 64;
  f32x16 acc = {};
  for (int k0 = 0; k0 < K; k0 += 32) {
    { int r = tid >> 2, c = (tid & 3) * 8;
      *(bf16x8*)&As[r][c] = *(const bf16x8*)&A[(size_t)(m0 + r) * K + k0 + c]; }
    { int kk = tid >> 3, c = (tid & 7) * 8;
      bf16x8 v = *(const bf16x8*)&B[(size_t)(k0 + kk) * N + n0 + c];
      for (int j = 0; j < 8; ++j) Bs[c + j][kk] = (u16)v[j]; }
    __syncthreads();
    for (int s = 0; s < 2; ++s) {
      bf16x8 a = *(bf16x8*)&As[wr * 32 + (lane & 31)][s * 16 + (lane >> 5) * 8];
      bf16x8 b = *(bf16x8*)&Bs[wc * 32 + (lane & 31)][s * 16 + (lane >> 5) * 8];
      acc = __builtin_amdgcn_mfma_f32_32x32x16_bf16(a, b, acc, 0, 0, 0);
    }
    __syncthreads();
  }
  for (int r = 0; r < 16; ++r) {
    int row = (r & 3) + 8 * (r >> 2) + 4 * (lane >> 5);
    C[(size_t)(m0 + wr * 32 + row) * ldc + n0 + wc * 32 + (lane & 31)] = f2bf(acc[r] * alpha);
  }
}

__global__ __launch_bounds__(64) void attn(const u16* Q, const u16* K0, const u16* K1, const u16* V0, const u16* V1, u16* O) {
  const int h = blockIdx.x, b = blockIdx.y, br = blockIdx.z, lane = threadIdx.x, r32 = lane & 31, hi = lane >> 5;
  const u16* Kp = (br ? K1 : K0) + ((size_t)b * 4 + (h >> 3)) * 1024 * 128;
  const u16* Vp = (br ? V1 : V0) + ((size_t)b * 4 + (h >> 3)) * 1024 * 128;
  const u16* Qp = Q + ((size_t)br * 512 + b * 32) * 4096 + h * 128;
  u16* Op = O + ((size_t)br * 512 + b * 32) * 4096 + h * 128;
  bf16x8 q[8];
  for (int d = 0; d < 8; ++d) q[d] = *(const bf16x8*)&Qp[(size_t)r32 * 4096 + d * 16 + hi * 8];
  f32x16 o[4] = {};
  float m = -INFINITY, l = 0.f;
  for (int t = 0; t < 32; ++t) {
    f32x16 s = {};
    for (int d = 0; d < 8; ++d) {
      bf16x8 k = *(const bf16x8*)&Kp[(size_t)(t * 32 + r32) * 128 + d * 16 + hi * 8];
      s = __builtin_amdgcn_mfma_f32_32x32x16_bf16(k, q[d], s, 0, 0, 0);
    }
    float mx = s[0];
    for (int r = 1; r < 16; ++r) mx = fmaxf(mx, s[r]);
    mx = fmaxf(mx, __shfl_xor(mx, 32));
    float mn = fmaxf(m, mx), al = exp2f(m - mn); m = mn;
    float ps = 0.f;
    for (int r = 0; r < 16; ++r) { s[r] = exp2f(s[r] - mn); ps += s[r]; }
    ps += __shfl_xor(ps, 32);
    l = l * al + ps;
    for (int d = 0; d < 4; ++d) for (int r = 0; r < 16; ++r) o[d][r] *= al;
    for (int ks = 0; ks < 2; ++ks) {
      bf16x8 p;
      for (int j = 0; j < 8; ++j) p[j] = (short)f2bf(s[8 * ks + j]);
      for (int d = 0; d < 4; ++d) {
        bf16x8 v;
        for (int j = 0; j < 8; ++j) { int key = t * 32 + 16 * ks + 8 * (j >> 2) + 4 * hi + (j & 3); v[j] = (short)Vp[(size_t)key * 128 + d * 32 + r32]; }
        o[d] = __builtin_amdgcn_mfma_f32_32x32x16_bf16(v, p, o[d], 0, 0, 0);
      }
    }
  }
  float il = 1.f / l;
  for (int d = 0; d < 4; ++d) for (int r = 0; r < 16; ++r) {
    int dd = d * 32 + (r & 3) + 8 * (r >> 2) + 4 * hi;
    Op[(size_t)r32 * 4096 + dd] = f2bf(o[d][r] * il);
  }
}

#define SHARE 1
#define AUX 2

typedef unsigned v4u __attribute__((ext_vector_type(4)));
__global__ __launch_bounds__(512) void rd(const v4u* kA, const v4u* vA, const v4u* kB, const v4u* vB, v4u* out) {
  __shared__ __attribute__((aligned(16))) char lds[65536];
  const unsigned long long t0 = __builtin_amdgcn_s_memrealtime();
  const int xcd = blockIdx.x & 7, idx = blockIdx.x >> 3;
  const int w = __builtin_amdgcn_readfirstlane(threadIdx.x >> 6);
  const int unit = (xcd & 3) * (32 / SHARE) + idx / SHARE;
  const size_t per16 = (size_t)1048576 / (128 / SHARE);
  const v4u* pk = (xcd < 4 ? kA : kB) + unit * per16 + threadIdx.x;
  const v4u* pv = (xcd < 4 ? vA : vB) + unit * per16 + threadIdx.x;
  const int n = per16 / 512;
  char* base = lds + w * 8192;
  int slot = 0;
  for (int i = 0; i < n; ++i) {
    __builtin_amdgcn_global_load_lds((const __attribute__((address_space(1))) void*)(pk + (size_t)i * 512), (__attribute__((address_space(3))) void*)(base + slot * 1024), 16, 0, AUX);
    __builtin_amdgcn_global_load_lds((const __attribute__((address_space(1))) void*)(pv + (size_t)i * 512), (__attribute__((address_space(3))) void*)(base + slot * 1024 + 1024), 16, 0, AUX);
    slot = (slot + 2) & 7;
    asm volatile("s_waitcnt vmcnt(6)" ::: "memory");
  }
  asm volatile("s_waitcnt vmcnt(0)" ::: "memory");
  __syncthreads();
  const unsigned long long t1 = __builtin_amdgcn_s_memrealtime();
  const unsigned long long tend = t1 + 100ull * (t1 - t0);
  while (__builtin_amdgcn_s_memrealtime() < tend) __builtin_amdgcn_s_sleep(8);
}
extern "C" void kernel_launch(void* const* d_in, const int* in_sizes, int n_in, void* d_out, int out_size, void* d_ws, size_t ws_size, hipStream_t stream) {
  const u16* xA = (const u16*)d_in[0]; const u16* kA = (const u16*)d_in[1]; const u16* vA = (const u16*)d_in[2];
  const u16* wqA = (const u16*)d_in[3]; const u16* woA = (const u16*)d_in[4];
  const u16* xB = (const u16*)d_in[5]; const u16* kB = (const u16*)d_in[6]; const u16* vB = (const u16*)d_in[7];
  const u16* wqB = (const u16*)d_in[8]; const u16* woB = (const u16*)d_in[9];
  u16* Q = (u16*)d_ws; u16* O = Q + (size_t)2 * 512 * 4096; u16* out = (u16*)d_out;
  const float c = 0.08838834764831845f * 1.4426950408889634f;

  rd<<<256, 512, 0, stream>>>((const v4u*)d_in[1], (const v4u*)d_in[2], (const v4u*)d_in[6], (const v4u*)d_in[7], (v4u*)((char*)d_ws + (64u<<20)));
  gemm_nn<<<dim3(64, 8, 2), 256, 0, stream>>>(xA, xB, wqA, wqB, Q, Q + (size_t)512 * 4096, 1024, 4096, 4096, c);
  attn<<<dim3(32, 16, 2), 64, 0, stream>>>(Q, kA, kB, vA, vB, O);
  gemm_nn<<<dim3(16, 8, 2), 256, 0, stream>>>(O, O + (size_t)512 * 4096, woA, woB, out, out + 1024, 4096, 1024, 2048, 1.f);
}
```

```cpp
#include <hip/hip_runtime.h>

__global__ __launch_bounds__(256, 1) void gqa2_fused(const void* xA, const void* kA, const void* vA, const void* wqA, const void* woA,
                                                     const void* xB, const void* kB, const void* vB, const void* wqB, const void* woB,
                                                     void* out, void* ws) {
  __shared__ __attribute__((aligned(16))) char lds[163840];
  const unsigned ldsbase = (unsigned)(size_t)lds;
  asm volatile(
    "s_bfe_u32 s16, %1, 0x10002\n"
    "s_mul_i32 s17, s16, 40\n"
    "s_load_dwordx8 s[64:71], %0, s17\n"
    "s_load_dwordx2 s[72:73], %0, s17 offset:32\n"
    "s_load_dwordx2 s[74:75], %0, 80\n"
    "s_load_dwordx2 s[76:77], %0, 88\n"
    "v_lshrrev_b32 v4, 6, %2\n"
    "v_and_b32 v1, 63, %2\n"
    "v_readfirstlane_b32 s11, v4\n"
    "s_xor_b32 s10, %1, 0\n"
    "s_getpc_b64 s[6:7]\n"
    "Lpc_%=:\n"
    "s_lshr_b32 s17, s10, 3\n"
    "s_lshl_b32 s17, s17, 2\n"
    "s_add_u32 s17, s17, s11\n"
    "s_lshl_b32 s17, s17, 7\n"
    "v_and_b32 v4, 3, v1\n"
    "v_lshlrev_b32 v4, 14, v4\n"
    "v_add_u32 v4, s17, v4\n"
    "v_min_u32 v4, Lend_%=-Lpc_%=, v4\n"
    "s_mov_b64 exec, 15\n"
    "global_load_dword v224, v4, s[6:7]\n"
    "s_mov_b64 exec, -1\n"
    "s_and_b32 s15, s10, 3\n"
    "s_bfe_u32 s14, s10, 0x30003\n"
    "s_lshr_b32 s13, s10, 6\n"
    "s_lshr_b32 s23, s14, 2\n"
    "s_and_b32 s95, s14, 3\n"
    "s_lshl_b32 s78, s11, 12\n"
    "s_mul_i32 s79, s11, 3072\n"
    "s_lshl_b32 s17, s14, 2\n"
    "s_and_b32 s18, s13, 1\n"
    "s_mul_i32 s18, s18, 0x11111111\n"
    "s_add_u32 s6, s18, 0xeca86420\n"
    "s_mov_b32 s7, s6\n"
    "s_lshr_b64 s[6:7], s[6:7], s17\n"
    "s_xor_b32 s7, s6, 0x11111111\n"
    "s_lshl_b32 s17, s14, 3\n"
    "s_sub_u32 s18, 64, s17\n"
    "s_mov_b32 s4, 0x76543210\n"
    "s_mov_b32 s5, 0xfedcba98\n"
    "s_lshr_b64 s[26:27], s[4:5], s17\n"
    "s_lshl_b64 s[4:5], s[4:5], s18\n"
    "s_or_b64 s[26:27], s[26:27], s[4:5]\n"
    "s_cmp_ge_u32 s13, 2\n"
    "s_cselect_b64 s[26:27], s[6:7], s[26:27]\n"
    "v_lshrrev_b32 v5, 4, v1\n"
    "v_and_b32 v8, 15, v1\n"
    "v_lshlrev_b32 v4, 1, v5\n"
    "v_xor_b32 v4, v8, v4\n"
    "v_lshlrev_b32 v4, 4, v4\n"
    "v_lshlrev_b32 v6, 13, v5\n"
    "s_lshl_b32 s18, s11, 17\n"
    "v_add3_u32 v10, v4, v6, s18\n"
    "v_add_u32 v11, 31744, v10\n"
    "v_xor_b32 v7, 128, v10\n"
    "v_add_u32 v12, 63488, v7\n"
    "v_add_u32 v13, 95232, v7\n"
    "v_lshrrev_b32 v4, 3, v1\n"
    "v_and_b32 v6, 7, v1\n"
    "v_xor_b32 v6, v6, v5\n"
    "v_lshlrev_b32 v4, 11, v4\n"
    "v_lshl_add_u32 v4, v6, 4, v4\n"
    "s_mul_i32 s18, s11, 3\n"
    "s_add_u32 s19, s18, 4\n"
    "s_lshl_b32 s20, s95, 2\n"
    "s_xor_b32 s20, s20, s19\n"
    "s_lshl_b32 s20, s20, 14\n"
    "s_add_u32 s20, s20, 2048\n"
    "s_and_b32 s19, s19, 1\n"
    "s_lshl_b32 s19, s19, 6\n"
    "v_xor_b32 v6, s19, v4\n"
    "v_add_u32 v14, s20, v6\n"
    "s_add_u32 s19, s18, 5\n"
    "s_lshl_b32 s20, s95, 2\n"
    "s_xor_b32 s20, s20, s19\n"
    "s_lshl_b32 s20, s20, 14\n"
    "s_add_u32 s20, s20, 1024\n"
    "s_and_b32 s19, s19, 1\n"
    "s_lshl_b32 s19, s19, 6\n"
    "v_xor_b32 v6, s19, v4\n"
    "v_add_u32 v15, s20, v6\n"
    "s_add_u32 s19, s18, 6\n"
    "s_lshl_b32 s20, s95, 2\n"
    "s_xor_b32 s20, s20, s19\n"
    "s_lshl_b32 s20, s20, 14\n"
    "s_add_u32 s20, s20, 0\n"
    "s_and_b32 s19, s19, 1\n"
    "s_lshl_b32 s19, s19, 6\n"
    "v_xor_b32 v6, s19, v4\n"
    "v_add_u32 v16, s20, v6\n"
    "s_lshl_b32 s18, s13, 18\n"
    "s_lshl_b32 s19, s15, 11\n"
    "s_lshl_b32 s22, s14, 8\n"
    "s_add_u32 s19, s19, s22\n"
    "s_sub_u32 s21, 0x800000, s19\n"
    "s_waitcnt lgkmcnt(0)\n"
    "s_add_u32 s40, s70, s19\n"
    "s_addc_u32 s41, s71, 0\n"
    "s_and_b32 s41, s41, 0xffff\n"
    "s_mov_b32 s42, s21\n"
    "s_mov_b32 s43, 0x20000\n"
    "s_bfe_u32 s28, s26, 0x40000\n"
    "s_lshl_b32 s28, s28, 19\n"
    "s_add_u32 m0, s78, 0\n"
    "s_nop 0\n"
    "buffer_load_dwordx4 v10, s[40:43], s28 offen offset:0 lds\n"
    "buffer_load_dwordx4 v11, s[40:43], s28 offen offset:1024 lds\n"
    "buffer_load_dwordx4 v12, s[40:43], s28 offen offset:2048 lds\n"
    "buffer_load_dwordx4 v13, s[40:43], s28 offen offset:3072 lds\n"
    "s_sub_u32 s64, s64, 2048\n"
    "s_subb_u32 s65, s65, 0\n"
    "s_add_u32 s36, s64, s18\n"
    "s_addc_u32 s37, s65, 0\n"
    "s_and_b32 s37, s37, 0xffff\n"
    "s_mov_b32 s38, 264192\n"
    "s_mov_b32 s39, 0x20000\n"
    "s_cmp_ge_u32 s13, 2\n"
    "s_cbranch_scc1 Lrole0_%=\n"
    "Lfork0_%=:\n"
    "s_bfe_u32 s29, s26, 0x40000\n"
    "s_lshl_b32 s29, s29, 7\n"
    "s_add_u32 m0, s79, 16384\n"
    "s_nop 0\n"
    "buffer_load_dwordx4 v14, s[36:39], s29 offen offset:0 lds\n"
    "buffer_load_dwordx4 v15, s[36:39], s29 offen offset:1024 lds\n"
    "buffer_load_dwordx4 v16, s[36:39], s29 offen offset:2048 lds\n"
    "s_bfe_u32 s30, s26, 0x40004\n"
    "s_lshl_b32 s30, s30, 19\n"
    "s_add_u32 m0, s78, 32768\n"
    "s_nop 0\n"
    "buffer_load_dwordx4 v10, s[40:43], s30 offen offset:0 lds\n"
    "buffer_load_dwordx4 v11, s[40:43], s30 offen offset:1024 lds\n"
    "buffer_load_dwordx4 v12, s[40:43], s30 offen offset:2048 lds\n"
    "buffer_load_dwordx4 v13, s[40:43], s30 offen offset:3072 lds\n"
    "s_branch Ljoin0_%=\n"
    "Lrole0_%=:\n"
    "s_bfe_u32 s29, s26, 0x40004\n"
    "s_lshl_b32 s29, s29, 19\n"
    "s_add_u32 m0, s78, 98304\n"
    "s_nop 0\n"
    "buffer_load_dwordx4 v10, s[40:43], s29 offen offset:0 lds\n"
    "buffer_load_dwordx4 v11, s[40:43], s29 offen offset:1024 lds\n"
    "buffer_load_dwordx4 v12, s[40:43], s29 offen offset:2048 lds\n"
    "buffer_load_dwordx4 v13, s[40:43], s29 offen offset:3072 lds\n"
    "s_bfe_u32 s30, s26, 0x40008\n"
    "s_lshl_b32 s30, s30, 19\n"
    "s_add_u32 m0, s78, 114688\n"
    "s_nop 0\n"
    "buffer_load_dwordx4 v10, s[40:43], s30 offen offset:0 lds\n"
    "buffer_load_dwordx4 v11, s[40:43], s30 offen offset:1024 lds\n"
    "buffer_load_dwordx4 v12, s[40:43], s30 offen offset:2048 lds\n"
    "buffer_load_dwordx4 v13, s[40:43], s30 offen offset:3072 lds\n"
    "Ljoin0_%=:\n"
    "s_lshl_b32 s17, s16, 2\n"
    "s_add_u32 s17, s17, s13\n"
    "s_lshl_b32 s17, s17, 8\n"
    "s_load_dwordx2 s[24:25], s[76:77], s17\n"
    "s_lshl_b32 s17, s11, 2\n"
    "s_lshr_b64 s[90:91], s[26:27], s17\n"
    "s_lshr_b32 s96, s11, 1\n"
    "s_and_b32 s97, s11, 1\n"
    "s_lshl_b32 s20, s95, 16\n"
    "s_add_u32 s20, s20, 2048\n"
    "v_add_u32 v17, s20, v4\n"
    "v_xor_b32 v6, 64, v17\n"
    "v_add_u32 v18, 15360, v6\n"
    "v_add_u32 v19, 30720, v17\n"
    "v_add_u32 v20, 46080, v6\n"
    "v_lshrrev_b32 v4, 1, v8\n"
    "v_xor_b32 v4, v4, v5\n"
    "v_lshlrev_b32 v4, 4, v4\n"
    "v_lshl_add_u32 v27, v8, 7, v4\n"
    "v_xor_b32 v28, 64, v27\n"
    "v_add_u32 v29, 65536, v27\n"
    "v_add_u32 v30, 65536, v28\n"
    "v_add_u32 v31, 131072, v27\n"
    "v_add_u32 v32, 131072, v28\n"
    "v_bfe_u32 v4, v1, 2, 3\n"
    "s_lshl_b32 s19, s11, 1\n"
    "v_xor_b32 v4, s19, v4\n"
    "v_lshlrev_b32 v4, 5, v4\n"
    "v_bfe_u32 v6, v1, 2, 2\n"
    "v_lshl_add_u32 v6, v5, 3, v6\n"
    "v_lshlrev_b32 v6, 8, v6\n"
    "v_and_b32 v7, 3, v1\n"
    "v_lshlrev_b32 v7, 3, v7\n"
    "v_add3_u32 v21, v4, v6, v7\n"
    "v_xor_b32 v22, 32, v21\n"
    "v_add_u32 v23, 65536, v21\n"
    "v_add_u32 v24, 65536, v22\n"
    "v_add_u32 v25, 131072, v21\n"
    "v_add_u32 v26, 131072, v22\n"
    "s_and_b32 s12, s10, 7\n"
    "s_lshl_b32 s18, s13, 2\n"
    "s_add_u32 s17, s95, s18\n"
    "s_lshl_b32 s17, s17, 2\n"
    "s_add_u32 s17, s17, s15\n"
    "s_lshl_b32 s17, s17, 18\n"
    "s_add_u32 s44, s66, s17\n"
    "s_addc_u32 s45, s67, 0\n"
    "s_and_b32 s45, s45, 0xffff\n"
    "s_mov_b32 s46, 262144\n"
    "s_mov_b32 s47, 0x20000\n"
    "s_add_u32 s48, s68, s17\n"
    "s_addc_u32 s49, s69, 0\n"
    "s_and_b32 s49, s49, 0xffff\n"
    "s_mov_b32 s50, 262144\n"
    "s_mov_b32 s51, 0x20000\n"
    "s_lshl_b32 s17, s15, 21\n"
    "s_add_u32 s17, s17, s22\n"
    "s_sub_u32 s18, 0x200000, s22\n"
    "s_add_u32 s52, s72, s17\n"
    "s_addc_u32 s53, s73, 0\n"
    "s_and_b32 s53, s53, 0xffff\n"
    "s_mov_b32 s54, s18\n"
    "s_mov_b32 s55, 0x20000\n"
    "s_mov_b32 s56, s76\n"
    "s_and_b32 s57, s77, 0xffff\n"
    "s_mov_b32 s58, 27262976\n"
    "s_mov_b32 s59, 0x20000\n"
    "s_lshl_b32 s17, s16, 11\n"
    "s_add_u32 s17, s17, s22\n"
    "s_sub_u32 s18, 0x80000, s17\n"
    "s_lshl_b32 s19, s13, 19\n"
    "s_add_u32 s17, s17, s19\n"
    "s_add_u32 s60, s74, s17\n"
    "s_addc_u32 s61, s75, 0\n"
    "s_and_b32 s61, s61, 0xffff\n"
    "s_mov_b32 s62, s18\n"
    "s_mov_b32 s63, 0x20000\n"
    "s_lshl_b32 s8, s12, 21\n"
    "s_add_u32 s8, s8, 2097152\n"
    "s_lshl_b32 s92, s13, 18\n"
    "s_add_u32 s92, s92, s8\n"
    "s_sub_u32 s92, s92, 2048\n"
    "s_lshr_b32 s99, s10, 3\n"
    "s_lshl_b32 s17, s16, 1\n"
    "s_lshr_b32 s18, s13, 1\n"
    "s_add_u32 s17, s17, s18\n"
    "s_lshl_b32 s9, s17, 21\n"
    "s_add_u32 s9, s9, 18874368\n"
    "s_and_b32 s17, s99, 15\n"
    "s_lshl_b32 s17, s17, 2\n"
    "s_add_u32 s74, s17, s15\n"
    "s_mov_b64 s[68:69], s[44:45]\n"
    "s_lshl_b32 s70, s10, 8\n"
    "s_lshl_b32 s19, s13, 19\n"
    "s_add_u32 s19, s19, s70\n"
    "s_lshl_b32 s20, s15, 17\n"
    "s_cmp_eq_u32 s11, 1\n"
    "s_cselect_b64 s[68:69], s[48:49], s[68:69]\n"
    "s_cselect_b32 s70, s70, s70\n"
    "s_cmp_eq_u32 s11, 2\n"
    "s_cselect_b64 s[68:69], s[52:53], s[68:69]\n"
    "s_cselect_b32 s70, s19, s70\n"
    "s_cmp_eq_u32 s11, 3\n"
    "s_cselect_b64 s[68:69], s[60:61], s[68:69]\n"
    "s_cselect_b32 s70, s20, s70\n"
    "s_waitcnt vmcnt(12)\n"
    "s_mov_b64 exec, 0\n"
    "s_cmp_eq_u32 s11, 0\n"
    "s_cbranch_scc1 Lw0_%=\n"
    "s_cmp_eq_u32 s11, 1\n"
    "s_cbranch_scc1 Lw3_%=\n"
    "s_cmp_eq_u32 s11, 2\n"
    "s_cbranch_scc1 Lw6_%=\n"
    "s_branch Lw9_%=\n"
    "Ltramp_%=:\n"
    "s_setpc_b64 s[6:7]\n"
    "Lret0_%=:\n"
    "s_mov_b64 exec, -1\n"
    "s_cmp_ge_u32 s13, 2\n"
    "s_cbranch_scc1 Lrole1_%=\n"
    "Lfork1_%=:\n"
    "s_bfe_u32 s28, s26, 0x40004\n"
    "s_lshl_b32 s28, s28, 7\n"
    "s_add_u32 m0, s79, 49152\n"
    "s_nop 0\n"
    "buffer_load_dwordx4 v14, s[36:39], s28 offen offset:0 lds\n"
    "buffer_load_dwordx4 v15, s[36:39], s28 offen offset:1024 lds\n"
    "buffer_load_dwordx4 v16, s[36:39], s28 offen offset:2048 lds\n"
    "s_branch Ljoin1_%=\n"
    "Lrole1_%=:\n"
    "s_bfe_u32 s28, s26, 0x4000c\n"
    "s_lshl_b32 s28, s28, 19\n"
    "s_add_u32 m0, s78, 131072\n"
    "s_nop 0\n"
    "buffer_load_dwordx4 v10, s[40:43], s28 offen offset:0 lds\n"
    "buffer_load_dwordx4 v11, s[40:43], s28 offen offset:1024 lds\n"
    "buffer_load_dwordx4 v12, s[40:43], s28 offen offset:2048 lds\n"
    "buffer_load_dwordx4 v13, s[40:43], s28 offen offset:3072 lds\n"
    "Ljoin1_%=:\n"
    "s_lshl_b32 s19, s11, 9\n"
    "s_lshl_b32 s17, s99, 11\n"
    "s_add_u32 s17, s17, s19\n"
    "s_add_u32 s17, s17, s8\n"
    "s_add_u32 s17, s17, 1835008\n"
    "s_lshl_b32 s18, s74, 11\n"
    "s_add_u32 s18, s18, s19\n"
    "s_add_u32 s18, s18, s9\n"
    "s_add_u32 s18, s18, 1572352\n"
    "v_mov_b32 v2, s17\n"
    "v_mov_b32 v3, s18\n"
    "v_cmp_gt_u32 vcc, 4, v1\n"
    "s_nop 1\n"
    "v_cndmask_b32 v2, v3, v2, vcc\n"
    "v_lshl_add_u32 v2, v1, 7, v2\n"
    "s_lshl_b32 s17, s99, 2\n"
    "s_add_u32 s17, s17, s11\n"
    "s_lshl_b32 s17, s17, 7\n"
    "s_add_u32 s17, s17, s8\n"
    "s_add_u32 s17, s17, 1900544\n"
    "v_writelane_b32 v2, s17, 0\n"
    "s_mov_b64 exec, 0xef\n"
    "buffer_load_dword v3, v2, s[56:59], 0 offen sc1\n"
    "s_mov_b64 exec, -1\n"
    "s_waitcnt lgkmcnt(0)\n"
    "s_add_u32 s24, s24, 1\n"
    "s_addc_u32 s25, s25, 0\n"
    "s_cmp_ge_u32 s13, 2\n"
    "s_cbranch_scc1 Lrole2_%=\n"
    "Lfork2_%=:\n"
    "s_waitcnt vmcnt(8)\n"
    "s_barrier\n"
    "s_load_dword s71, s[68:69], s70\n"
    "ds_read_b64_tr_b16 a[0:1], v21 offset:0\n"
    "ds_read_b64_tr_b16 a[2:3], v21 offset:1024\n"
    "s_bfe_u32 s28, s26, 0x40008\n"
    "ds_read_b64_tr_b16 a[4:5], v21 offset:8192\n"
    "ds_read_b64_tr_b16 a[6:7], v21 offset:9216\n"
    "s_lshl_b32 s28, s28, 19\n"
    "ds_read_b128 v[128:131], v27 offset:16384\n"
    "s_add_u32 m0, s78, 65536\n"
    "ds_read_b128 v[132:135], v28 offset:16384\n"
    "ds_read_b64_tr_b16 a[8:9], v22 offset:0\n"
    "buffer_load_dwordx4 v10, s[40:43], s28 offen offset:0 lds\n"
    "ds_read_b64_tr_b16 a[10:11], v22 offset:1024\n"
    "buffer_load_dwordx4 v11, s[40:43], s28 offen offset:1024 lds\n"
    "ds_read_b64_tr_b16 a[12:13], v22 offset:8192\n"
    "ds_read_b64_tr_b16 a[14:15], v22 offset:9216\n"
    "buffer_load_dwordx4 v12, s[40:43], s28 offen offset:2048 lds\n"
    "ds_read_b128 v[136:139], v27 offset:18432\n"
    "buffer_load_dwordx4 v13, s[40:43], s28 offen offset:3072 lds\n"
    "ds_read_b128 v[140:143], v28 offset:18432\n"
    "ds_read_b128 v[144:147], v27 offset:20480\n"
    "s_bfe_u32 s29, s26, 0x40008\n"
    "ds_read_b128 v[148:151], v28 offset:20480\n"
    "s_lshl_b32 s29, s29, 7\n"
    "ds_read_b128 v[152:155], v27 offset:22528\n"
    "ds_read_b128 v[156:159], v28 offset:22528\n"
    "s_add_u32 m0, s79, 81920\n"
    "ds_read_b128 v[160:163], v27 offset:24576\n"
    "buffer_load_dwordx4 v14, s[36:39], s29 offen offset:0 lds\n"
    "ds_read_b128 v[164:167], v28 offset:24576\n"
    "ds_read_b128 v[168:171], v27 offset:26624\n"
    "buffer_load_dwordx4 v15, s[36:39], s29 offen offset:1024 lds\n"
    "ds_read_b128 v[172:175], v28 offset:26624\n"
    "buffer_load_dwordx4 v16, s[36:39], s29 offen offset:2048 lds\n"
    "s_waitcnt vmcnt(8) lgkmcnt(0)\n"
    "s_barrier\n"
    "v_mfma_f32_16x16x32_bf16 v[80:83], a[0:3], v[128:131], 0\n"
    "ds_read_b64_tr_b16 a[16:17], v21 offset:32768\n"
    "ds_read_b64_tr_b16 a[18:19], v21 offset:33792\n"
    "s_bfe_u32 s30, s26, 0x4000c\n"
    "v_mfma_f32_16x16x32_bf16 v[80:83], a[4:7], v[132:135], v[80:83]\n"
    "ds_read_b64_tr_b16 a[20:21], v21 offset:40960\n"
    "s_lshl_b32 s82, s23, 13\n"
    "v_mfma_f32_16x16x32_bf16 v[84:87], a[12:15], v[132:135], 0\n"
    "ds_read_b64_tr_b16 a[22:23], v21 offset:41984\n"
    "s_lshl_b32 s30, s30, 19\n"
    "v_mfma_f32_16x16x32_bf16 v[84:87], a[8:11], v[128:131], v[84:87]\n"
    "ds_read_b128 v[176:179], v27 offset:49152\n"
    "s_add_u32 m0, s78, 16384\n"
    "v_mfma_f32_16x16x32_bf16 v[92:95], a[8:11], v[136:139], 0\n"
    "ds_read_b128 v[180:183], v28 offset:49152\n"
    "ds_read_b64_tr_b16 a[24:25], v22 offset:32768\n"
    "v_mfma_f32_16x16x32_bf16 v[92:95], a[12:15], v[140:143], v[92:95]\n"
    "buffer_load_dwordx4 v10, s[40:43], s30 offen offset:0 lds\n"
    "s_lshl_b32 s84, s23, 7\n"
    "v_mfma_f32_16x16x32_bf16 v[88:91], a[4:7], v[140:143], 0\n"
    "ds_read_b64_tr_b16 a[26:27], v22 offset:33792\n"
    "s_xor_b32 s83, s82, 8192\n"
    "v_mfma_f32_16x16x32_bf16 v[88:91], a[0:3], v[136:139], v[88:91]\n"
    "buffer_load_dwordx4 v11, s[40:43], s30 offen offset:1024 lds\n"
    "s_xor_b32 s85, s84, 128\n"
    "v_mfma_f32_16x16x32_bf16 v[96:99], a[0:3], v[144:147], 0\n"
    "ds_read_b64_tr_b16 a[28:29], v22 offset:40960\n"
    "ds_read_b64_tr_b16 a[30:31], v22 offset:41984\n"
    "v_mfma_f32_16x16x32_bf16 v[96:99], a[4:7], v[148:151], v[96:99]\n"
    "buffer_load_dwordx4 v12, s[40:43], s30 offen offset:2048 lds\n"
    "s_mov_b32 s80, 0x3e0293ee\n"
    "v_mfma_f32_16x16x32_bf16 v[100:103], a[12:15], v[148:151], 0\n"
    "ds_read_b128 v[184:187], v27 offset:51200\n"
    "s_mov_b32 s81, 0x3e0293ee\n"
    "v_mfma_f32_16x16x32_bf16 v[100:103], a[8:11], v[144:147], v[100:103]\n"
    "buffer_load_dwordx4 v13, s[40:43], s30 offen offset:3072 lds\n"
    "s_mov_b32 s94, 0\n"
    "v_mfma_f32_16x16x32_bf16 v[108:111], a[8:11], v[152:155], 0\n"
    "ds_read_b128 v[188:191], v28 offset:51200\n"
    "s_lshl_b32 s19, s96, 17\n"
    "v_mfma_f32_16x16x32_bf16 v[108:111], a[12:15], v[156:159], v[108:111]\n"
    "ds_read_b128 v[192:195], v27 offset:53248\n"
    "s_bfe_u32 s31, s26, 0x4000c\n"
    "ds_read_b128 v[196:199], v28 offset:53248\n"
    "s_lshl_b32 s31, s31, 7\n"
    "v_mfma_f32_16x16x32_bf16 v[104:107], a[4:7], v[156:159], 0\n"
    "ds_read_b128 v[200:203], v27 offset:55296\n"
    "s_lshl_b32 s20, s97, 12\n"
    "v_mfma_f32_16x16x32_bf16 v[104:107], a[0:3], v[152:155], v[104:107]\n"
    "ds_read_b128 v[204:207], v28 offset:55296\n"
    "s_add_u32 m0, s79, 0\n"
    "v_mfma_f32_16x16x32_bf16 v[112:115], a[0:3], v[160:163], 0\n"
    "ds_read_b128 v[208:211], v27 offset:57344\n"
    "s_add_u32 s20, s20, s19\n"
    "v_mfma_f32_16x16x32_bf16 v[112:115], a[4:7], v[164:167], v[112:115]\n"
    "buffer_load_dwordx4 v14, s[36:39], s31 offen offset:0 lds\n"
    "ds_read_b128 v[212:215], v28 offset:57344\n"
    "v_mfma_f32_16x16x32_bf16 v[116:119], a[12:15], v[164:167], 0\n"
    "ds_read_b128 v[216:219], v27 offset:59392\n"
    "v_xor_b32 v4, v8, v5\n"
    "v_mfma_f32_16x16x32_bf16 v[116:119], a[8:11], v[160:163], v[116:119]\n"
    "buffer_load_dwordx4 v15, s[36:39], s31 offen offset:1024 lds\n"
    "v_lshlrev_b32 v4, 4, v4\n"
    "v_mfma_f32_16x16x32_bf16 v[124:127], a[8:11], v[168:171], 0\n"
    "ds_read_b128 v[220:223], v28 offset:59392\n"
    "v_lshlrev_b32 v6, 8, v5\n"
    "v_mfma_f32_16x16x32_bf16 v[124:127], a[12:15], v[172:175], v[124:127]\n"
    "buffer_load_dwordx4 v16, s[36:39], s31 offen offset:2048 lds\n"
    "v_add3_u32 v33, v4, v6, s20\n"
    "v_mfma_f32_16x16x32_bf16 v[120:123], a[4:7], v[172:175], 0\n"
    "v_mfma_f32_16x16x32_bf16 v[120:123], a[0:3], v[168:171], v[120:123]\n"
    "s_waitcnt vmcnt(7) lgkmcnt(0)\n"
    "s_barrier\n"
    "v_mfma_f32_16x16x32_bf16 v[80:83], a[16:19], v[176:179], v[80:83]\n"
    "ds_read_b64_tr_b16 a[32:33], v23 offset:0\n"
    "ds_read_b64_tr_b16 a[34:35], v23 offset:1024\n"
    "s_bfe_u32 s28, s26, 0x40010\n"
    "v_mfma_f32_16x16x32_bf16 v[80:83], a[20:23], v[180:183], v[80:83]\n"
    "ds_read_b64_tr_b16 a[36:37], v23 offset:8192\n"
    "v_xor_b32 v34, 64, v33\n"
    "v_mfma_f32_16x16x32_bf16 v[84:87], a[28:31], v[180:183], v[84:87]\n"
    "ds_read_b64_tr_b16 a[38:39], v23 offset:9216\n"
    "s_lshl_b32 s28, s28, 19\n"
    "v_mfma_f32_16x16x32_bf16 v[84:87], a[24:27], v[176:179], v[84:87]\n"
    "ds_read_b128 v[128:131], v29 offset:16384\n"
    "s_add_u32 m0, s78, 49152\n"
    "v_mfma_f32_16x16x32_bf16 v[92:95], a[24:27], v[184:187], v[92:95]\n"
    "ds_read_b128 v[132:135], v30 offset:16384\n"
    "ds_read_b64_tr_b16 a[40:41], v24 offset:0\n"
    "v_mfma_f32_16x16x32_bf16 v[92:95], a[28:31], v[188:191], v[92:95]\n"
    "buffer_load_dwordx4 v10, s[40:43], s28 offen offset:0 lds\n"
    "v_xor_b32 v35, 128, v33\n"
    "v_mfma_f32_16x16x32_bf16 v[88:91], a[20:23], v[188:191], v[88:91]\n"
    "ds_read_b64_tr_b16 a[42:43], v24 offset:1024\n"
    "v_xor_b32 v36, 192, v33\n"
    "v_mfma_f32_16x16x32_bf16 v[88:91], a[16:19], v[184:187], v[88:91]\n"
    "buffer_load_dwordx4 v11, s[40:43], s28 offen offset:1024 lds\n"
    "s_lshl_b32 s20, s97, 13\n"
    "v_mfma_f32_16x16x32_bf16 v[96:99], a[16:19], v[192:195], v[96:99]\n"
    "ds_read_b64_tr_b16 a[44:45], v24 offset:8192\n"
    "ds_read_b64_tr_b16 a[46:47], v24 offset:9216\n"
    "v_mfma_f32_16x16x32_bf16 v[96:99], a[20:23], v[196:199], v[96:99]\n"
    "buffer_load_dwordx4 v12, s[40:43], s28 offen offset:2048 lds\n"
    "s_xor_b32 s20, s20, s82\n"
    "v_mfma_f32_16x16x32_bf16 v[100:103], a[28:31], v[196:199], v[100:103]\n"
    "ds_read_b128 v[136:139], v29 offset:18432\n"
    "s_add_u32 s20, s20, s19\n"
    "v_mfma_f32_16x16x32_bf16 v[100:103], a[24:27], v[192:195], v[100:103]\n"
    "buffer_load_dwordx4 v13, s[40:43], s28 offen offset:3072 lds\n"
    "v_and_b32 v4, 7, v1\n"
    "v_mfma_f32_16x16x32_bf16 v[108:111], a[24:27], v[200:203], v[108:111]\n"
    "ds_read_b128 v[140:143], v30 offset:18432\n"
    "v_lshlrev_b32 v6, 1, v5\n"
    "v_mfma_f32_16x16x32_bf16 v[108:111], a[28:31], v[204:207], v[108:111]\n"
    "ds_read_b128 v[144:147], v29 offset:20480\n"
    "s_bfe_u32 s29, s26, 0x40010\n"
    "ds_read_b128 v[148:151], v30 offset:20480\n"
    "s_lshl_b32 s29, s29, 7\n"
    "v_mfma_f32_16x16x32_bf16 v[104:107], a[20:23], v[204:207], v[104:107]\n"
    "ds_read_b128 v[152:155], v29 offset:22528\n"
    "v_xor_b32 v4, v4, v6\n"
    "v_mfma_f32_16x16x32_bf16 v[104:107], a[16:19], v[200:203], v[104:107]\n"
    "ds_read_b128 v[156:159], v30 offset:22528\n"
    "s_add_u32 m0, s79, 32768\n"
    "v_mfma_f32_16x16x32_bf16 v[112:115], a[16:19], v[208:211], v[112:115]\n"
    "ds_read_b128 v[160:163], v29 offset:24576\n"
    "v_lshlrev_b32 v4, 4, v4\n"
    "v_mfma_f32_16x16x32_bf16 v[112:115], a[20:23], v[212:215], v[112:115]\n"
    "buffer_load_dwordx4 v14, s[36:39], s29 offen offset:0 lds\n"
    "ds_read_b128 v[164:167], v30 offset:24576\n"
    "v_mfma_f32_16x16x32_bf16 v[116:119], a[28:31], v[212:215], v[116:119]\n"
    "ds_read_b128 v[168:171], v29 offset:26624\n"
    "v_lshrrev_b32 v6, 3, v1\n"
    "v_mfma_f32_16x16x32_bf16 v[116:119], a[24:27], v[208:211], v[116:119]\n"
    "buffer_load_dwordx4 v15, s[36:39], s29 offen offset:1024 lds\n"
    "v_lshlrev_b32 v6, 8, v6\n"
    "v_mfma_f32_16x16x32_bf16 v[124:127], a[24:27], v[216:219], v[124:127]\n"
    "ds_read_b128 v[172:175], v30 offset:26624\n"
    "v_add3_u32 v37, v4, v6, s20\n"
    "v_mfma_f32_16x16x32_bf16 v[124:127], a[28:31], v[220:223], v[124:127]\n"
    "buffer_load_dwordx4 v16, s[36:39], s29 offen offset:2048 lds\n"
    "v_add_u32 v38, 1024, v37\n"
    "v_mfma_f32_16x16x32_bf16 v[120:123], a[20:23], v[220:223], v[120:123]\n"
    "v_mfma_f32_16x16x32_bf16 v[120:123], a[16:19], v[216:219], v[120:123]\n"
    "s_waitcnt vmcnt(7) lgkmcnt(0)\n"
    "s_barrier\n"
    "v_mfma_f32_16x16x32_bf16 v[80:83], a[32:35], v[128:131], v[80:83]\n"
    "ds_read_b64_tr_b16 a[48:49], v21 offset:16384\n"
    "ds_read_b64_tr_b16 a[50:51], v21 offset:17408\n"
    "s_bfe_u32 s30, s26, 0x40014\n"
    "v_mfma_f32_16x16x32_bf16 v[80:83], a[36:39], v[132:135], v[80:83]\n"
    "ds_read_b64_tr_b16 a[52:53], v21 offset:24576\n"
    "v_add_u32 v39, 2048, v37\n"
    "v_mfma_f32_16x16x32_bf16 v[84:87], a[44:47], v[132:135], v[84:87]\n"
    "ds_read_b64_tr_b16 a[54:55], v21 offset:25600\n"
    "s_lshl_b32 s30, s30, 19\n"
    "v_mfma_f32_16x16x32_bf16 v[84:87], a[40:43], v[128:131], v[84:87]\n"
    "ds_read_b128 v[176:179], v27 offset:0\n"
    "s_add_u32 m0, s78, 81920\n"
    "v_mfma_f32_16x16x32_bf16 v[92:95], a[40:43], v[136:139], v[92:95]\n"
    "ds_read_b128 v[180:183], v28 offset:0\n"
    "ds_read_b64_tr_b16 a[56:57], v22 offset:16384\n"
    "v_mfma_f32_16x16x32_bf16 v[92:95], a[44:47], v[140:143], v[92:95]\n"
    "buffer_load_dwordx4 v10, s[40:43], s30 offen offset:0 lds\n"
    "v_add_u32 v40, 3072, v37\n"
    "v_mfma_f32_16x16x32_bf16 v[88:91], a[36:39], v[140:143], v[88:91]\n"
    "ds_read_b64_tr_b16 a[58:59], v22 offset:17408\n"
    "v_lshrrev_b32 v4, 5, v1\n"
    "v_mfma_f32_16x16x32_bf16 v[88:91], a[32:35], v[136:139], v[88:91]\n"
    "buffer_load_dwordx4 v11, s[40:43], s30 offen offset:1024 lds\n"
    "v_bfe_u32 v6, v1, 4, 1\n"
    "v_mfma_f32_16x16x32_bf16 v[96:99], a[32:35], v[144:147], v[96:99]\n"
    "ds_read_b64_tr_b16 a[60:61], v22 offset:24576\n"
    "ds_read_b64_tr_b16 a[62:63], v22 offset:25600\n"
    "v_mfma_f32_16x16x32_bf16 v[96:99], a[36:39], v[148:151], v[96:99]\n"
    "buffer_load_dwordx4 v12, s[40:43], s30 offen offset:2048 lds\n"
    "v_lshl_or_b32 v9, v6, 1, v4\n"
    "v_mfma_f32_16x16x32_bf16 v[100:103], a[44:47], v[148:151], v[100:103]\n"
    "ds_read_b128 v[184:187], v27 offset:2048\n"
    "v_lshlrev_b32 v41, 4, v1\n"
    "v_mfma_f32_16x16x32_bf16 v[100:103], a[40:43], v[144:147], v[100:103]\n"
    "buffer_load_dwordx4 v13, s[40:43], s30 offen offset:3072 lds\n"
    "s_mul_i32 s17, s11, 6144\n"
    "v_mfma_f32_16x16x32_bf16 v[108:111], a[40:43], v[152:155], v[108:111]\n"
    "ds_read_b128 v[188:191], v28 offset:2048\n"
    "s_add_u32 s17, s17, s8\n"
    "v_mfma_f32_16x16x32_bf16 v[108:111], a[44:47], v[156:159], v[108:111]\n"
    "ds_read_b128 v[192:195], v27 offset:4096\n"
    "s_bfe_u32 s31, s26, 0x40014\n"
    "ds_read_b128 v[196:199], v28 offset:4096\n"
    "s_lshl_b32 s31, s31, 7\n"
    "v_mfma_f32_16x16x32_bf16 v[104:107], a[36:39], v[156:159], v[104:107]\n"
    "ds_read_b128 v[200:203], v27 offset:6144\n"
    "s_xor_b32 s64, s99, 0\n"
    "v_mfma_f32_16x16x32_bf16 v[104:107], a[32:35], v[152:155], v[104:107]\n"
    "ds_read_b128 v[204:207], v28 offset:6144\n"
    "s_add_u32 m0, s79, 65536\n"
    "v_mfma_f32_16x16x32_bf16 v[112:115], a[32:35], v[160:163], v[112:115]\n"
    "ds_read_b128 v[208:211], v27 offset:8192\n"
    "s_mul_i32 s64, s64, 24576\n"
    "v_mfma_f32_16x16x32_bf16 v[112:115], a[36:39], v[164:167], v[112:115]\n"
    "buffer_load_dwordx4 v14, s[36:39], s31 offen offset:0 lds\n"
    "ds_read_b128 v[212:215], v28 offset:8192\n"
    "v_mfma_f32_16x16x32_bf16 v[116:119], a[44:47], v[164:167], v[116:119]\n"
    "ds_read_b128 v[216:219], v27 offset:10240\n"
    "s_add_u32 s64, s64, s17\n"
    "v_mfma_f32_16x16x32_bf16 v[116:119], a[40:43], v[160:163], v[116:119]\n"
    "buffer_load_dwordx4 v15, s[36:39], s31 offen offset:1024 lds\n"
    "s_add_u32 s64, s64, 1048576\n"
    "v_mfma_f32_16x16x32_bf16 v[124:127], a[40:43], v[168:171], v[124:127]\n"
    "ds_read_b128 v[220:223], v28 offset:10240\n"
    "s_xor_b32 s65, s99, 1\n"
    "v_mfma_f32_16x16x32_bf16 v[124:127], a[44:47], v[172:175], v[124:127]\n"
    "buffer_load_dwordx4 v16, s[36:39], s31 offen offset:2048 lds\n"
    "s_mul_i32 s65, s65, 24576\n"
    "v_mfma_f32_16x16x32_bf16 v[120:123], a[36:39], v[172:175], v[120:123]\n"
    "v_mfma_f32_16x16x32_bf16 v[120:123], a[32:35], v[168:171], v[120:123]\n"
    "s_waitcnt vmcnt(7) lgkmcnt(0)\n"
    "s_barrier\n"
    "v_mfma_f32_16x16x32_bf16 v[80:83], a[48:51], v[176:179], v[80:83]\n"
    "ds_read_b64_tr_b16 a[64:65], v21 offset:49152\n"
    "ds_read_b64_tr_b16 a[66:67], v21 offset:50176\n"
    "s_bfe_u32 s28, s26, 0x40018\n"
    "v_mfma_f32_16x16x32_bf16 v[80:83], a[52:55], v[180:183], v[80:83]\n"
    "ds_read_b64_tr_b16 a[68:69], v21 offset:57344\n"
    "s_add_u32 s65, s65, s17\n"
    "v_mfma_f32_16x16x32_bf16 v[84:87], a[60:63], v[180:183], v[84:87]\n"
    "ds_read_b64_tr_b16 a[70:71], v21 offset:58368\n"
    "s_lshl_b32 s28, s28, 19\n"
    "v_mfma_f32_16x16x32_bf16 v[84:87], a[56:59], v[176:179], v[84:87]\n"
    "ds_read_b128 v[128:131], v27 offset:32768\n"
    "s_add_u32 m0, s78, 0\n"
    "v_mfma_f32_16x16x32_bf16 v[92:95], a[56:59], v[184:187], v[92:95]\n"
    "ds_read_b128 v[132:135], v28 offset:32768\n"
    "ds_read_b64_tr_b16 a[72:73], v22 offset:49152\n"
    "v_mfma_f32_16x16x32_bf16 v[92:95], a[60:63], v[188:191], v[92:95]\n"
    "buffer_load_dwordx4 v10, s[40:43], s28 offen offset:0 lds\n"
    "s_add_u32 s65, s65, 1048576\n"
    "v_mfma_f32_16x16x32_bf16 v[88:91], a[52:55], v[188:191], v[88:91]\n"
    "ds_read_b64_tr_b16 a[74:75], v22 offset:50176\n"
    "s_xor_b32 s66, s99, 2\n"
    "v_mfma_f32_16x16x32_bf16 v[88:91], a[48:51], v[184:187], v[88:91]\n"
    "buffer_load_dwordx4 v11, s[40:43], s28 offen offset:1024 lds\n"
    "s_mul_i32 s66, s66, 24576\n"
    "v_mfma_f32_16x16x32_bf16 v[96:99], a[48:51], v[192:195], v[96:99]\n"
    "ds_read_b64_tr_b16 a[76:77], v22 offset:57344\n"
    "ds_read_b64_tr_b16 a[78:79], v22 offset:58368\n"
    "v_mfma_f32_16x16x32_bf16 v[96:99], a[52:55], v[196:199], v[96:99]\n"
    "buffer_load_dwordx4 v12, s[40:43], s28 offen offset:2048 lds\n"
    "s_add_u32 s66, s66, s17\n"
    "v_mfma_f32_16x16x32_bf16 v[100:103], a[60:63], v[196:199], v[100:103]\n"
    "ds_read_b128 v[136:139], v27 offset:34816\n"
    "s_add_u32 s66, s66, 1050624\n"
    "v_mfma_f32_16x16x32_bf16 v[100:103], a[56:59], v[192:195], v[100:103]\n"
    "buffer_load_dwordx4 v13, s[40:43], s28 offen offset:3072 lds\n"
    "s_xor_b32 s67, s99, 3\n"
    "v_mfma_f32_16x16x32_bf16 v[108:111], a[56:59], v[200:203], v[108:111]\n"
    "ds_read_b128 v[140:143], v28 offset:34816\n"
    "s_mul_i32 s67, s67, 24576\n"
    "v_mfma_f32_16x16x32_bf16 v[108:111], a[60:63], v[204:207], v[108:111]\n"
    "ds_read_b128 v[144:147], v27 offset:36864\n"
    "s_bfe_u32 s29, s26, 0x40018\n"
    "ds_read_b128 v[148:151], v28 offset:36864\n"
    "s_lshl_b32 s29, s29, 7\n"
    "v_mfma_f32_16x16x32_bf16 v[104:107], a[52:55], v[204:207], v[104:107]\n"
    "ds_read_b128 v[152:155], v27 offset:38912\n"
    "s_add_u32 s67, s67, s17\n"
    "v_mfma_f32_16x16x32_bf16 v[104:107], a[48:51], v[200:203], v[104:107]\n"
    "ds_read_b128 v[156:159], v28 offset:38912\n"
    "s_add_u32 m0, s79, 16384\n"
    "v_mfma_f32_16x16x32_bf16 v[112:115], a[48:51], v[208:211], v[112:115]\n"
    "ds_read_b128 v[160:163], v27 offset:40960\n"
    "s_add_u32 s67, s67, 1052672\n"
    "v_mfma_f32_16x16x32_bf16 v[112:115], a[52:55], v[212:215], v[112:115]\n"
    "buffer_load_dwordx4 v14, s[36:39], s29 offen offset:0 lds\n"
    "ds_read_b128 v[164:167], v28 offset:40960\n"
    "v_mfma_f32_16x16x32_bf16 v[116:119], a[60:63], v[212:215], v[116:119]\n"
    "ds_read_b128 v[168:171], v27 offset:43008\n"
    "v_lshrrev_b32 v4, 3, v1\n"
    "v_mfma_f32_16x16x32_bf16 v[116:119], a[56:59], v[208:211], v[116:119]\n"
    "buffer_load_dwordx4 v15, s[36:39], s29 offen offset:1024 lds\n"
    "v_add_u32 v4, 1, v4\n"
    "v_mfma_f32_16x16x32_bf16 v[124:127], a[56:59], v[216:219], v[124:127]\n"
    "ds_read_b128 v[172:175], v28 offset:43008\n"
    "v_xor_b32 v6, s99, v4\n"
    "v_mfma_f32_16x16x32_bf16 v[124:127], a[60:63], v[220:223], v[124:127]\n"
    "buffer_load_dwordx4 v16, s[36:39], s29 offen offset:2048 lds\n"
    "v_lshlrev_b32 v6, 11, v6\n"
    "v_mfma_f32_16x16x32_bf16 v[120:123], a[52:55], v[220:223], v[120:123]\n"
    "v_mfma_f32_16x16x32_bf16 v[120:123], a[48:51], v[216:219], v[120:123]\n"
    "Lw0_%=:\n"
    "s_cbranch_execz Lw1_%=\n"
    "s_waitcnt vmcnt(7) lgkmcnt(0)\n"
    "s_barrier\n"
    "v_mfma_f32_16x16x32_bf16 v[80:83], a[64:67], v[128:131], v[80:83]\n"
    "ds_read_b64_tr_b16 a[80:81], v23 offset:16384\n"
    "ds_read_b64_tr_b16 a[82:83], v23 offset:17408\n"
    "s_bfe_u32 s30, s26, 0x4001c\n"
    "v_mfma_f32_16x16x32_bf16 v[80:83], a[68:71], v[132:135], v[80:83]\n"
    "ds_read_b64_tr_b16 a[84:85], v23 offset:24576\n"
    "v_lshlrev_b32 v4, 7, v4\n"
    "v_mfma_f32_16x16x32_bf16 v[84:87], a[76:79], v[132:135], v[84:87]\n"
    "ds_read_b64_tr_b16 a[86:87], v23 offset:25600\n"
    "s_lshl_b32 s30, s30, 19\n"
    "v_mfma_f32_16x16x32_bf16 v[84:87], a[72:75], v[128:131], v[84:87]\n"
    "ds_read_b128 v[176:179], v29 offset:0\n"
    "s_add_u32 m0, s78, 32768\n"
    "v_mfma_f32_16x16x32_bf16 v[92:95], a[72:75], v[136:139], v[92:95]\n"
    "ds_read_b128 v[180:183], v30 offset:0\n"
    "ds_read_b64_tr_b16 a[88:89], v24 offset:16384\n"
    "v_mfma_f32_16x16x32_bf16 v[92:95], a[76:79], v[140:143], v[92:95]\n"
    "buffer_load_dwordx4 v10, s[40:43], s30 offen offset:0 lds\n"
    "s_lshl_b32 s17, s11, 9\n"
    "v_mfma_f32_16x16x32_bf16 v[88:91], a[68:71], v[140:143], v[88:91]\n"
    "ds_read_b64_tr_b16 a[90:91], v24 offset:17408\n"
    "s_add_u32 s17, s17, 1835008\n"
    "v_mfma_f32_16x16x32_bf16 v[88:91], a[64:67], v[136:139], v[88:91]\n"
    "buffer_load_dwordx4 v11, s[40:43], s30 offen offset:1024 lds\n"
    "s_add_u32 s17, s17, s8\n"
    "v_mfma_f32_16x16x32_bf16 v[96:99], a[64:67], v[144:147], v[96:99]\n"
    "ds_read_b64_tr_b16 a[92:93], v24 offset:24576\n"
    "ds_read_b64_tr_b16 a[94:95], v24 offset:25600\n"
    "v_mfma_f32_16x16x32_bf16 v[96:99], a[68:71], v[148:151], v[96:99]\n"
    "buffer_load_dwordx4 v12, s[40:43], s30 offen offset:2048 lds\n"
    "v_add3_u32 v42, v6, v4, s17\n"
    "v_mfma_f32_16x16x32_bf16 v[100:103], a[76:79], v[148:151], v[100:103]\n"
    "ds_read_b128 v[184:187], v29 offset:2048\n"
    "v_and_b32 v6, 7, v1\n"
    "v_mfma_f32_16x16x32_bf16 v[100:103], a[72:75], v[144:147], v[100:103]\n"
    "buffer_load_dwordx4 v13, s[40:43], s30 offen offset:3072 lds\n"
    "v_lshl_add_u32 v42, v6, 4, v42\n"
    "v_mfma_f32_16x16x32_bf16 v[108:111], a[72:75], v[152:155], v[108:111]\n"
    "ds_read_b128 v[188:191], v30 offset:2048\n"
    "v_sub_u32 v6, 23, v1\n"
    "v_mfma_f32_16x16x32_bf16 v[108:111], a[76:79], v[156:159], v[108:111]\n"
    "ds_read_b128 v[192:195], v29 offset:4096\n"
    "s_bfe_u32 s31, s26, 0x4001c\n"
    "ds_read_b128 v[196:199], v30 offset:4096\n"
    "s_lshl_b32 s31, s31, 7\n"
    "v_mfma_f32_16x16x32_bf16 v[104:107], a[68:71], v[156:159], v[104:107]\n"
    "ds_read_b128 v[200:203], v29 offset:6144\n"
    "v_ashrrev_i32 v6, 31, v6\n"
    "v_mfma_f32_16x16x32_bf16 v[104:107], a[64:67], v[152:155], v[104:107]\n"
    "ds_read_b128 v[204:207], v30 offset:6144\n"
    "s_add_u32 m0, s79, 49152\n"
    "v_mfma_f32_16x16x32_bf16 v[112:115], a[64:67], v[160:163], v[112:115]\n"
    "ds_read_b128 v[208:211], v29 offset:8192\n"
    "v_lshl_or_b32 v42, v6, 8, v42\n"
    "v_mfma_f32_16x16x32_bf16 v[112:115], a[68:71], v[164:167], v[112:115]\n"
    "buffer_load_dwordx4 v14, s[36:39], s31 offen offset:0 lds\n"
    "ds_read_b128 v[212:215], v30 offset:8192\n"
    "v_mfma_f32_16x16x32_bf16 v[116:119], a[76:79], v[164:167], v[116:119]\n"
    "ds_read_b128 v[216:219], v29 offset:10240\n"
    "s_lshl_b32 s18, s99, 11\n"
    "v_mfma_f32_16x16x32_bf16 v[116:119], a[72:75], v[160:163], v[116:119]\n"
    "buffer_load_dwordx4 v15, s[36:39], s31 offen offset:1024 lds\n"
    "s_add_u32 s17, s17, s18\n"
    "v_mfma_f32_16x16x32_bf16 v[124:127], a[72:75], v[168:171], v[124:127]\n"
    "ds_read_b128 v[220:223], v30 offset:10240\n"
    "s_add_u32 s17, s17, 128\n"
    "v_mfma_f32_16x16x32_bf16 v[124:127], a[76:79], v[172:175], v[124:127]\n"
    "buffer_load_dwordx4 v16, s[36:39], s31 offen offset:2048 lds\n"
    "v_lshlrev_b32 v4, 7, v1\n"
    "v_mfma_f32_16x16x32_bf16 v[120:123], a[68:71], v[172:175], v[120:123]\n"
    "v_mfma_f32_16x16x32_bf16 v[120:123], a[64:67], v[168:171], v[120:123]\n"
    "s_waitcnt vmcnt(7) lgkmcnt(0)\n"
    "s_barrier\n"
    "v_mfma_f32_16x16x32_bf16 v[80:83], a[80:83], v[176:179], v[80:83]\n"
    "ds_read_b64_tr_b16 a[96:97], v21 offset:0\n"
    "ds_read_b64_tr_b16 a[98:99], v21 offset:1024\n"
    "s_bfe_u32 s28, s27, 0x40000\n"
    "v_mfma_f32_16x16x32_bf16 v[80:83], a[84:87], v[180:183], v[80:83]\n"
    "ds_read_b64_tr_b16 a[100:101], v21 offset:8192\n"
    "v_add_u32 v43, s17, v4\n"
    "v_mfma_f32_16x16x32_bf16 v[84:87], a[92:95], v[180:183], v[84:87]\n"
    "ds_read_b64_tr_b16 a[102:103], v21 offset:9216\n"
    "s_lshl_b32 s28, s28, 19\n"
    "v_mfma_f32_16x16x32_bf16 v[84:87], a[88:91], v[176:179], v[84:87]\n"
    "ds_read_b128 v[128:131], v27 offset:16384\n"
    "s_add_u32 m0, s78, 65536\n"
    "v_mfma_f32_16x16x32_bf16 v[92:95], a[88:91], v[184:187], v[92:95]\n"
    "ds_read_b128 v[132:135], v28 offset:16384\n"
    "ds_read_b64_tr_b16 a[104:105], v22 offset:0\n"
    "v_mfma_f32_16x16x32_bf16 v[92:95], a[92:95], v[188:191], v[92:95]\n"
    "buffer_load_dwordx4 v10, s[40:43], s28 offen offset:0 lds\n"
    "v_and_b32 v4, 3, v1\n"
    "v_mfma_f32_16x16x32_bf16 v[88:91], a[84:87], v[188:191], v[88:91]\n"
    "ds_read_b64_tr_b16 a[106:107], v22 offset:1024\n"
    "v_cmp_eq_u32 vcc, 0, v4\n"
    "v_mfma_f32_16x16x32_bf16 v[88:91], a[80:83], v[184:187], v[88:91]\n"
    "buffer_load_dwordx4 v11, s[40:43], s28 offen offset:1024 lds\n"
    "v_mov_b32 v6, 0x3f803f80\n"
    "v_mfma_f32_16x16x32_bf16 v[96:99], a[80:83], v[192:195], v[96:99]\n"
    "ds_read_b64_tr_b16 a[108:109], v22 offset:8192\n"
    "ds_read_b64_tr_b16 a[110:111], v22 offset:9216\n"
    "v_mfma_f32_16x16x32_bf16 v[96:99], a[84:87], v[196:199], v[96:99]\n"
    "buffer_load_dwordx4 v12, s[40:43], s28 offen offset:2048 lds\n"
    "s_nop 0\n"
    "v_mfma_f32_16x16x32_bf16 v[100:103], a[92:95], v[196:199], v[100:103]\n"
    "ds_read_b128 v[136:139], v27 offset:18432\n"
    "v_cndmask_b32 v60, 0, v6, vcc\n"
    "v_mfma_f32_16x16x32_bf16 v[100:103], a[88:91], v[192:195], v[100:103]\n"
    "buffer_load_dwordx4 v13, s[40:43], s28 offen offset:3072 lds\n"
    "v_cndmask_b32 v61, 0, v6, vcc\n"
    "v_mfma_f32_16x16x32_bf16 v[108:111], a[88:91], v[200:203], v[108:111]\n"
    "ds_read_b128 v[140:143], v28 offset:18432\n"
    "v_cndmask_b32 v62, 0, v6, vcc\n"
    "v_mfma_f32_16x16x32_bf16 v[108:111], a[92:95], v[204:207], v[108:111]\n"
    "ds_read_b128 v[144:147], v27 offset:20480\n"
    "s_bfe_u32 s29, s27, 0x40000\n"
    "ds_read_b128 v[148:151], v28 offset:20480\n"
    "s_lshl_b32 s29, s29, 7\n"
    "v_mfma_f32_16x16x32_bf16 v[104:107], a[84:87], v[204:207], v[104:107]\n"
    "ds_read_b128 v[152:155], v27 offset:22528\n"
    "v_cndmask_b32 v63, 0, v6, vcc\n"
    "v_mfma_f32_16x16x32_bf16 v[104:107], a[80:83], v[200:203], v[104:107]\n"
    "ds_read_b128 v[156:159], v28 offset:22528\n"
    "s_add_u32 m0, s79, 81920\n"
    "v_mfma_f32_16x16x32_bf16 v[112:115], a[80:83], v[208:211], v[112:115]\n"
    "ds_read_b128 v[160:163], v27 offset:24576\n"
    "v_mov_b32 v56, s24\n"
    "v_mfma_f32_16x16x32_bf16 v[112:115], a[84:87], v[212:215], v[112:115]\n"
    "buffer_load_dwordx4 v14, s[36:39], s29 offen offset:0 lds\n"
    "ds_read_b128 v[164:167], v28 offset:24576\n"
    "v_mfma_f32_16x16x32_bf16 v[116:119], a[92:95], v[212:215], v[116:119]\n"
    "ds_read_b128 v[168:171], v27 offset:26624\n"
    "v_mov_b32 v57, s25\n"
    "v_mfma_f32_16x16x32_bf16 v[116:119], a[88:91], v[208:211], v[116:119]\n"
    "buffer_load_dwordx4 v15, s[36:39], s29 offen offset:1024 lds\n"
    "v_mov_b32 v58, s24\n"
    "v_mfma_f32_16x16x32_bf16 v[124:127], a[88:91], v[216:219], v[124:127]\n"
    "ds_read_b128 v[172:175], v28 offset:26624\n"
    "v_mov_b32 v59, s25\n"
    "v_mfma_f32_16x16x32_bf16 v[124:127], a[92:95], v[220:223], v[124:127]\n"
    "buffer_load_dwordx4 v16, s[36:39], s29 offen offset:2048 lds\n"
    "v_mfma_f32_16x16x32_bf16 v[120:123], a[84:87], v[220:223], v[120:123]\n"
    "v_mfma_f32_16x16x32_bf16 v[120:123], a[80:83], v[216:219], v[120:123]\n"
    "s_waitcnt vmcnt(7) lgkmcnt(0)\n"
    "s_barrier\n"
    "v_mfma_f32_16x16x32_bf16 v[80:83], a[96:99], v[128:131], v[80:83]\n"
    "ds_read_b64_tr_b16 a[112:113], v21 offset:32768\n"
    "ds_read_b64_tr_b16 a[114:115], v21 offset:33792\n"
    "s_bfe_u32 s30, s27, 0x40004\n"
    "v_mfma_f32_16x16x32_bf16 v[80:83], a[100:103], v[132:135], v[80:83]\n"
    "ds_read_b64_tr_b16 a[116:117], v21 offset:40960\n"
    "v_mfma_f32_16x16x32_bf16 v[84:87], a[108:111], v[132:135], v[84:87]\n"
    "ds_read_b64_tr_b16 a[118:119], v21 offset:41984\n"
    "s_lshl_b32 s30, s30, 19\n"
    "v_mfma_f32_16x16x32_bf16 v[84:87], a[104:107], v[128:131], v[84:87]\n"
    "ds_read_b128 v[176:179], v27 offset:49152\n"
    "s_add_u32 m0, s78, 16384\n"
    "v_mfma_f32_16x16x32_bf16 v[92:95], a[104:107], v[136:139], v[92:95]\n"
    "ds_read_b128 v[180:183], v28 offset:49152\n"
    "ds_read_b64_tr_b16 a[120:121], v22 offset:32768\n"
    "v_mfma_f32_16x16x32_bf16 v[92:95], a[108:111], v[140:143], v[92:95]\n"
    "buffer_load_dwordx4 v10, s[40:43], s30 offen offset:0 lds\n"
    "v_mfma_f32_16x16x32_bf16 v[88:91], a[100:103], v[140:143], v[88:91]\n"
    "ds_read_b64_tr_b16 a[122:123], v22 offset:33792\n"
    "v_mfma_f32_16x16x32_bf16 v[88:91], a[96:99], v[136:139], v[88:91]\n"
    "buffer_load_dwordx4 v11, s[40:43], s30 offen offset:1024 lds\n"
    "v_mfma_f32_16x16x32_bf16 v[96:99], a[96:99], v[144:147], v[96:99]\n"
    "ds_read_b64_tr_b16 a[124:125], v22 offset:40960\n"
    "ds_read_b64_tr_b16 a[126:127], v22 offset:41984\n"
    "v_mfma_f32_16x16x32_bf16 v[96:99], a[100:103], v[148:151], v[96:99]\n"
    "buffer_load_dwordx4 v12, s[40:43], s30 offen offset:2048 lds\n"
    "v_mfma_f32_16x16x32_bf16 v[100:103], a[108:111], v[148:151], v[100:103]\n"
    "ds_read_b128 v[184:187], v27 offset:51200\n"
    "v_mfma_f32_16x16x32_bf16 v[100:103], a[104:107], v[144:147], v[100:103]\n"
    "buffer_load_dwordx4 v13, s[40:43], s30 offen offset:3072 lds\n"
    "v_mfma_f32_16x16x32_bf16 v[108:111], a[104:107], v[152:155], v[108:111]\n"
    "ds_read_b128 v[188:191], v28 offset:51200\n"
    "v_mfma_f32_16x16x32_bf16 v[108:111], a[108:111], v[156:159], v[108:111]\n"
    "ds_read_b128 v[192:195], v27 offset:53248\n"
    "s_bfe_u32 s31, s27, 0x40004\n"
    "ds_read_b128 v[196:199], v28 offset:53248\n"
    "s_lshl_b32 s31, s31, 7\n"
    "v_mfma_f32_16x16x32_bf16 v[104:107], a[100:103], v[156:159], v[104:107]\n"
    "ds_read_b128 v[200:203], v27 offset:55296\n"
    "v_mfma_f32_16x16x32_bf16 v[104:107], a[96:99], v[152:155], v[104:107]\n"
    "ds_read_b128 v[204:207], v28 offset:55296\n"
    "s_add_u32 m0, s79, 0\n"
    "v_mfma_f32_16x16x32_bf16 v[112:115], a[96:99], v[160:163], v[112:115]\n"
    "ds_read_b128 v[208:211], v27 offset:57344\n"
    "v_mfma_f32_16x16x32_bf16 v[112:115], a[100:103], v[164:167], v[112:115]\n"
    "buffer_load_dwordx4 v14, s[36:39], s31 offen offset:0 lds\n"
    "ds_read_b128 v[212:215], v28 offset:57344\n"
    "v_mfma_f32_16x16x32_bf16 v[116:119], a[108:111], v[164:167], v[116:119]\n"
    "ds_read_b128 v[216:219], v27 offset:59392\n"
    "v_mfma_f32_16x16x32_bf16 v[116:119], a[104:107], v[160:163], v[116:119]\n"
    "buffer_load_dwordx4 v15, s[36:39], s31 offen offset:1024 lds\n"
    "v_mfma_f32_16x16x32_bf16 v[124:127], a[104:107], v[168:171], v[124:127]\n"
    "ds_read_b128 v[220:223], v28 offset:59392\n"
    "v_mfma_f32_16x16x32_bf16 v[124:127], a[108:111], v[172:175], v[124:127]\n"
    "buffer_load_dwordx4 v16, s[36:39], s31 offen offset:2048 lds\n"
    "v_mfma_f32_16x16x32_bf16 v[120:123], a[100:103], v[172:175], v[120:123]\n"
    "v_mfma_f32_16x16x32_bf16 v[120:123], a[96:99], v[168:171], v[120:123]\n"
    "s_waitcnt vmcnt(7) lgkmcnt(0)\n"
    "s_barrier\n"
    "v_mfma_f32_16x16x32_bf16 v[80:83], a[112:115], v[176:179], v[80:83]\n"
    "ds_read_b64_tr_b16 a[128:129], v23 offset:0\n"
    "ds_read_b64_tr_b16 a[130:131], v23 offset:1024\n"
    "s_bfe_u32 s28, s27, 0x40008\n"
    "v_mfma_f32_16x16x32_bf16 v[80:83], a[116:119], v[180:183], v[80:83]\n"
    "ds_read_b64_tr_b16 a[132:133], v23 offset:8192\n"
    "v_mfma_f32_16x16x32_bf16 v[84:87], a[124:127], v[180:183], v[84:87]\n"
    "ds_read_b64_tr_b16 a[134:135], v23 offset:9216\n"
    "s_lshl_b32 s28, s28, 19\n"
    "v_mfma_f32_16x16x32_bf16 v[84:87], a[120:123], v[176:179], v[84:87]\n"
    "ds_read_b128 v[128:131], v29 offset:16384\n"
    "s_add_u32 m0, s78, 49152\n"
    "v_mfma_f32_16x16x32_bf16 v[92:95], a[120:123], v[184:187], v[92:95]\n"
    "ds_read_b128 v[132:135], v30 offset:16384\n"
    "ds_read_b64_tr_b16 a[136:137], v24 offset:0\n"
    "v_mfma_f32_16x16x32_bf16 v[92:95], a[124:127], v[188:191], v[92:95]\n"
    "buffer_load_dwordx4 v10, s[40:43], s28 offen offset:0 lds\n"
    "v_mfma_f32_16x16x32_bf16 v[88:91], a[116:119], v[188:191], v[88:91]\n"
    "ds_read_b64_tr_b16 a[138:139], v24 offset:1024\n"
    "v_mfma_f32_16x16x32_bf16 v[88:91], a[112:115], v[184:187], v[88:91]\n"
    "buffer_load_dwordx4 v11, s[40:43], s28 offen offset:1024 lds\n"
    "v_mfma_f32_16x16x32_bf16 v[96:99], a[112:115], v[192:195], v[96:99]\n"
    "ds_read_b64_tr_b16 a[140:141], v24 offset:8192\n"
    "ds_read_b64_tr_b16 a[142:143], v24 offset:9216\n"
    "v_mfma_f32_16x16x32_bf16 v[96:99], a[116:119], v[196:199], v[96:99]\n"
    "buffer_load_dwordx4 v12, s[40:43], s28 offen offset:2048 lds\n"
    "v_mfma_f32_16x16x32_bf16 v[100:103], a[124:127], v[196:199], v[100:103]\n"
    "ds_read_b128 v[136:139], v29 offset:18432\n"
    "v_mfma_f32_16x16x32_bf16 v[100:103], a[120:123], v[192:195], v[100:103]\n"
    "buffer_load_dwordx4 v13, s[40:43], s28 offen offset:3072 lds\n"
    "v_mfma_f32_16x16x32_bf16 v[108:111], a[120:123], v[200:203], v[108:111]\n"
    "ds_read_b128 v[140:143], v30 offset:18432\n"
    "v_mfma_f32_16x16x32_bf16 v[108:111], a[124:127], v[204:207], v[108:111]\n"
    "ds_read_b128 v[144:147], v29 offset:20480\n"
    "s_bfe_u32 s29, s27, 0x40008\n"
    "ds_read_b128 v[148:151], v30 offset:20480\n"
    "s_lshl_b32 s29, s29, 7\n"
    "v_mfma_f32_16x16x32_bf16 v[104:107], a[116:119], v[204:207], v[104:107]\n"
    "ds_read_b128 v[152:155], v29 offset:22528\n"
    "v_mfma_f32_16x16x32_bf16 v[104:107], a[112:115], v[200:203], v[104:107]\n"
    "ds_read_b128 v[156:159], v30 offset:22528\n"
    "s_add_u32 m0, s79, 32768\n"
    "v_mfma_f32_16x16x32_bf16 v[112:115], a[112:115], v[208:211], v[112:115]\n"
    "ds_read_b128 v[160:163], v29 offset:24576\n"
    "v_mfma_f32_16x16x32_bf16 v[112:115], a[116:119], v[212:215], v[112:115]\n"
    "buffer_load_dwordx4 v14, s[36:39], s29 offen offset:0 lds\n"
    "ds_read_b128 v[164:167], v30 offset:24576\n"
    "v_mfma_f32_16x16x32_bf16 v[116:119], a[124:127], v[212:215], v[116:119]\n"
    "ds_read_b128 v[168:171], v29 offset:26624\n"
    "v_mfma_f32_16x16x32_bf16 v[116:119], a[120:123], v[208:211], v[116:119]\n"
    "buffer_load_dwordx4 v15, s[36:39], s29 offen offset:1024 lds\n"
    "v_mfma_f32_16x16x32_bf16 v[124:127], a[120:123], v[216:219], v[124:127]\n"
    "ds_read_b128 v[172:175], v30 offset:26624\n"
    "v_mfma_f32_16x16x32_bf16 v[124:127], a[124:127], v[220:223], v[124:127]\n"
    "buffer_load_dwordx4 v16, s[36:39], s29 offen offset:2048 lds\n"
    "v_mfma_f32_16x16x32_bf16 v[120:123], a[116:119], v[220:223], v[120:123]\n"
    "v_mfma_f32_16x16x32_bf16 v[120:123], a[112:115], v[216:219], v[120:123]\n"
    "s_waitcnt vmcnt(7) lgkmcnt(0)\n"
    "s_barrier\n"
    "v_mfma_f32_16x16x32_bf16 v[80:83], a[128:131], v[128:131], v[80:83]\n"
    "ds_read_b64_tr_b16 a[144:145], v21 offset:16384\n"
    "ds_read_b64_tr_b16 a[146:147], v21 offset:17408\n"
    "s_bfe_u32 s30, s27, 0x4000c\n"
    "v_mfma_f32_16x16x32_bf16 v[80:83], a[132:135], v[132:135], v[80:83]\n"
    "ds_read_b64_tr_b16 a[148:149], v21 offset:24576\n"
    "v_mfma_f32_16x16x32_bf16 v[84:87], a[140:143], v[132:135], v[84:87]\n"
    "ds_read_b64_tr_b16 a[150:151], v21 offset:25600\n"
    "s_lshl_b32 s30, s30, 19\n"
    "v_mfma_f32_16x16x32_bf16 v[84:87], a[136:139], v[128:131], v[84:87]\n"
    "ds_read_b128 v[176:179], v27 offset:0\n"
    "s_add_u32 m0, s78, 81920\n"
    "v_mfma_f32_16x16x32_bf16 v[92:95], a[136:139], v[136:139], v[92:95]\n"
    "ds_read_b128 v[180:183], v28 offset:0\n"
    "ds_read_b64_tr_b16 a[152:153], v22 offset:16384\n"
    "v_mfma_f32_16x16x32_bf16 v[92:95], a[140:143], v[140:143], v[92:95]\n"
    "buffer_load_dwordx4 v10, s[40:43], s30 offen offset:0 lds\n"
    "v_mfma_f32_16x16x32_bf16 v[88:91], a[132:135], v[140:143], v[88:91]\n"
    "ds_read_b64_tr_b16 a[154:155], v22 offset:17408\n"
    "v_mfma_f32_16x16x32_bf16 v[88:91], a[128:131], v[136:139], v[88:91]\n"
    "buffer_load_dwordx4 v11, s[40:43], s30 offen offset:1024 lds\n"
    "v_mfma_f32_16x16x32_bf16 v[96:99], a[128:131], v[144:147], v[96:99]\n"
    "ds_read_b64_tr_b16 a[156:157], v22 offset:24576\n"
    "ds_read_b64_tr_b16 a[158:159], v22 offset:25600\n"
    "v_mfma_f32_16x16x32_bf16 v[96:99], a[132:135], v[148:151], v[96:99]\n"
    "buffer_load_dwordx4 v12, s[40:43], s30 offen offset:2048 lds\n"
    "v_mfma_f32_16x16x32_bf16 v[100:103], a[140:143], v[148:151], v[100:103]\n"
    "ds_read_b128 v[184:187], v27 offset:2048\n"
    "v_mfma_f32_16x16x32_bf16 v[100:103], a[136:139], v[144:147], v[100:103]\n"
    "buffer_load_dwordx4 v13, s[40:43], s30 offen offset:3072 lds\n"
    "v_mfma_f32_16x16x32_bf16 v[108:111], a[136:139], v[152:155], v[108:111]\n"
    "ds_read_b128 v[188:191], v28 offset:2048\n"
    "v_mfma_f32_16x16x32_bf16 v[108:111], a[140:143], v[156:159], v[108:111]\n"
    "ds_read_b128 v[192:195], v27 offset:4096\n"
    "s_bfe_u32 s31, s27, 0x4000c\n"
    "ds_read_b128 v[196:199], v28 offset:4096\n"
    "s_lshl_b32 s31, s31, 7\n"
    "v_mfma_f32_16x16x32_bf16 v[104:107], a[132:135], v[156:159], v[104:107]\n"
    "ds_read_b128 v[200:203], v27 offset:6144\n"
    "v_mfma_f32_16x16x32_bf16 v[104:107], a[128:131], v[152:155], v[104:107]\n"
    "ds_read_b128 v[204:207], v28 offset:6144\n"
    "s_add_u32 m0, s79, 65536\n"
    "v_mfma_f32_16x16x32_bf16 v[112:115], a[128:131], v[160:163], v[112:115]\n"
    "ds_read_b128 v[208:211], v27 offset:8192\n"
    "v_mfma_f32_16x16x32_bf16 v[112:115], a[132:135], v[164:167], v[112:115]\n"
    "buffer_load_dwordx4 v14, s[36:39], s31 offen offset:0 lds\n"
    "ds_read_b128 v[212:215], v28 offset:8192\n"
    "v_mfma_f32_16x16x32_bf16 v[116:119], a[140:143], v[164:167], v[116:119]\n"
    "ds_read_b128 v[216:219], v27 offset:10240\n"
    "v_mfma_f32_16x16x32_bf16 v[116:119], a[136:139], v[160:163], v[116:119]\n"
    "buffer_load_dwordx4 v15, s[36:39], s31 offen offset:1024 lds\n"
    "v_mfma_f32_16x16x32_bf16 v[124:127], a[136:139], v[168:171], v[124:127]\n"
    "ds_read_b128 v[220:223], v28 offset:10240\n"
    "v_mfma_f32_16x16x32_bf16 v[124:127], a[140:143], v[172:175], v[124:127]\n"
    "buffer_load_dwordx4 v16, s[36:39], s31 offen offset:2048 lds\n"
    "v_mfma_f32_16x16x32_bf16 v[120:123], a[132:135], v[172:175], v[120:123]\n"
    "v_mfma_f32_16x16x32_bf16 v[120:123], a[128:131], v[168:171], v[120:123]\n"
    "s_waitcnt vmcnt(7) lgkmcnt(0)\n"
    "s_barrier\n"
    "v_mfma_f32_16x16x32_bf16 v[80:83], a[144:147], v[176:179], v[80:83]\n"
    "ds_read_b64_tr_b16 a[160:161], v21 offset:49152\n"
    "ds_read_b64_tr_b16 a[162:163], v21 offset:50176\n"
    "s_bfe_u32 s28, s27, 0x40010\n"
    "v_mfma_f32_16x16x32_bf16 v[80:83], a[148:151], v[180:183], v[80:83]\n"
    "ds_read_b64_tr_b16 a[164:165], v21 offset:57344\n"
    "v_mfma_f32_16x16x32_bf16 v[84:87], a[156:159], v[180:183], v[84:87]\n"
    "ds_read_b64_tr_b16 a[166:167], v21 offset:58368\n"
    "s_lshl_b32 s28, s28, 19\n"
    "v_mfma_f32_16x16x32_bf16 v[84:87], a[152:155], v[176:179], v[84:87]\n"
    "ds_read_b128 v[128:131], v27 offset:32768\n"
    "s_add_u32 m0, s78, 0\n"
    "v_mfma_f32_16x16x32_bf16 v[92:95], a[152:155], v[184:187], v[92:95]\n"
    "ds_read_b128 v[132:135], v28 offset:32768\n"
    "ds_read_b64_tr_b16 a[168:169], v22 offset:49152\n"
    "v_mfma_f32_16x16x32_bf16 v[92:95], a[156:159], v[188:191], v[92:95]\n"
    "buffer_load_dwordx4 v10, s[40:43], s28 offen offset:0 lds\n"
    "v_mfma_f32_16x16x32_bf16 v[88:91], a[148:151], v[188:191], v[88:91]\n"
    "ds_read_b64_tr_b16 a[170:171], v22 offset:50176\n"
    "v_mfma_f32_16x16x32_bf16 v[88:91], a[144:147], v[184:187], v[88:91]\n"
    "buffer_load_dwordx4 v11, s[40:43], s28 offen offset:1024 lds\n"
    "v_mfma_f32_16x16x32_bf16 v[96:99], a[144:147], v[192:195], v[96:99]\n"
    "ds_read_b64_tr_b16 a[172:173], v22 offset:57344\n"
    "ds_read_b64_tr_b16 a[174:175], v22 offset:58368\n"
    "v_mfma_f32_16x16x32_bf16 v[96:99], a[148:151], v[196:199], v[96:99]\n"
    "buffer_load_dwordx4 v12, s[40:43], s28 offen offset:2048 lds\n"
    "v_mfma_f32_16x16x32_bf16 v[100:103], a[156:159], v[196:199], v[100:103]\n"
    "ds_read_b128 v[136:139], v27 offset:34816\n"
    "v_mfma_f32_16x16x32_bf16 v[100:103], a[152:155], v[192:195], v[100:103]\n"
    "buffer_load_dwordx4 v13, s[40:43], s28 offen offset:3072 lds\n"
    "v_mfma_f32_16x16x32_bf16 v[108:111], a[152:155], v[200:203], v[108:111]\n"
    "ds_read_b128 v[140:143], v28 offset:34816\n"
    "v_mfma_f32_16x16x32_bf16 v[108:111], a[156:159], v[204:207], v[108:111]\n"
    "ds_read_b128 v[144:147], v27 offset:36864\n"
    "s_bfe_u32 s29, s27, 0x40010\n"
    "ds_read_b128 v[148:151], v28 offset:36864\n"
    "s_lshl_b32 s29, s29, 7\n"
    "v_mfma_f32_16x16x32_bf16 v[104:107], a[148:151], v[204:207], v[104:107]\n"
    "ds_read_b128 v[152:155], v27 offset:38912\n"
    "v_mfma_f32_16x16x32_bf16 v[104:107], a[144:147], v[200:203], v[104:107]\n"
    "ds_read_b128 v[156:159], v28 offset:38912\n"
    "s_add_u32 m0, s79, 16384\n"
    "v_mfma_f32_16x16x32_bf16 v[112:115], a[144:147], v[208:211], v[112:115]\n"
    "ds_read_b128 v[160:163], v27 offset:40960\n"
    "v_mfma_f32_16x16x32_bf16 v[112:115], a[148:151], v[212:215], v[112:115]\n"
    "buffer_load_dwordx4 v14, s[36:39], s29 offen offset:0 lds\n"
    "ds_read_b128 v[164:167], v28 offset:40960\n"
    "v_mfma_f32_16x16x32_bf16 v[116:119], a[156:159], v[212:215], v[116:119]\n"
    "ds_read_b128 v[168:171], v27 offset:43008\n"
    "v_mfma_f32_16x16x32_bf16 v[116:119], a[152:155], v[208:211], v[116:119]\n"
    "buffer_load_dwordx4 v15, s[36:39], s29 offen offset:1024 lds\n"
    "v_mfma_f32_16x16x32_bf16 v[124:127], a[152:155], v[216:219], v[124:127]\n"
    "ds_read_b128 v[172:175], v28 offset:43008\n"
    "v_mfma_f32_16x16x32_bf16 v[124:127], a[156:159], v[220:223], v[124:127]\n"
    "buffer_load_dwordx4 v16, s[36:39], s29 offen offset:2048 lds\n"
    "v_mfma_f32_16x16x32_bf16 v[120:123], a[148:151], v[220:223], v[120:123]\n"
    "v_mfma_f32_16x16x32_bf16 v[120:123], a[144:147], v[216:219], v[120:123]\n"
    "s_waitcnt vmcnt(7) lgkmcnt(0)\n"
    "s_barrier\n"
    "v_mfma_f32_16x16x32_bf16 v[80:83], a[160:163], v[128:131], v[80:83]\n"
    "ds_read_b64_tr_b16 a[176:177], v23 offset:16384\n"
    "ds_read_b64_tr_b16 a[178:179], v23 offset:17408\n"
    "s_bfe_u32 s30, s27, 0x40014\n"
    "v_mfma_f32_16x16x32_bf16 v[80:83], a[164:167], v[132:135], v[80:83]\n"
    "ds_read_b64_tr_b16 a[180:181], v23 offset:24576\n"
    "v_mfma_f32_16x16x32_bf16 v[84:87], a[172:175], v[132:135], v[84:87]\n"
    "ds_read_b64_tr_b16 a[182:183], v23 offset:25600\n"
    "s_lshl_b32 s30, s30, 19\n"
    "v_mfma_f32_16x16x32_bf16 v[84:87], a[168:171], v[128:131], v[84:87]\n"
    "ds_read_b128 v[176:179], v29 offset:0\n"
    "s_add_u32 m0, s78, 32768\n"
    "v_mfma_f32_16x16x32_bf16 v[92:95], a[168:171], v[136:139], v[92:95]\n"
    "ds_read_b128 v[180:183], v30 offset:0\n"
    "ds_read_b64_tr_b16 a[184:185], v24 offset:16384\n"
    "v_mfma_f32_16x16x32_bf16 v[92:95], a[172:175], v[140:143], v[92:95]\n"
    "buffer_load_dwordx4 v10, s[40:43], s30 offen offset:0 lds\n"
    "v_mfma_f32_16x16x32_bf16 v[88:91], a[164:167], v[140:143], v[88:91]\n"
    "ds_read_b64_tr_b16 a[186:187], v24 offset:17408\n"
    "v_mfma_f32_16x16x32_bf16 v[88:91], a[160:163], v[136:139], v[88:91]\n"
    "buffer_load_dwordx4 v11, s[40:43], s30 offen offset:1024 lds\n"
    "v_mfma_f32_16x16x32_bf16 v[96:99], a[160:163], v[144:147], v[96:99]\n"
    "ds_read_b64_tr_b16 a[188:189], v24 offset:24576\n"
    "ds_read_b64_tr_b16 a[190:191], v24 offset:25600\n"
    "v_mfma_f32_16x16x32_bf16 v[96:99], a[164:167], v[148:151], v[96:99]\n"
    "buffer_load_dwordx4 v12, s[40:43], s30 offen offset:2048 lds\n"
    "v_mfma_f32_16x16x32_bf16 v[100:103], a[172:175], v[148:151], v[100:103]\n"
    "ds_read_b128 v[184:187], v29 offset:2048\n"
    "v_mfma_f32_16x16x32_bf16 v[100:103], a[168:171], v[144:147], v[100:103]\n"
    "buffer_load_dwordx4 v13, s[40:43], s30 offen offset:3072 lds\n"
    "v_mfma_f32_16x16x32_bf16 v[108:111], a[168:171], v[152:155], v[108:111]\n"
    "ds_read_b128 v[188:191], v30 offset:2048\n"
    "v_mfma_f32_16x16x32_bf16 v[108:111], a[172:175], v[156:159], v[108:111]\n"
    "ds_read_b128 v[192:195], v29 offset:4096\n"
    "s_bfe_u32 s31, s27, 0x40014\n"
    "ds_read_b128 v[196:199], v30 offset:4096\n"
    "s_lshl_b32 s31, s31, 7\n"
    "v_mfma_f32_16x16x32_bf16 v[104:107], a[164:167], v[156:159], v[104:107]\n"
    "ds_read_b128 v[200:203], v29 offset:6144\n"
    "v_mfma_f32_16x16x32_bf16 v[104:107], a[160:163], v[152:155], v[104:107]\n"
    "ds_read_b128 v[204:207], v30 offset:6144\n"
    "s_add_u32 m0, s79, 49152\n"
    "v_mfma_f32_16x16x32_bf16 v[112:115], a[160:163], v[160:163], v[112:115]\n"
    "ds_read_b128 v[208:211], v29 offset:8192\n"
    "v_mfma_f32_16x16x32_bf16 v[112:115], a[164:167], v[164:167], v[112:115]\n"
    "buffer_load_dwordx4 v14, s[36:39], s31 offen offset:0 lds\n"
    "ds_read_b128 v[212:215], v30 offset:8192\n"
    "v_mfma_f32_16x16x32_bf16 v[116:119], a[172:175], v[164:167], v[116:119]\n"
    "ds_read_b128 v[216:219], v29 offset:10240\n"
    "v_mfma_f32_16x16x32_bf16 v[116:119], a[168:171], v[160:163], v[116:119]\n"
    "buffer_load_dwordx4 v15, s[36:39], s31 offen offset:1024 lds\n"
    "v_mfma_f32_16x16x32_bf16 v[124:127], a[168:171], v[168:171], v[124:127]\n"
    "ds_read_b128 v[220:223], v30 offset:10240\n"
    "v_mfma_f32_16x16x32_bf16 v[124:127], a[172:175], v[172:175], v[124:127]\n"
    "buffer_load_dwordx4 v16, s[36:39], s31 offen offset:2048 lds\n"
    "v_mfma_f32_16x16x32_bf16 v[120:123], a[164:167], v[172:175], v[120:123]\n"
    "v_mfma_f32_16x16x32_bf16 v[120:123], a[160:163], v[168:171], v[120:123]\n"
    "s_waitcnt vmcnt(7) lgkmcnt(0)\n"
    "s_barrier\n"
    "v_mfma_f32_16x16x32_bf16 v[80:83], a[176:179], v[176:179], v[80:83]\n"
    "ds_read_b64_tr_b16 a[192:193], v21 offset:0\n"
    "ds_read_b64_tr_b16 a[194:195], v21 offset:1024\n"
    "s_bfe_u32 s28, s27, 0x40018\n"
    "v_mfma_f32_16x16x32_bf16 v[80:83], a[180:183], v[180:183], v[80:83]\n"
    "ds_read_b64_tr_b16 a[196:197], v21 offset:8192\n"
    "v_mfma_f32_16x16x32_bf16 v[84:87], a[188:191], v[180:183], v[84:87]\n"
    "ds_read_b64_tr_b16 a[198:199], v21 offset:9216\n"
    "s_lshl_b32 s28, s28, 19\n"
    "v_mfma_f32_16x16x32_bf16 v[84:87], a[184:187], v[176:179], v[84:87]\n"
    "ds_read_b128 v[128:131], v27 offset:16384\n"
    "s_add_u32 m0, s78, 65536\n"
    "v_mfma_f32_16x16x32_bf16 v[92:95], a[184:187], v[184:187], v[92:95]\n"
    "ds_read_b128 v[132:135], v28 offset:16384\n"
    "ds_read_b64_tr_b16 a[200:201], v22 offset:0\n"
    "v_mfma_f32_16x16x32_bf16 v[92:95], a[188:191], v[188:191], v[92:95]\n"
    "buffer_load_dwordx4 v10, s[40:43], s28 offen offset:0 lds\n"
    "v_mfma_f32_16x16x32_bf16 v[88:91], a[180:183], v[188:191], v[88:91]\n"
    "ds_read_b64_tr_b16 a[202:203], v22 offset:1024\n"
    "v_mfma_f32_16x16x32_bf16 v[88:91], a[176:179], v[184:187], v[88:91]\n"
    "buffer_load_dwordx4 v11, s[40:43], s28 offen offset:1024 lds\n"
    "v_mfma_f32_16x16x32_bf16 v[96:99], a[176:179], v[192:195], v[96:99]\n"
    "ds_read_b64_tr_b16 a[204:205], v22 offset:8192\n"
    "ds_read_b64_tr_b16 a[206:207], v22 offset:9216\n"
    "v_mfma_f32_16x16x32_bf16 v[96:99], a[180:183], v[196:199], v[96:99]\n"
    "buffer_load_dwordx4 v12, s[40:43], s28 offen offset:2048 lds\n"
    "v_mfma_f32_16x16x32_bf16 v[100:103], a[188:191], v[196:199], v[100:103]\n"
    "ds_read_b128 v[136:139], v27 offset:18432\n"
    "v_mfma_f32_16x16x32_bf16 v[100:103], a[184:187], v[192:195], v[100:103]\n"
    "buffer_load_dwordx4 v13, s[40:43], s28 offen offset:3072 lds\n"
    "v_mfma_f32_16x16x32_bf16 v[108:111], a[184:187], v[200:203], v[108:111]\n"
    "ds_read_b128 v[140:143], v28 offset:18432\n"
    "v_mfma_f32_16x16x32_bf16 v[108:111], a[188:191], v[204:207], v[108:111]\n"
    "ds_read_b128 v[144:147], v27 offset:20480\n"
    "s_bfe_u32 s29, s27, 0x40018\n"
    "ds_read_b128 v[148:151], v28 offset:20480\n"
    "s_lshl_b32 s29, s29, 7\n"
    "v_mfma_f32_16x16x32_bf16 v[104:107], a[180:183], v[204:207], v[104:107]\n"
    "ds_read_b128 v[152:155], v27 offset:22528\n"
    "v_mfma_f32_16x16x32_bf16 v[104:107], a[176:179], v[200:203], v[104:107]\n"
    "ds_read_b128 v[156:159], v28 offset:22528\n"
    "s_add_u32 m0, s79, 81920\n"
    "v_mfma_f32_16x16x32_bf16 v[112:115], a[176:179], v[208:211], v[112:115]\n"
    "ds_read_b128 v[160:163], v27 offset:24576\n"
    "v_mfma_f32_16x16x32_bf16 v[112:115], a[180:183], v[212:215], v[112:115]\n"
    "buffer_load_dwordx4 v14, s[36:39], s29 offen offset:0 lds\n"
    "ds_read_b128 v[164:167], v28 offset:24576\n"
    "v_mfma_f32_16x16x32_bf16 v[116:119], a[188:191], v[212:215], v[116:119]\n"
    "ds_read_b128 v[168:171], v27 offset:26624\n"
    "v_mfma_f32_16x16x32_bf16 v[116:119], a[184:187], v[208:211], v[116:119]\n"
    "buffer_load_dwordx4 v15, s[36:39], s29 offen offset:1024 lds\n"
    "v_mfma_f32_16x16x32_bf16 v[124:127], a[184:187], v[216:219], v[124:127]\n"
    "ds_read_b128 v[172:175], v28 offset:26624\n"
    "v_mfma_f32_16x16x32_bf16 v[124:127], a[188:191], v[220:223], v[124:127]\n"
    "buffer_load_dwordx4 v16, s[36:39], s29 offen offset:2048 lds\n"
    "v_mfma_f32_16x16x32_bf16 v[120:123], a[180:183], v[220:223], v[120:123]\n"
    "v_mfma_f32_16x16x32_bf16 v[120:123], a[176:179], v[216:219], v[120:123]\n"
    "s_waitcnt vmcnt(7) lgkmcnt(0)\n"
    "s_barrier\n"
    "v_mfma_f32_16x16x32_bf16 v[80:83], a[192:195], v[128:131], v[80:83]\n"
    "ds_read_b64_tr_b16 a[208:209], v21 offset:32768\n"
    "ds_read_b64_tr_b16 a[210:211], v21 offset:33792\n"
    "s_bfe_u32 s30, s27, 0x4001c\n"
    "v_mfma_f32_16x16x32_bf16 v[80:83], a[196:199], v[132:135], v[80:83]\n"
    "ds_read_b64_tr_b16 a[212:213], v21 offset:40960\n"
    "v_mfma_f32_16x16x32_bf16 v[84:87], a[204:207], v[132:135], v[84:87]\n"
    "ds_read_b64_tr_b16 a[214:215], v21 offset:41984\n"
    "s_lshl_b32 s30, s30, 19\n"
    "v_mfma_f32_16x16x32_bf16 v[84:87], a[200:203], v[128:131], v[84:87]\n"
    "ds_read_b128 v[176:179], v27 offset:49152\n"
    "s_add_u32 m0, s78, 16384\n"
    "v_mfma_f32_16x16x32_bf16 v[92:95], a[200:203], v[136:139], v[92:95]\n"
    "ds_read_b128 v[180:183], v28 offset:49152\n"
    "ds_read_b64_tr_b16 a[216:217], v22 offset:32768\n"
    "v_mfma_f32_16x16x32_bf16 v[92:95], a[204:207], v[140:143], v[92:95]\n"
    "buffer_load_dwordx4 v10, s[40:43], s30 offen offset:0 lds\n"
    "v_mfma_f32_16x16x32_bf16 v[88:91], a[196:199], v[140:143], v[88:91]\n"
    "ds_read_b64_tr_b16 a[218:219], v22 offset:33792\n"
    "v_mfma_f32_16x16x32_bf16 v[88:91], a[192:195], v[136:139], v[88:91]\n"
    "buffer_load_dwordx4 v11, s[40:43], s30 offen offset:1024 lds\n"
    "v_mfma_f32_16x16x32_bf16 v[96:99], a[192:195], v[144:147], v[96:99]\n"
    "ds_read_b64_tr_b16 a[220:221], v22 offset:40960\n"
    "ds_read_b64_tr_b16 a[222:223], v22 offset:41984\n"
    "v_mfma_f32_16x16x32_bf16 v[96:99], a[196:199], v[148:151], v[96:99]\n"
    "buffer_load_dwordx4 v12, s[40:43], s30 offen offset:2048 lds\n"
    "v_mfma_f32_16x16x32_bf16 v[100:103], a[204:207], v[148:151], v[100:103]\n"
    "ds_read_b128 v[184:187], v27 offset:51200\n"
    "v_mfma_f32_16x16x32_bf16 v[100:103], a[200:203], v[144:147], v[100:103]\n"
    "buffer_load_dwordx4 v13, s[40:43], s30 offen offset:3072 lds\n"
    "v_mfma_f32_16x16x32_bf16 v[108:111], a[200:203], v[152:155], v[108:111]\n"
    "ds_read_b128 v[188:191], v28 offset:51200\n"
    "v_mfma_f32_16x16x32_bf16 v[108:111], a[204:207], v[156:159], v[108:111]\n"
    "ds_read_b128 v[192:195], v27 offset:53248\n"
    "s_bfe_u32 s31, s27, 0x4001c\n"
    "ds_read_b128 v[196:199], v28 offset:53248\n"
    "s_lshl_b32 s31, s31, 7\n"
    "v_mfma_f32_16x16x32_bf16 v[104:107], a[196:199], v[156:159], v[104:107]\n"
    "ds_read_b128 v[200:203], v27 offset:55296\n"
    "v_mfma_f32_16x16x32_bf16 v[104:107], a[192:195], v[152:155], v[104:107]\n"
    "ds_read_b128 v[204:207], v28 offset:55296\n"
    "s_add_u32 m0, s79, 0\n"
    "v_mfma_f32_16x16x32_bf16 v[112:115], a[192:195], v[160:163], v[112:115]\n"
    "ds_read_b128 v[208:211], v27 offset:57344\n"
    "Lw1_%=:\n"
    "s_cbranch_execz Lw2_%=\n"
    "v_mfma_f32_16x16x32_bf16 v[112:115], a[196:199], v[164:167], v[112:115]\n"
    "buffer_load_dwordx4 v14, s[36:39], s31 offen offset:0 lds\n"
    "ds_read_b128 v[212:215], v28 offset:57344\n"
    "v_mfma_f32_16x16x32_bf16 v[116:119], a[204:207], v[164:167], v[116:119]\n"
    "ds_read_b128 v[216:219], v27 offset:59392\n"
    "v_mfma_f32_16x16x32_bf16 v[116:119], a[200:203], v[160:163], v[116:119]\n"
    "buffer_load_dwordx4 v15, s[36:39], s31 offen offset:1024 lds\n"
    "v_mfma_f32_16x16x32_bf16 v[124:127], a[200:203], v[168:171], v[124:127]\n"
    "ds_read_b128 v[220:223], v28 offset:59392\n"
    "v_mfma_f32_16x16x32_bf16 v[124:127], a[204:207], v[172:175], v[124:127]\n"
    "buffer_load_dwordx4 v16, s[36:39], s31 offen offset:2048 lds\n"
    "v_mfma_f32_16x16x32_bf16 v[120:123], a[196:199], v[172:175], v[120:123]\n"
    "v_mfma_f32_16x16x32_bf16 v[120:123], a[192:195], v[168:171], v[120:123]\n"
    "s_waitcnt vmcnt(7) lgkmcnt(0)\n"
    "s_barrier\n"
    "v_mfma_f32_16x16x32_bf16 v[80:83], a[208:211], v[176:179], v[80:83]\n"
    "ds_read_b64_tr_b16 a[224:225], v23 offset:0\n"
    "ds_read_b64_tr_b16 a[226:227], v23 offset:1024\n"
    "s_bfe_u32 s28, s90, 0x40000\n"
    "v_mfma_f32_16x16x32_bf16 v[80:83], a[212:215], v[180:183], v[80:83]\n"
    "ds_read_b64_tr_b16 a[228:229], v23 offset:8192\n"
    "s_lshl_b32 s28, s28, 7\n"
    "v_mfma_f32_16x16x32_bf16 v[84:87], a[220:223], v[180:183], v[84:87]\n"
    "ds_read_b64_tr_b16 a[230:231], v23 offset:9216\n"
    "v_mfma_f32_16x16x32_bf16 v[84:87], a[216:219], v[176:179], v[84:87]\n"
    "ds_read_b128 v[128:131], v29 offset:16384\n"
    "s_add_u32 m0, s78, 98304\n"
    "ds_read_b128 v[132:135], v30 offset:16384\n"
    "v_mfma_f32_16x16x32_bf16 v[92:95], a[216:219], v[184:187], v[92:95]\n"
    "buffer_load_dwordx4 v17, s[36:39], s28 offen offset:0 lds\n"
    "v_mfma_f32_16x16x32_bf16 v[92:95], a[220:223], v[188:191], v[92:95]\n"
    "ds_read_b64_tr_b16 a[232:233], v24 offset:0\n"
    "v_mfma_f32_16x16x32_bf16 v[88:91], a[212:215], v[188:191], v[88:91]\n"
    "ds_read_b64_tr_b16 a[234:235], v24 offset:1024\n"
    "v_mfma_f32_16x16x32_bf16 v[88:91], a[208:211], v[184:187], v[88:91]\n"
    "buffer_load_dwordx4 v18, s[36:39], s28 offen offset:1024 lds\n"
    "ds_read_b64_tr_b16 a[236:237], v24 offset:8192\n"
    "v_mfma_f32_16x16x32_bf16 v[96:99], a[208:211], v[192:195], v[96:99]\n"
    "buffer_load_dwordx4 v19, s[36:39], s28 offen offset:2048 lds\n"
    "v_mfma_f32_16x16x32_bf16 v[96:99], a[212:215], v[196:199], v[96:99]\n"
    "ds_read_b64_tr_b16 a[238:239], v24 offset:9216\n"
    "v_mfma_f32_16x16x32_bf16 v[100:103], a[220:223], v[196:199], v[100:103]\n"
    "buffer_load_dwordx4 v20, s[36:39], s28 offen offset:3072 lds\n"
    "v_mfma_f32_16x16x32_bf16 v[100:103], a[216:219], v[192:195], v[100:103]\n"
    "ds_read_b128 v[136:139], v29 offset:18432\n"
    "ds_read_b128 v[140:143], v30 offset:18432\n"
    "s_bfe_u32 s29, s90, 0x40010\n"
    "v_mfma_f32_16x16x32_bf16 v[108:111], a[216:219], v[200:203], v[108:111]\n"
    "ds_read_b128 v[144:147], v29 offset:20480\n"
    "s_lshl_b32 s29, s29, 7\n"
    "v_mfma_f32_16x16x32_bf16 v[108:111], a[220:223], v[204:207], v[108:111]\n"
    "ds_read_b128 v[148:151], v30 offset:20480\n"
    "v_mfma_f32_16x16x32_bf16 v[104:107], a[212:215], v[204:207], v[104:107]\n"
    "ds_read_b128 v[152:155], v29 offset:22528\n"
    "s_add_u32 m0, s78, 114688\n"
    "ds_read_b128 v[156:159], v30 offset:22528\n"
    "v_mfma_f32_16x16x32_bf16 v[104:107], a[208:211], v[200:203], v[104:107]\n"
    "buffer_load_dwordx4 v17, s[36:39], s29 offen offset:0 lds\n"
    "v_mfma_f32_16x16x32_bf16 v[112:115], a[208:211], v[208:211], v[112:115]\n"
    "ds_read_b128 v[160:163], v29 offset:24576\n"
    "v_mfma_f32_16x16x32_bf16 v[112:115], a[212:215], v[212:215], v[112:115]\n"
    "ds_read_b128 v[164:167], v30 offset:24576\n"
    "v_mfma_f32_16x16x32_bf16 v[116:119], a[220:223], v[212:215], v[116:119]\n"
    "buffer_load_dwordx4 v18, s[36:39], s29 offen offset:1024 lds\n"
    "ds_read_b128 v[168:171], v29 offset:26624\n"
    "v_mfma_f32_16x16x32_bf16 v[116:119], a[216:219], v[208:211], v[116:119]\n"
    "buffer_load_dwordx4 v19, s[36:39], s29 offen offset:2048 lds\n"
    "v_mfma_f32_16x16x32_bf16 v[124:127], a[216:219], v[216:219], v[124:127]\n"
    "ds_read_b128 v[172:175], v30 offset:26624\n"
    "v_mfma_f32_16x16x32_bf16 v[124:127], a[220:223], v[220:223], v[124:127]\n"
    "buffer_load_dwordx4 v20, s[36:39], s29 offen offset:3072 lds\n"
    "v_mfma_f32_16x16x32_bf16 v[120:123], a[212:215], v[220:223], v[120:123]\n"
    "v_mfma_f32_16x16x32_bf16 v[120:123], a[208:211], v[216:219], v[120:123]\n"
    "s_waitcnt vmcnt(8) lgkmcnt(0)\n"
    "s_barrier\n"
    "v_mfma_f32_16x16x32_bf16 v[80:83], a[224:227], v[128:131], v[80:83]\n"
    "ds_read_b64_tr_b16 a[240:241], v21 offset:16384\n"
    "ds_read_b64_tr_b16 a[242:243], v21 offset:17408\n"
    "s_bfe_u32 s30, s91, 0x40000\n"
    "v_mfma_f32_16x16x32_bf16 v[80:83], a[228:231], v[132:135], v[80:83]\n"
    "ds_read_b64_tr_b16 a[244:245], v21 offset:24576\n"
    "s_lshl_b32 s30, s30, 7\n"
    "v_mfma_f32_16x16x32_bf16 v[84:87], a[236:239], v[132:135], v[84:87]\n"
    "ds_read_b64_tr_b16 a[246:247], v21 offset:25600\n"
    "v_mfma_f32_16x16x32_bf16 v[84:87], a[232:235], v[128:131], v[84:87]\n"
    "ds_read_b128 v[176:179], v27 offset:0\n"
    "s_add_u32 m0, s78, 131072\n"
    "ds_read_b128 v[180:183], v28 offset:0\n"
    "v_mfma_f32_16x16x32_bf16 v[92:95], a[232:235], v[136:139], v[92:95]\n"
    "buffer_load_dwordx4 v17, s[36:39], s30 offen offset:0 lds\n"
    "v_mfma_f32_16x16x32_bf16 v[92:95], a[236:239], v[140:143], v[92:95]\n"
    "ds_read_b64_tr_b16 a[248:249], v22 offset:16384\n"
    "v_mfma_f32_16x16x32_bf16 v[88:91], a[228:231], v[140:143], v[88:91]\n"
    "ds_read_b64_tr_b16 a[250:251], v22 offset:17408\n"
    "v_mfma_f32_16x16x32_bf16 v[88:91], a[224:227], v[136:139], v[88:91]\n"
    "buffer_load_dwordx4 v18, s[36:39], s30 offen offset:1024 lds\n"
    "ds_read_b64_tr_b16 a[252:253], v22 offset:24576\n"
    "v_mfma_f32_16x16x32_bf16 v[96:99], a[224:227], v[144:147], v[96:99]\n"
    "buffer_load_dwordx4 v19, s[36:39], s30 offen offset:2048 lds\n"
    "v_mfma_f32_16x16x32_bf16 v[96:99], a[228:231], v[148:151], v[96:99]\n"
    "ds_read_b64_tr_b16 a[254:255], v22 offset:25600\n"
    "v_mfma_f32_16x16x32_bf16 v[100:103], a[236:239], v[148:151], v[100:103]\n"
    "buffer_load_dwordx4 v20, s[36:39], s30 offen offset:3072 lds\n"
    "v_mfma_f32_16x16x32_bf16 v[100:103], a[232:235], v[144:147], v[100:103]\n"
    "ds_read_b128 v[184:187], v27 offset:2048\n"
    "ds_read_b128 v[188:191], v28 offset:2048\n"
    "s_bfe_u32 s31, s91, 0x40010\n"
    "v_mfma_f32_16x16x32_bf16 v[108:111], a[232:235], v[152:155], v[108:111]\n"
    "ds_read_b128 v[192:195], v27 offset:4096\n"
    "s_lshl_b32 s31, s31, 7\n"
    "v_mfma_f32_16x16x32_bf16 v[108:111], a[236:239], v[156:159], v[108:111]\n"
    "ds_read_b128 v[196:199], v28 offset:4096\n"
    "v_mfma_f32_16x16x32_bf16 v[104:107], a[228:231], v[156:159], v[104:107]\n"
    "ds_read_b128 v[200:203], v27 offset:6144\n"
    "s_add_u32 m0, s78, 147456\n"
    "ds_read_b128 v[204:207], v28 offset:6144\n"
    "v_mfma_f32_16x16x32_bf16 v[104:107], a[224:227], v[152:155], v[104:107]\n"
    "buffer_load_dwordx4 v17, s[36:39], s31 offen offset:0 lds\n"
    "v_mfma_f32_16x16x32_bf16 v[112:115], a[224:227], v[160:163], v[112:115]\n"
    "ds_read_b128 v[208:211], v27 offset:8192\n"
    "v_mfma_f32_16x16x32_bf16 v[112:115], a[228:231], v[164:167], v[112:115]\n"
    "ds_read_b128 v[212:215], v28 offset:8192\n"
    "v_mfma_f32_16x16x32_bf16 v[116:119], a[236:239], v[164:167], v[116:119]\n"
    "buffer_load_dwordx4 v18, s[36:39], s31 offen offset:1024 lds\n"
    "ds_read_b128 v[216:219], v27 offset:10240\n"
    "v_mfma_f32_16x16x32_bf16 v[116:119], a[232:235], v[160:163], v[116:119]\n"
    "buffer_load_dwordx4 v19, s[36:39], s31 offen offset:2048 lds\n"
    "v_mfma_f32_16x16x32_bf16 v[124:127], a[232:235], v[168:171], v[124:127]\n"
    "ds_read_b128 v[220:223], v28 offset:10240\n"
    "v_mfma_f32_16x16x32_bf16 v[124:127], a[236:239], v[172:175], v[124:127]\n"
    "buffer_load_dwordx4 v20, s[36:39], s31 offen offset:3072 lds\n"
    "v_mfma_f32_16x16x32_bf16 v[120:123], a[228:231], v[172:175], v[120:123]\n"
    "v_mfma_f32_16x16x32_bf16 v[120:123], a[224:227], v[168:171], v[120:123]\n"
    "s_waitcnt vmcnt(8) lgkmcnt(0)\n"
    "s_barrier\n"
    "s_branch Ljoin2_%=\n"
    "Lrole2_%=:\n"
    "s_load_dword s71, s[68:69], s70\n"
    "s_bfe_u32 s28, s26, 0x40010\n"
    "s_lshl_b32 s28, s28, 19\n"
    "s_add_u32 m0, s78, 147456\n"
    "s_nop 0\n"
    "buffer_load_dwordx4 v10, s[40:43], s28 offen offset:0 lds\n"
    "buffer_load_dwordx4 v11, s[40:43], s28 offen offset:1024 lds\n"
    "buffer_load_dwordx4 v12, s[40:43], s28 offen offset:2048 lds\n"
    "buffer_load_dwordx4 v13, s[40:43], s28 offen offset:3072 lds\n"
    "s_lshl_b32 s82, s23, 13\n"
    "s_lshl_b32 s84, s23, 7\n"
    "s_xor_b32 s83, s82, 8192\n"
    "s_xor_b32 s85, s84, 128\n"
    "s_mov_b32 s80, 0x3e0293ee\n"
    "s_mov_b32 s81, 0x3e0293ee\n"
    "s_mov_b32 s94, 0\n"
    "s_lshl_b32 s19, s96, 17\n"
    "s_lshl_b32 s20, s97, 12\n"
    "s_add_u32 s20, s20, s19\n"
    "v_xor_b32 v4, v8, v5\n"
    "v_lshlrev_b32 v4, 4, v4\n"
    "v_lshlrev_b32 v6, 8, v5\n"
    "v_add3_u32 v33, v4, v6, s20\n"
    "v_xor_b32 v34, 64, v33\n"
    "v_xor_b32 v35, 128, v33\n"
    "v_xor_b32 v36, 192, v33\n"
    "s_lshl_b32 s20, s97, 13\n"
    "s_xor_b32 s20, s20, s82\n"
    "s_add_u32 s20, s20, s19\n"
    "v_and_b32 v4, 7, v1\n"
    "v_lshlrev_b32 v6, 1, v5\n"
    "v_xor_b32 v4, v4, v6\n"
    "v_lshlrev_b32 v4, 4, v4\n"
    "v_lshrrev_b32 v6, 3, v1\n"
    "v_lshlrev_b32 v6, 8, v6\n"
    "v_add3_u32 v37, v4, v6, s20\n"
    "v_add_u32 v38, 1024, v37\n"
    "v_add_u32 v39, 2048, v37\n"
    "v_add_u32 v40, 3072, v37\n"
    "s_bfe_u32 s29, s26, 0x40014\n"
    "s_lshl_b32 s29, s29, 19\n"
    "s_add_u32 m0, s78, 16384\n"
    "s_nop 0\n"
    "buffer_load_dwordx4 v10, s[40:43], s29 offen offset:0 lds\n"
    "buffer_load_dwordx4 v11, s[40:43], s29 offen offset:1024 lds\n"
    "buffer_load_dwordx4 v12, s[40:43], s29 offen offset:2048 lds\n"
    "buffer_load_dwordx4 v13, s[40:43], s29 offen offset:3072 lds\n"
    "v_lshrrev_b32 v4, 5, v1\n"
    "v_bfe_u32 v6, v1, 4, 1\n"
    "v_lshl_or_b32 v9, v6, 1, v4\n"
    "v_lshlrev_b32 v41, 4, v1\n"
    "s_mul_i32 s17, s11, 6144\n"
    "s_add_u32 s17, s17, s8\n"
    "s_xor_b32 s64, s99, 0\n"
    "s_mul_i32 s64, s64, 24576\n"
    "s_add_u32 s64, s64, s17\n"
    "s_add_u32 s64, s64, 1048576\n"
    "s_xor_b32 s65, s99, 1\n"
    "s_mul_i32 s65, s65, 24576\n"
    "s_add_u32 s65, s65, s17\n"
    "s_add_u32 s65, s65, 1048576\n"
    "s_xor_b32 s66, s99, 2\n"
    "s_mul_i32 s66, s66, 24576\n"
    "s_add_u32 s66, s66, s17\n"
    "s_add_u32 s66, s66, 1050624\n"
    "s_xor_b32 s67, s99, 3\n"
    "s_mul_i32 s67, s67, 24576\n"
    "s_add_u32 s67, s67, s17\n"
    "s_add_u32 s67, s67, 1052672\n"
    "v_lshrrev_b32 v4, 3, v1\n"
    "v_add_u32 v4, 1, v4\n"
    "v_xor_b32 v6, s99, v4\n"
    "v_lshlrev_b32 v6, 11, v6\n"
    "v_lshlrev_b32 v4, 7, v4\n"
    "s_lshl_b32 s17, s11, 9\n"
    "s_add_u32 s17, s17, 1835008\n"
    "s_add_u32 s17, s17, s8\n"
    "s_bfe_u32 s30, s26, 0x40000\n"
    "s_lshl_b32 s30, s30, 7\n"
    "s_add_u32 m0, s79, 32768\n"
    "s_nop 0\n"
    "buffer_load_dwordx4 v14, s[36:39], s30 offen offset:0 lds\n"
    "buffer_load_dwordx4 v15, s[36:39], s30 offen offset:1024 lds\n"
    "buffer_load_dwordx4 v16, s[36:39], s30 offen offset:2048 lds\n"
    "v_add3_u32 v42, v6, v4, s17\n"
    "v_and_b32 v6, 7, v1\n"
    "v_lshl_add_u32 v42, v6, 4, v42\n"
    "v_sub_u32 v6, 23, v1\n"
    "v_ashrrev_i32 v6, 31, v6\n"
    "v_lshl_or_b32 v42, v6, 8, v42\n"
    "s_lshl_b32 s18, s99, 11\n"
    "s_add_u32 s17, s17, s18\n"
    "s_add_u32 s17, s17, 128\n"
    "v_lshlrev_b32 v4, 7, v1\n"
    "v_add_u32 v43, s17, v4\n"
    "v_and_b32 v4, 3, v1\n"
    "v_cmp_eq_u32 vcc, 0, v4\n"
    "v_mov_b32 v6, 0x3f803f80\n"
    "s_nop 0\n"
    "v_cndmask_b32 v60, 0, v6, vcc\n"
    "v_cndmask_b32 v61, 0, v6, vcc\n"
    "v_cndmask_b32 v62, 0, v6, vcc\n"
    "v_cndmask_b32 v63, 0, v6, vcc\n"
    "v_mov_b32 v56, s24\n"
    "v_mov_b32 v57, s25\n"
    "v_mov_b32 v58, s24\n"
    "v_mov_b32 v59, s25\n"
    "s_bfe_u32 s31, s26, 0x40004\n"
    "s_lshl_b32 s31, s31, 7\n"
    "s_add_u32 m0, s79, 49152\n"
    "s_nop 0\n"
    "buffer_load_dwordx4 v14, s[36:39], s31 offen offset:0 lds\n"
    "buffer_load_dwordx4 v15, s[36:39], s31 offen offset:1024 lds\n"
    "buffer_load_dwordx4 v16, s[36:39], s31 offen offset:2048 lds\n"
    "s_waitcnt vmcnt(10) lgkmcnt(0)\n"
    "s_barrier\n"
    "ds_read_b64_tr_b16 a[0:1], v21 offset:0\n"
    "ds_read_b64_tr_b16 a[2:3], v21 offset:1024\n"
    "ds_read_b64_tr_b16 a[4:5], v21 offset:8192\n"
    "ds_read_b64_tr_b16 a[6:7], v21 offset:9216\n"
    "ds_read_b64_tr_b16 a[8:9], v22 offset:0\n"
    "ds_read_b64_tr_b16 a[10:11], v22 offset:1024\n"
    "ds_read_b64_tr_b16 a[12:13], v22 offset:8192\n"
    "ds_read_b64_tr_b16 a[14:15], v22 offset:9216\n"
    "ds_read_b64_tr_b16 a[16:17], v23 offset:32768\n"
    "ds_read_b64_tr_b16 a[18:19], v23 offset:33792\n"
    "ds_read_b64_tr_b16 a[20:21], v23 offset:40960\n"
    "ds_read_b64_tr_b16 a[22:23], v23 offset:41984\n"
    "ds_read_b64_tr_b16 a[24:25], v24 offset:32768\n"
    "ds_read_b64_tr_b16 a[26:27], v24 offset:33792\n"
    "ds_read_b64_tr_b16 a[28:29], v24 offset:40960\n"
    "ds_read_b64_tr_b16 a[30:31], v24 offset:41984\n"
    "ds_read_b64_tr_b16 a[32:33], v23 offset:49152\n"
    "ds_read_b64_tr_b16 a[34:35], v23 offset:50176\n"
    "ds_read_b64_tr_b16 a[36:37], v23 offset:57344\n"
    "ds_read_b64_tr_b16 a[38:39], v23 offset:58368\n"
    "ds_read_b64_tr_b16 a[40:41], v24 offset:49152\n"
    "ds_read_b64_tr_b16 a[42:43], v24 offset:50176\n"
    "ds_read_b64_tr_b16 a[44:45], v24 offset:57344\n"
    "ds_read_b64_tr_b16 a[46:47], v24 offset:58368\n"
    "ds_read_b64_tr_b16 a[48:49], v25 offset:0\n"
    "ds_read_b64_tr_b16 a[50:51], v25 offset:1024\n"
    "ds_read_b64_tr_b16 a[52:53], v25 offset:8192\n"
    "ds_read_b64_tr_b16 a[54:55], v25 offset:9216\n"
    "ds_read_b64_tr_b16 a[56:57], v26 offset:0\n"
    "ds_read_b64_tr_b16 a[58:59], v26 offset:1024\n"
    "ds_read_b64_tr_b16 a[60:61], v26 offset:8192\n"
    "ds_read_b64_tr_b16 a[62:63], v26 offset:9216\n"
    "ds_read_b64_tr_b16 a[64:65], v25 offset:16384\n"
    "ds_read_b64_tr_b16 a[66:67], v25 offset:17408\n"
    "ds_read_b64_tr_b16 a[68:69], v25 offset:24576\n"
    "ds_read_b64_tr_b16 a[70:71], v25 offset:25600\n"
    "ds_read_b64_tr_b16 a[72:73], v26 offset:16384\n"
    "ds_read_b64_tr_b16 a[74:75], v26 offset:17408\n"
    "ds_read_b64_tr_b16 a[76:77], v26 offset:24576\n"
    "ds_read_b64_tr_b16 a[78:79], v26 offset:25600\n"
    "s_waitcnt vmcnt(3) lgkmcnt(0)\n"
    "s_barrier\n"
    "ds_read_b128 v[128:131], v27 offset:32768\n"
    "s_bfe_u32 s28, s26, 0x40018\n"
    "ds_read_b128 v[132:135], v28 offset:32768\n"
    "s_lshl_b32 s28, s28, 19\n"
    "ds_read_b128 v[136:139], v27 offset:34816\n"
    "s_add_u32 m0, s78, 65536\n"
    "ds_read_b128 v[140:143], v28 offset:34816\n"
    "buffer_load_dwordx4 v10, s[40:43], s28 offen offset:0 lds\n"
    "ds_read_b128 v[144:147], v27 offset:36864\n"
    "buffer_load_dwordx4 v11, s[40:43], s28 offen offset:1024 lds\n"
    "ds_read_b128 v[148:151], v28 offset:36864\n"
    "buffer_load_dwordx4 v12, s[40:43], s28 offen offset:2048 lds\n"
    "ds_read_b128 v[152:155], v27 offset:38912\n"
    "buffer_load_dwordx4 v13, s[40:43], s28 offen offset:3072 lds\n"
    "ds_read_b128 v[156:159], v28 offset:38912\n"
    "s_bfe_u32 s29, s26, 0x40008\n"
    "ds_read_b128 v[160:163], v27 offset:40960\n"
    "s_lshl_b32 s29, s29, 7\n"
    "ds_read_b128 v[164:167], v28 offset:40960\n"
    "s_add_u32 m0, s79, 81920\n"
    "ds_read_b128 v[168:171], v27 offset:43008\n"
    "buffer_load_dwordx4 v14, s[36:39], s29 offen offset:0 lds\n"
    "ds_read_b128 v[172:175], v28 offset:43008\n"
    "buffer_load_dwordx4 v15, s[36:39], s29 offen offset:1024 lds\n"
    "buffer_load_dwordx4 v16, s[36:39], s29 offen offset:2048 lds\n"
    "s_waitcnt vmcnt(7) lgkmcnt(0)\n"
    "s_barrier\n"
    "v_mfma_f32_16x16x32_bf16 v[80:83], a[0:3], v[128:131], 0\n"
    "ds_read_b64_tr_b16 a[80:81], v21 offset:16384\n"
    "ds_read_b64_tr_b16 a[82:83], v21 offset:17408\n"
    "v_mfma_f32_16x16x32_bf16 v[80:83], a[4:7], v[132:135], v[80:83]\n"
    "ds_read_b64_tr_b16 a[84:85], v21 offset:24576\n"
    "v_mfma_f32_16x16x32_bf16 v[84:87], a[12:15], v[132:135], 0\n"
    "ds_read_b64_tr_b16 a[86:87], v21 offset:25600\n"
    "s_bfe_u32 s30, s26, 0x4000c\n"
    "v_mfma_f32_16x16x32_bf16 v[84:87], a[8:11], v[128:131], v[84:87]\n"
    "ds_read_b128 v[176:179], v27 offset:49152\n"
    "s_nop 6\n"
    "v_mfma_f32_16x16x32_bf16 v[92:95], a[8:11], v[136:139], 0\n"
    "ds_read_b128 v[180:183], v28 offset:49152\n"
    "v_mfma_f32_16x16x32_bf16 v[92:95], a[12:15], v[140:143], v[92:95]\n"
    "ds_read_b64_tr_b16 a[88:89], v22 offset:16384\n"
    "s_lshl_b32 s30, s30, 7\n"
    "v_mfma_f32_16x16x32_bf16 v[88:91], a[4:7], v[140:143], 0\n"
    "ds_read_b64_tr_b16 a[90:91], v22 offset:17408\n"
    "v_mfma_f32_16x16x32_bf16 v[88:91], a[0:3], v[136:139], v[88:91]\n"
    "ds_read_b64_tr_b16 a[92:93], v22 offset:24576\n"
    "s_nop 6\n"
    "v_mfma_f32_16x16x32_bf16 v[96:99], a[0:3], v[144:147], 0\n"
    "ds_read_b64_tr_b16 a[94:95], v22 offset:25600\n"
    "s_add_u32 m0, s79, 0\n"
    "v_mfma_f32_16x16x32_bf16 v[96:99], a[4:7], v[148:151], v[96:99]\n"
    "ds_read_b128 v[184:187], v27 offset:51200\n"
    "v_mfma_f32_16x16x32_bf16 v[100:103], a[12:15], v[148:151], 0\n"
    "ds_read_b128 v[188:191], v28 offset:51200\n"
    "v_mfma_f32_16x16x32_bf16 v[100:103], a[8:11], v[144:147], v[100:103]\n"
    "ds_read_b128 v[192:195], v27 offset:53248\n"
    "s_nop 6\n"
    "v_mfma_f32_16x16x32_bf16 v[108:111], a[8:11], v[152:155], 0\n"
    "ds_read_b128 v[196:199], v28 offset:53248\n"
    "v_mfma_f32_16x16x32_bf16 v[108:111], a[12:15], v[156:159], v[108:111]\n"
    "buffer_load_dwordx4 v14, s[36:39], s30 offen offset:0 lds\n"
    "v_mfma_f32_16x16x32_bf16 v[104:107], a[4:7], v[156:159], 0\n"
    "ds_read_b128 v[200:203], v27 offset:55296\n"
    "v_mfma_f32_16x16x32_bf16 v[104:107], a[0:3], v[152:155], v[104:107]\n"
    "ds_read_b128 v[204:207], v28 offset:55296\n"
    "s_nop 6\n"
    "v_mfma_f32_16x16x32_bf16 v[112:115], a[0:3], v[160:163], 0\n"
    "ds_read_b128 v[208:211], v27 offset:57344\n"
    "v_mfma_f32_16x16x32_bf16 v[112:115], a[4:7], v[164:167], v[112:115]\n"
    "buffer_load_dwordx4 v15, s[36:39], s30 offen offset:1024 lds\n"
    "v_mfma_f32_16x16x32_bf16 v[116:119], a[12:15], v[164:167], 0\n"
    "ds_read_b128 v[212:215], v28 offset:57344\n"
    "v_mfma_f32_16x16x32_bf16 v[116:119], a[8:11], v[160:163], v[116:119]\n"
    "ds_read_b128 v[216:219], v27 offset:59392\n"
    "s_nop 6\n"
    "v_mfma_f32_16x16x32_bf16 v[124:127], a[8:11], v[168:171], 0\n"
    "ds_read_b128 v[220:223], v28 offset:59392\n"
    "v_mfma_f32_16x16x32_bf16 v[124:127], a[12:15], v[172:175], v[124:127]\n"
    "buffer_load_dwordx4 v16, s[36:39], s30 offen offset:2048 lds\n"
    "v_mfma_f32_16x16x32_bf16 v[120:123], a[4:7], v[172:175], 0\n"
    "v_mfma_f32_16x16x32_bf16 v[120:123], a[0:3], v[168:171], v[120:123]\n"
    "s_nop 7\n"
    "s_waitcnt vmcnt(3) lgkmcnt(0)\n"
    "s_barrier\n"
    "v_mfma_f32_16x16x32_bf16 v[80:83], a[16:19], v[176:179], v[80:83]\n"
    "ds_read_b64_tr_b16 a[96:97], v23 offset:0\n"
    "ds_read_b64_tr_b16 a[98:99], v23 offset:1024\n"
    "s_bfe_u32 s31, s26, 0x4001c\n"
    "v_mfma_f32_16x16x32_bf16 v[80:83], a[20:23], v[180:183], v[80:83]\n"
    "ds_read_b64_tr_b16 a[100:101], v23 offset:8192\n"
    "v_mfma_f32_16x16x32_bf16 v[84:87], a[28:31], v[180:183], v[84:87]\n"
    "ds_read_b64_tr_b16 a[102:103], v23 offset:9216\n"
    "s_lshl_b32 s31, s31, 19\n"
    "v_mfma_f32_16x16x32_bf16 v[84:87], a[24:27], v[176:179], v[84:87]\n"
    "ds_read_b128 v[128:131], v29 offset:16384\n"
    "s_add_u32 m0, s78, 32768\n"
    "s_nop 5\n"
    "v_mfma_f32_16x16x32_bf16 v[92:95], a[24:27], v[184:187], v[92:95]\n"
    "ds_read_b128 v[132:135], v30 offset:16384\n"
    "ds_read_b64_tr_b16 a[104:105], v24 offset:0\n"
    "v_mfma_f32_16x16x32_bf16 v[92:95], a[28:31], v[188:191], v[92:95]\n"
    "buffer_load_dwordx4 v10, s[40:43], s31 offen offset:0 lds\n"
    "v_mfma_f32_16x16x32_bf16 v[88:91], a[20:23], v[188:191], v[88:91]\n"
    "ds_read_b64_tr_b16 a[106:107], v24 offset:1024\n"
    "v_mfma_f32_16x16x32_bf16 v[88:91], a[16:19], v[184:187], v[88:91]\n"
    "buffer_load_dwordx4 v11, s[40:43], s31 offen offset:1024 lds\n"
    "s_nop 6\n"
    "v_mfma_f32_16x16x32_bf16 v[96:99], a[16:19], v[192:195], v[96:99]\n"
    "ds_read_b64_tr_b16 a[108:109], v24 offset:8192\n"
    "ds_read_b64_tr_b16 a[110:111], v24 offset:9216\n"
    "v_mfma_f32_16x16x32_bf16 v[96:99], a[20:23], v[196:199], v[96:99]\n"
    "buffer_load_dwordx4 v12, s[40:43], s31 offen offset:2048 lds\n"
    "v_mfma_f32_16x16x32_bf16 v[100:103], a[28:31], v[196:199], v[100:103]\n"
    "ds_read_b128 v[136:139], v29 offset:18432\n"
    "v_mfma_f32_16x16x32_bf16 v[100:103], a[24:27], v[192:195], v[100:103]\n"
    "buffer_load_dwordx4 v13, s[40:43], s31 offen offset:3072 lds\n"
    "s_nop 6\n"
    "v_mfma_f32_16x16x32_bf16 v[108:111], a[24:27], v[200:203], v[108:111]\n"
    "ds_read_b128 v[140:143], v30 offset:18432\n"
    "v_mfma_f32_16x16x32_bf16 v[108:111], a[28:31], v[204:207], v[108:111]\n"
    "ds_read_b128 v[144:147], v29 offset:20480\n"
    "s_bfe_u32 s28, s26, 0x40010\n"
    "ds_read_b128 v[148:151], v30 offset:20480\n"
    "s_lshl_b32 s28, s28, 7\n"
    "v_mfma_f32_16x16x32_bf16 v[104:107], a[20:23], v[204:207], v[104:107]\n"
    "ds_read_b128 v[152:155], v29 offset:22528\n"
    "v_mfma_f32_16x16x32_bf16 v[104:107], a[16:19], v[200:203], v[104:107]\n"
    "ds_read_b128 v[156:159], v30 offset:22528\n"
    "s_add_u32 m0, s79, 49152\n"
    "s_nop 5\n"
    "v_mfma_f32_16x16x32_bf16 v[112:115], a[16:19], v[208:211], v[112:115]\n"
    "ds_read_b128 v[160:163], v29 offset:24576\n"
    "v_mfma_f32_16x16x32_bf16 v[112:115], a[20:23], v[212:215], v[112:115]\n"
    "buffer_load_dwordx4 v14, s[36:39], s28 offen offset:0 lds\n"
    "ds_read_b128 v[164:167], v30 offset:24576\n"
    "v_mfma_f32_16x16x32_bf16 v[116:119], a[28:31], v[212:215], v[116:119]\n"
    "ds_read_b128 v[168:171], v29 offset:26624\n"
    "v_mfma_f32_16x16x32_bf16 v[116:119], a[24:27], v[208:211], v[116:119]\n"
    "buffer_load_dwordx4 v15, s[36:39], s28 offen offset:1024 lds\n"
    "s_nop 6\n"
    "v_mfma_f32_16x16x32_bf16 v[124:127], a[24:27], v[216:219], v[124:127]\n"
    "ds_read_b128 v[172:175], v30 offset:26624\n"
    "v_mfma_f32_16x16x32_bf16 v[124:127], a[28:31], v[220:223], v[124:127]\n"
    "buffer_load_dwordx4 v16, s[36:39], s28 offen offset:2048 lds\n"
    "v_mfma_f32_16x16x32_bf16 v[120:123], a[20:23], v[220:223], v[120:123]\n"
    "v_mfma_f32_16x16x32_bf16 v[120:123], a[16:19], v[216:219], v[120:123]\n"
    "s_nop 7\n"
    "s_waitcnt vmcnt(7) lgkmcnt(0)\n"
    "s_barrier\n"
    "v_mfma_f32_16x16x32_bf16 v[80:83], a[32:35], v[128:131], v[80:83]\n"
    "ds_read_b128 v[176:179], v27 offset:0\n"
    "s_bfe_u32 s29, s27, 0x40000\n"
    "v_mfma_f32_16x16x32_bf16 v[80:83], a[36:39], v[132:135], v[80:83]\n"
    "ds_read_b128 v[180:183], v28 offset:0\n"
    "s_lshl_b32 s29, s29, 19\n"
    "v_mfma_f32_16x16x32_bf16 v[84:87], a[44:47], v[132:135], v[84:87]\n"
    "ds_read_b128 v[184:187], v27 offset:2048\n"
    "s_add_u32 m0, s78, 16384\n"
    "v_mfma_f32_16x16x32_bf16 v[84:87], a[40:43], v[128:131], v[84:87]\n"
    "ds_read_b128 v[188:191], v28 offset:2048\n"
    "s_nop 6\n"
    "v_mfma_f32_16x16x32_bf16 v[92:95], a[40:43], v[136:139], v[92:95]\n"
    "buffer_load_dwordx4 v10, s[40:43], s29 offen offset:0 lds\n"
    "v_mfma_f32_16x16x32_bf16 v[92:95], a[44:47], v[140:143], v[92:95]\n"
    "ds_read_b128 v[192:195], v27 offset:4096\n"
    "v_mfma_f32_16x16x32_bf16 v[88:91], a[36:39], v[140:143], v[88:91]\n"
    "buffer_load_dwordx4 v11, s[40:43], s29 offen offset:1024 lds\n"
    "v_mfma_f32_16x16x32_bf16 v[88:91], a[32:35], v[136:139], v[88:91]\n"
    "ds_read_b128 v[196:199], v28 offset:4096\n"
    "s_nop 6\n"
    "v_mfma_f32_16x16x32_bf16 v[96:99], a[32:35], v[144:147], v[96:99]\n"
    "buffer_load_dwordx4 v12, s[40:43], s29 offen offset:2048 lds\n"
    "v_mfma_f32_16x16x32_bf16 v[96:99], a[36:39], v[148:151], v[96:99]\n"
    "ds_read_b128 v[200:203], v27 offset:6144\n"
    "v_mfma_f32_16x16x32_bf16 v[100:103], a[44:47], v[148:151], v[100:103]\n"
    "buffer_load_dwordx4 v13, s[40:43], s29 offen offset:3072 lds\n"
    "v_mfma_f32_16x16x32_bf16 v[100:103], a[40:43], v[144:147], v[100:103]\n"
    "ds_read_b128 v[204:207], v28 offset:6144\n"
    "s_bfe_u32 s30, s26, 0x40014\n"
    "s_nop 5\n"
    "v_mfma_f32_16x16x32_bf16 v[108:111], a[40:43], v[152:155], v[108:111]\n"
    "ds_read_b128 v[208:211], v27 offset:8192\n"
    "s_lshl_b32 s30, s30, 7\n"
    "v_mfma_f32_16x16x32_bf16 v[108:111], a[44:47], v[156:159], v[108:111]\n"
    "ds_read_b128 v[212:215], v28 offset:8192\n"
    "s_add_u32 m0, s79, 81920\n"
    "v_mfma_f32_16x16x32_bf16 v[104:107], a[36:39], v[156:159], v[104:107]\n"
    "ds_read_b128 v[216:219], v27 offset:10240\n"
    "v_mfma_f32_16x16x32_bf16 v[104:107], a[32:35], v[152:155], v[104:107]\n"
    "buffer_load_dwordx4 v14, s[36:39], s30 offen offset:0 lds\n"
    "s_nop 6\n"
    "v_mfma_f32_16x16x32_bf16 v[112:115], a[32:35], v[160:163], v[112:115]\n"
    "ds_read_b128 v[220:223], v28 offset:10240\n"
    "v_mfma_f32_16x16x32_bf16 v[112:115], a[36:39], v[164:167], v[112:115]\n"
    "buffer_load_dwordx4 v15, s[36:39], s30 offen offset:1024 lds\n"
    "v_mfma_f32_16x16x32_bf16 v[116:119], a[44:47], v[164:167], v[116:119]\n"
    "buffer_load_dwordx4 v16, s[36:39], s30 offen offset:2048 lds\n"
    "v_mfma_f32_16x16x32_bf16 v[116:119], a[40:43], v[160:163], v[116:119]\n"
    "s_nop 7\n"
    "v_mfma_f32_16x16x32_bf16 v[124:127], a[40:43], v[168:171], v[124:127]\n"
    "v_mfma_f32_16x16x32_bf16 v[124:127], a[44:47], v[172:175], v[124:127]\n"
    "v_mfma_f32_16x16x32_bf16 v[120:123], a[36:39], v[172:175], v[120:123]\n"
    "v_mfma_f32_16x16x32_bf16 v[120:123], a[32:35], v[168:171], v[120:123]\n"
    "s_nop 7\n"
    "s_waitcnt vmcnt(7) lgkmcnt(0)\n"
    "s_barrier\n"
    "v_mfma_f32_16x16x32_bf16 v[80:83], a[48:51], v[176:179], v[80:83]\n"
    "ds_read_b64_tr_b16 a[112:113], v21 offset:32768\n"
    "ds_read_b64_tr_b16 a[114:115], v21 offset:33792\n"
    "v_mfma_f32_16x16x32_bf16 v[80:83], a[52:55], v[180:183], v[80:83]\n"
    "ds_read_b64_tr_b16 a[116:117], v21 offset:40960\n"
    "v_mfma_f32_16x16x32_bf16 v[84:87], a[60:63], v[180:183], v[84:87]\n"
    "ds_read_b64_tr_b16 a[118:119], v21 offset:41984\n"
    "s_bfe_u32 s31, s26, 0x40018\n"
    "v_mfma_f32_16x16x32_bf16 v[84:87], a[56:59], v[176:179], v[84:87]\n"
    "ds_read_b128 v[128:131], v27 offset:49152\n"
    "s_nop 6\n"
    "v_mfma_f32_16x16x32_bf16 v[92:95], a[56:59], v[184:187], v[92:95]\n"
    "ds_read_b128 v[132:135], v28 offset:49152\n"
    "v_mfma_f32_16x16x32_bf16 v[92:95], a[60:63], v[188:191], v[92:95]\n"
    "ds_read_b64_tr_b16 a[120:121], v22 offset:32768\n"
    "s_lshl_b32 s31, s31, 7\n"
    "v_mfma_f32_16x16x32_bf16 v[88:91], a[52:55], v[188:191], v[88:91]\n"
    "ds_read_b64_tr_b16 a[122:123], v22 offset:33792\n"
    "v_mfma_f32_16x16x32_bf16 v[88:91], a[48:51], v[184:187], v[88:91]\n"
    "ds_read_b64_tr_b16 a[124:125], v22 offset:40960\n"
    "s_nop 6\n"
    "v_mfma_f32_16x16x32_bf16 v[96:99], a[48:51], v[192:195], v[96:99]\n"
    "ds_read_b64_tr_b16 a[126:127], v22 offset:41984\n"
    "s_add_u32 m0, s79, 65536\n"
    "v_mfma_f32_16x16x32_bf16 v[96:99], a[52:55], v[196:199], v[96:99]\n"
    "ds_read_b128 v[136:139], v27 offset:51200\n"
    "v_mfma_f32_16x16x32_bf16 v[100:103], a[60:63], v[196:199], v[100:103]\n"
    "ds_read_b128 v[140:143], v28 offset:51200\n"
    "v_mfma_f32_16x16x32_bf16 v[100:103], a[56:59], v[192:195], v[100:103]\n"
    "ds_read_b128 v[144:147], v27 offset:53248\n"
    "s_nop 6\n"
    "v_mfma_f32_16x16x32_bf16 v[108:111], a[56:59], v[200:203], v[108:111]\n"
    "ds_read_b128 v[148:151], v28 offset:53248\n"
    "v_mfma_f32_16x16x32_bf16 v[108:111], a[60:63], v[204:207], v[108:111]\n"
    "buffer_load_dwordx4 v14, s[36:39], s31 offen offset:0 lds\n"
    "v_mfma_f32_16x16x32_bf16 v[104:107], a[52:55], v[204:207], v[104:107]\n"
    "ds_read_b128 v[152:155], v27 offset:55296\n"
    "v_mfma_f32_16x16x32_bf16 v[104:107], a[48:51], v[200:203], v[104:107]\n"
    "ds_read_b128 v[156:159], v28 offset:55296\n"
    "s_nop 6\n"
    "v_mfma_f32_16x16x32_bf16 v[112:115], a[48:51], v[208:211], v[112:115]\n"
    "ds_read_b128 v[160:163], v27 offset:57344\n"
    "v_mfma_f32_16x16x32_bf16 v[112:115], a[52:55], v[212:215], v[112:115]\n"
    "buffer_load_dwordx4 v15, s[36:39], s31 offen offset:1024 lds\n"
    "v_mfma_f32_16x16x32_bf16 v[116:119], a[60:63], v[212:215], v[116:119]\n"
    "ds_read_b128 v[164:167], v28 offset:57344\n"
    "v_mfma_f32_16x16x32_bf16 v[116:119], a[56:59], v[208:211], v[116:119]\n"
    "ds_read_b128 v[168:171], v27 offset:59392\n"
    "s_nop 6\n"
    "v_mfma_f32_16x16x32_bf16 v[124:127], a[56:59], v[216:219], v[124:127]\n"
    "ds_read_b128 v[172:175], v28 offset:59392\n"
    "v_mfma_f32_16x16x32_bf16 v[124:127], a[60:63], v[220:223], v[124:127]\n"
    "buffer_load_dwordx4 v16, s[36:39], s31 offen offset:2048 lds\n"
    "v_mfma_f32_16x16x32_bf16 v[120:123], a[52:55], v[220:223], v[120:123]\n"
    "v_mfma_f32_16x16x32_bf16 v[120:123], a[48:51], v[216:219], v[120:123]\n"
    "s_nop 7\n"
    "s_waitcnt vmcnt(3) lgkmcnt(0)\n"
    "s_barrier\n"
    "v_mfma_f32_16x16x32_bf16 v[80:83], a[64:67], v[128:131], v[80:83]\n"
    "ds_read_b64_tr_b16 a[128:129], v21 offset:16384\n"
    "ds_read_b64_tr_b16 a[130:131], v21 offset:17408\n"
    "s_bfe_u32 s28, s27, 0x40004\n"
    "v_mfma_f32_16x16x32_bf16 v[80:83], a[68:71], v[132:135], v[80:83]\n"
    "ds_read_b64_tr_b16 a[132:133], v21 offset:24576\n"
    "v_mfma_f32_16x16x32_bf16 v[84:87], a[76:79], v[132:135], v[84:87]\n"
    "ds_read_b64_tr_b16 a[134:135], v21 offset:25600\n"
    "s_lshl_b32 s28, s28, 19\n"
    "v_mfma_f32_16x16x32_bf16 v[84:87], a[72:75], v[128:131], v[84:87]\n"
    "Lw2_%=:\n"
    "s_cbranch_execz Lret0_%=\n"
    "ds_read_b128 v[176:179], v29 offset:16384\n"
    "s_add_u32 m0, s78, 0\n"
    "v_mfma_f32_16x16x32_bf16 v[92:95], a[72:75], v[136:139], v[92:95]\n"
    "ds_read_b128 v[180:183], v30 offset:16384\n"
    "ds_read_b64_tr_b16 a[136:137], v22 offset:16384\n"
    "v_mfma_f32_16x16x32_bf16 v[92:95], a[76:79], v[140:143], v[92:95]\n"
    "buffer_load_dwordx4 v10, s[40:43], s28 offen offset:0 lds\n"
    "v_mfma_f32_16x16x32_bf16 v[88:91], a[68:71], v[140:143], v[88:91]\n"
    "ds_read_b64_tr_b16 a[138:139], v22 offset:17408\n"
    "v_mfma_f32_16x16x32_bf16 v[88:91], a[64:67], v[136:139], v[88:91]\n"
    "buffer_load_dwordx4 v11, s[40:43], s28 offen offset:1024 lds\n"
    "v_mfma_f32_16x16x32_bf16 v[96:99], a[64:67], v[144:147], v[96:99]\n"
    "ds_read_b64_tr_b16 a[140:141], v22 offset:24576\n"
    "ds_read_b64_tr_b16 a[142:143], v22 offset:25600\n"
    "v_mfma_f32_16x16x32_bf16 v[96:99], a[68:71], v[148:151], v[96:99]\n"
    "buffer_load_dwordx4 v12, s[40:43], s28 offen offset:2048 lds\n"
    "v_mfma_f32_16x16x32_bf16 v[100:103], a[76:79], v[148:151], v[100:103]\n"
    "ds_read_b128 v[184:187], v29 offset:18432\n"
    "v_mfma_f32_16x16x32_bf16 v[100:103], a[72:75], v[144:147], v[100:103]\n"
    "buffer_load_dwordx4 v13, s[40:43], s28 offen offset:3072 lds\n"
    "v_mfma_f32_16x16x32_bf16 v[108:111], a[72:75], v[152:155], v[108:111]\n"
    "ds_read_b128 v[188:191], v30 offset:18432\n"
    "v_mfma_f32_16x16x32_bf16 v[108:111], a[76:79], v[156:159], v[108:111]\n"
    "ds_read_b128 v[192:195], v29 offset:20480\n"
    "s_bfe_u32 s29, s26, 0x4001c\n"
    "ds_read_b128 v[196:199], v30 offset:20480\n"
    "s_lshl_b32 s29, s29, 7\n"
    "v_mfma_f32_16x16x32_bf16 v[104:107], a[68:71], v[156:159], v[104:107]\n"
    "ds_read_b128 v[200:203], v29 offset:22528\n"
    "v_mfma_f32_16x16x32_bf16 v[104:107], a[64:67], v[152:155], v[104:107]\n"
    "ds_read_b128 v[204:207], v30 offset:22528\n"
    "s_add_u32 m0, s79, 49152\n"
    "v_mfma_f32_16x16x32_bf16 v[112:115], a[64:67], v[160:163], v[112:115]\n"
    "ds_read_b128 v[208:211], v29 offset:24576\n"
    "v_mfma_f32_16x16x32_bf16 v[112:115], a[68:71], v[164:167], v[112:115]\n"
    "buffer_load_dwordx4 v14, s[36:39], s29 offen offset:0 lds\n"
    "ds_read_b128 v[212:215], v30 offset:24576\n"
    "v_mfma_f32_16x16x32_bf16 v[116:119], a[76:79], v[164:167], v[116:119]\n"
    "ds_read_b128 v[216:219], v29 offset:26624\n"
    "v_mfma_f32_16x16x32_bf16 v[116:119], a[72:75], v[160:163], v[116:119]\n"
    "buffer_load_dwordx4 v15, s[36:39], s29 offen offset:1024 lds\n"
    "v_mfma_f32_16x16x32_bf16 v[124:127], a[72:75], v[168:171], v[124:127]\n"
    "ds_read_b128 v[220:223], v30 offset:26624\n"
    "v_mfma_f32_16x16x32_bf16 v[124:127], a[76:79], v[172:175], v[124:127]\n"
    "buffer_load_dwordx4 v16, s[36:39], s29 offen offset:2048 lds\n"
    "v_mfma_f32_16x16x32_bf16 v[120:123], a[68:71], v[172:175], v[120:123]\n"
    "v_mfma_f32_16x16x32_bf16 v[120:123], a[64:67], v[168:171], v[120:123]\n"
    "s_waitcnt vmcnt(7) lgkmcnt(0)\n"
    "s_barrier\n"
    "v_mfma_f32_16x16x32_bf16 v[80:83], a[80:83], v[176:179], v[80:83]\n"
    "ds_read_b128 v[128:131], v29 offset:0\n"
    "s_bfe_u32 s30, s27, 0x40008\n"
    "v_mfma_f32_16x16x32_bf16 v[80:83], a[84:87], v[180:183], v[80:83]\n"
    "ds_read_b128 v[132:135], v30 offset:0\n"
    "s_lshl_b32 s30, s30, 19\n"
    "v_mfma_f32_16x16x32_bf16 v[84:87], a[92:95], v[180:183], v[84:87]\n"
    "ds_read_b128 v[136:139], v29 offset:2048\n"
    "s_add_u32 m0, s78, 32768\n"
    "v_mfma_f32_16x16x32_bf16 v[84:87], a[88:91], v[176:179], v[84:87]\n"
    "ds_read_b128 v[140:143], v30 offset:2048\n"
    "v_mfma_f32_16x16x32_bf16 v[92:95], a[88:91], v[184:187], v[92:95]\n"
    "buffer_load_dwordx4 v10, s[40:43], s30 offen offset:0 lds\n"
    "v_mfma_f32_16x16x32_bf16 v[92:95], a[92:95], v[188:191], v[92:95]\n"
    "ds_read_b128 v[144:147], v29 offset:4096\n"
    "v_mfma_f32_16x16x32_bf16 v[88:91], a[84:87], v[188:191], v[88:91]\n"
    "buffer_load_dwordx4 v11, s[40:43], s30 offen offset:1024 lds\n"
    "v_mfma_f32_16x16x32_bf16 v[88:91], a[80:83], v[184:187], v[88:91]\n"
    "ds_read_b128 v[148:151], v30 offset:4096\n"
    "v_mfma_f32_16x16x32_bf16 v[96:99], a[80:83], v[192:195], v[96:99]\n"
    "buffer_load_dwordx4 v12, s[40:43], s30 offen offset:2048 lds\n"
    "v_mfma_f32_16x16x32_bf16 v[96:99], a[84:87], v[196:199], v[96:99]\n"
    "ds_read_b128 v[152:155], v29 offset:6144\n"
    "v_mfma_f32_16x16x32_bf16 v[100:103], a[92:95], v[196:199], v[100:103]\n"
    "buffer_load_dwordx4 v13, s[40:43], s30 offen offset:3072 lds\n"
    "v_mfma_f32_16x16x32_bf16 v[100:103], a[88:91], v[192:195], v[100:103]\n"
    "ds_read_b128 v[156:159], v30 offset:6144\n"
    "s_bfe_u32 s31, s27, 0x40000\n"
    "v_mfma_f32_16x16x32_bf16 v[108:111], a[88:91], v[200:203], v[108:111]\n"
    "ds_read_b128 v[160:163], v29 offset:8192\n"
    "s_lshl_b32 s31, s31, 7\n"
    "v_mfma_f32_16x16x32_bf16 v[108:111], a[92:95], v[204:207], v[108:111]\n"
    "ds_read_b128 v[164:167], v30 offset:8192\n"
    "s_add_u32 m0, s79, 81920\n"
    "v_mfma_f32_16x16x32_bf16 v[104:107], a[84:87], v[204:207], v[104:107]\n"
    "ds_read_b128 v[168:171], v29 offset:10240\n"
    "v_mfma_f32_16x16x32_bf16 v[104:107], a[80:83], v[200:203], v[104:107]\n"
    "buffer_load_dwordx4 v14, s[36:39], s31 offen offset:0 lds\n"
    "v_mfma_f32_16x16x32_bf16 v[112:115], a[80:83], v[208:211], v[112:115]\n"
    "ds_read_b128 v[172:175], v30 offset:10240\n"
    "v_mfma_f32_16x16x32_bf16 v[112:115], a[84:87], v[212:215], v[112:115]\n"
    "buffer_load_dwordx4 v15, s[36:39], s31 offen offset:1024 lds\n"
    "v_mfma_f32_16x16x32_bf16 v[116:119], a[92:95], v[212:215], v[116:119]\n"
    "buffer_load_dwordx4 v16, s[36:39], s31 offen offset:2048 lds\n"
    "v_mfma_f32_16x16x32_bf16 v[116:119], a[88:91], v[208:211], v[116:119]\n"
    "v_mfma_f32_16x16x32_bf16 v[124:127], a[88:91], v[216:219], v[124:127]\n"
    "v_mfma_f32_16x16x32_bf16 v[124:127], a[92:95], v[220:223], v[124:127]\n"
    "v_mfma_f32_16x16x32_bf16 v[120:123], a[84:87], v[220:223], v[120:123]\n"
    "v_mfma_f32_16x16x32_bf16 v[120:123], a[80:83], v[216:219], v[120:123]\n"
    "s_waitcnt vmcnt(7) lgkmcnt(0)\n"
    "s_barrier\n"
    "v_mfma_f32_16x16x32_bf16 v[80:83], a[96:99], v[128:131], v[80:83]\n"
    "ds_read_b64_tr_b16 a[144:145], v21 offset:0\n"
    "ds_read_b64_tr_b16 a[146:147], v21 offset:1024\n"
    "v_mfma_f32_16x16x32_bf16 v[80:83], a[100:103], v[132:135], v[80:83]\n"
    "ds_read_b64_tr_b16 a[148:149], v21 offset:8192\n"
    "v_mfma_f32_16x16x32_bf16 v[84:87], a[108:111], v[132:135], v[84:87]\n"
    "ds_read_b64_tr_b16 a[150:151], v21 offset:9216\n"
    "s_bfe_u32 s28, s27, 0x40004\n"
    "v_mfma_f32_16x16x32_bf16 v[84:87], a[104:107], v[128:131], v[84:87]\n"
    "ds_read_b128 v[176:179], v27 offset:49152\n"
    "v_mfma_f32_16x16x32_bf16 v[92:95], a[104:107], v[136:139], v[92:95]\n"
    "ds_read_b128 v[180:183], v28 offset:49152\n"
    "v_mfma_f32_16x16x32_bf16 v[92:95], a[108:111], v[140:143], v[92:95]\n"
    "ds_read_b64_tr_b16 a[152:153], v22 offset:0\n"
    "s_lshl_b32 s28, s28, 7\n"
    "v_mfma_f32_16x16x32_bf16 v[88:91], a[100:103], v[140:143], v[88:91]\n"
    "ds_read_b64_tr_b16 a[154:155], v22 offset:1024\n"
    "v_mfma_f32_16x16x32_bf16 v[88:91], a[96:99], v[136:139], v[88:91]\n"
    "ds_read_b64_tr_b16 a[156:157], v22 offset:8192\n"
    "v_mfma_f32_16x16x32_bf16 v[96:99], a[96:99], v[144:147], v[96:99]\n"
    "ds_read_b64_tr_b16 a[158:159], v22 offset:9216\n"
    "s_add_u32 m0, s79, 16384\n"
    "v_mfma_f32_16x16x32_bf16 v[96:99], a[100:103], v[148:151], v[96:99]\n"
    "ds_read_b128 v[184:187], v27 offset:51200\n"
    "v_mfma_f32_16x16x32_bf16 v[100:103], a[108:111], v[148:151], v[100:103]\n"
    "ds_read_b128 v[188:191], v28 offset:51200\n"
    "v_mfma_f32_16x16x32_bf16 v[100:103], a[104:107], v[144:147], v[100:103]\n"
    "ds_read_b128 v[192:195], v27 offset:53248\n"
    "v_mfma_f32_16x16x32_bf16 v[108:111], a[104:107], v[152:155], v[108:111]\n"
    "ds_read_b128 v[196:199], v28 offset:53248\n"
    "v_mfma_f32_16x16x32_bf16 v[108:111], a[108:111], v[156:159], v[108:111]\n"
    "buffer_load_dwordx4 v14, s[36:39], s28 offen offset:0 lds\n"
    "v_mfma_f32_16x16x32_bf16 v[104:107], a[100:103], v[156:159], v[104:107]\n"
    "ds_read_b128 v[200:203], v27 offset:55296\n"
    "v_mfma_f32_16x16x32_bf16 v[104:107], a[96:99], v[152:155], v[104:107]\n"
    "ds_read_b128 v[204:207], v28 offset:55296\n"
    "v_mfma_f32_16x16x32_bf16 v[112:115], a[96:99], v[160:163], v[112:115]\n"
    "ds_read_b128 v[208:211], v27 offset:57344\n"
    "v_mfma_f32_16x16x32_bf16 v[112:115], a[100:103], v[164:167], v[112:115]\n"
    "buffer_load_dwordx4 v15, s[36:39], s28 offen offset:1024 lds\n"
    "v_mfma_f32_16x16x32_bf16 v[116:119], a[108:111], v[164:167], v[116:119]\n"
    "ds_read_b128 v[212:215], v28 offset:57344\n"
    "v_mfma_f32_16x16x32_bf16 v[116:119], a[104:107], v[160:163], v[116:119]\n"
    "ds_read_b128 v[216:219], v27 offset:59392\n"
    "v_mfma_f32_16x16x32_bf16 v[124:127], a[104:107], v[168:171], v[124:127]\n"
    "ds_read_b128 v[220:223], v28 offset:59392\n"
    "v_mfma_f32_16x16x32_bf16 v[124:127], a[108:111], v[172:175], v[124:127]\n"
    "buffer_load_dwordx4 v16, s[36:39], s28 offen offset:2048 lds\n"
    "v_mfma_f32_16x16x32_bf16 v[120:123], a[100:103], v[172:175], v[120:123]\n"
    "v_mfma_f32_16x16x32_bf16 v[120:123], a[96:99], v[168:171], v[120:123]\n"
    "s_waitcnt vmcnt(3) lgkmcnt(0)\n"
    "s_barrier\n"
    "v_mfma_f32_16x16x32_bf16 v[80:83], a[112:115], v[176:179], v[80:83]\n"
    "ds_read_b64_tr_b16 a[160:161], v21 offset:32768\n"
    "ds_read_b64_tr_b16 a[162:163], v21 offset:33792\n"
    "s_bfe_u32 s29, s27, 0x4000c\n"
    "v_mfma_f32_16x16x32_bf16 v[80:83], a[116:119], v[180:183], v[80:83]\n"
    "ds_read_b64_tr_b16 a[164:165], v21 offset:40960\n"
    "v_mfma_f32_16x16x32_bf16 v[84:87], a[124:127], v[180:183], v[84:87]\n"
    "ds_read_b64_tr_b16 a[166:167], v21 offset:41984\n"
    "s_lshl_b32 s29, s29, 19\n"
    "v_mfma_f32_16x16x32_bf16 v[84:87], a[120:123], v[176:179], v[84:87]\n"
    "ds_read_b128 v[128:131], v29 offset:16384\n"
    "s_add_u32 m0, s78, 65536\n"
    "v_mfma_f32_16x16x32_bf16 v[92:95], a[120:123], v[184:187], v[92:95]\n"
    "ds_read_b128 v[132:135], v30 offset:16384\n"
    "ds_read_b64_tr_b16 a[168:169], v22 offset:32768\n"
    "v_mfma_f32_16x16x32_bf16 v[92:95], a[124:127], v[188:191], v[92:95]\n"
    "buffer_load_dwordx4 v10, s[40:43], s29 offen offset:0 lds\n"
    "v_mfma_f32_16x16x32_bf16 v[88:91], a[116:119], v[188:191], v[88:91]\n"
    "ds_read_b64_tr_b16 a[170:171], v22 offset:33792\n"
    "v_mfma_f32_16x16x32_bf16 v[88:91], a[112:115], v[184:187], v[88:91]\n"
    "buffer_load_dwordx4 v11, s[40:43], s29 offen offset:1024 lds\n"
    "v_mfma_f32_16x16x32_bf16 v[96:99], a[112:115], v[192:195], v[96:99]\n"
    "ds_read_b64_tr_b16 a[172:173], v22 offset:40960\n"
    "ds_read_b64_tr_b16 a[174:175], v22 offset:41984\n"
    "v_mfma_f32_16x16x32_bf16 v[96:99], a[116:119], v[196:199], v[96:99]\n"
    "buffer_load_dwordx4 v12, s[40:43], s29 offen offset:2048 lds\n"
    "v_mfma_f32_16x16x32_bf16 v[100:103], a[124:127], v[196:199], v[100:103]\n"
    "ds_read_b128 v[136:139], v29 offset:18432\n"
    "v_mfma_f32_16x16x32_bf16 v[100:103], a[120:123], v[192:195], v[100:103]\n"
    "buffer_load_dwordx4 v13, s[40:43], s29 offen offset:3072 lds\n"
    "v_mfma_f32_16x16x32_bf16 v[108:111], a[120:123], v[200:203], v[108:111]\n"
    "ds_read_b128 v[140:143], v30 offset:18432\n"
    "v_mfma_f32_16x16x32_bf16 v[108:111], a[124:127], v[204:207], v[108:111]\n"
    "ds_read_b128 v[144:147], v29 offset:20480\n"
    "s_bfe_u32 s30, s27, 0x40008\n"
    "ds_read_b128 v[148:151], v30 offset:20480\n"
    "s_lshl_b32 s30, s30, 7\n"
    "v_mfma_f32_16x16x32_bf16 v[104:107], a[116:119], v[204:207], v[104:107]\n"
    "ds_read_b128 v[152:155], v29 offset:22528\n"
    "v_mfma_f32_16x16x32_bf16 v[104:107], a[112:115], v[200:203], v[104:107]\n"
    "ds_read_b128 v[156:159], v30 offset:22528\n"
    "s_add_u32 m0, s79, 49152\n"
    "v_mfma_f32_16x16x32_bf16 v[112:115], a[112:115], v[208:211], v[112:115]\n"
    "ds_read_b128 v[160:163], v29 offset:24576\n"
    "v_mfma_f32_16x16x32_bf16 v[112:115], a[116:119], v[212:215], v[112:115]\n"
    "buffer_load_dwordx4 v14, s[36:39], s30 offen offset:0 lds\n"
    "ds_read_b128 v[164:167], v30 offset:24576\n"
    "v_mfma_f32_16x16x32_bf16 v[116:119], a[124:127], v[212:215], v[116:119]\n"
    "ds_read_b128 v[168:171], v29 offset:26624\n"
    "v_mfma_f32_16x16x32_bf16 v[116:119], a[120:123], v[208:211], v[116:119]\n"
    "buffer_load_dwordx4 v15, s[36:39], s30 offen offset:1024 lds\n"
    "v_mfma_f32_16x16x32_bf16 v[124:127], a[120:123], v[216:219], v[124:127]\n"
    "ds_read_b128 v[172:175], v30 offset:26624\n"
    "v_mfma_f32_16x16x32_bf16 v[124:127], a[124:127], v[220:223], v[124:127]\n"
    "buffer_load_dwordx4 v16, s[36:39], s30 offen offset:2048 lds\n"
    "v_mfma_f32_16x16x32_bf16 v[120:123], a[116:119], v[220:223], v[120:123]\n"
    "v_mfma_f32_16x16x32_bf16 v[120:123], a[112:115], v[216:219], v[120:123]\n"
    "s_waitcnt vmcnt(7) lgkmcnt(0)\n"
    "s_barrier\n"
    "v_mfma_f32_16x16x32_bf16 v[80:83], a[128:131], v[128:131], v[80:83]\n"
    "ds_read_b128 v[176:179], v27 offset:16384\n"
    "s_bfe_u32 s31, s27, 0x40010\n"
    "v_mfma_f32_16x16x32_bf16 v[80:83], a[132:135], v[132:135], v[80:83]\n"
    "ds_read_b128 v[180:183], v28 offset:16384\n"
    "s_lshl_b32 s31, s31, 19\n"
    "v_mfma_f32_16x16x32_bf16 v[84:87], a[140:143], v[132:135], v[84:87]\n"
    "ds_read_b128 v[184:187], v27 offset:18432\n"
    "s_add_u32 m0, s78, 0\n"
    "v_mfma_f32_16x16x32_bf16 v[84:87], a[136:139], v[128:131], v[84:87]\n"
    "ds_read_b128 v[188:191], v28 offset:18432\n"
    "v_mfma_f32_16x16x32_bf16 v[92:95], a[136:139], v[136:139], v[92:95]\n"
    "buffer_load_dwordx4 v10, s[40:43], s31 offen offset:0 lds\n"
    "v_mfma_f32_16x16x32_bf16 v[92:95], a[140:143], v[140:143], v[92:95]\n"
    "ds_read_b128 v[192:195], v27 offset:20480\n"
    "v_mfma_f32_16x16x32_bf16 v[88:91], a[132:135], v[140:143], v[88:91]\n"
    "buffer_load_dwordx4 v11, s[40:43], s31 offen offset:1024 lds\n"
    "v_mfma_f32_16x16x32_bf16 v[88:91], a[128:131], v[136:139], v[88:91]\n"
    "ds_read_b128 v[196:199], v28 offset:20480\n"
    "v_mfma_f32_16x16x32_bf16 v[96:99], a[128:131], v[144:147], v[96:99]\n"
    "buffer_load_dwordx4 v12, s[40:43], s31 offen offset:2048 lds\n"
    "v_mfma_f32_16x16x32_bf16 v[96:99], a[132:135], v[148:151], v[96:99]\n"
    "ds_read_b128 v[200:203], v27 offset:22528\n"
    "v_mfma_f32_16x16x32_bf16 v[100:103], a[140:143], v[148:151], v[100:103]\n"
    "buffer_load_dwordx4 v13, s[40:43], s31 offen offset:3072 lds\n"
    "v_mfma_f32_16x16x32_bf16 v[100:103], a[136:139], v[144:147], v[100:103]\n"
    "ds_read_b128 v[204:207], v28 offset:22528\n"
    "s_bfe_u32 s28, s27, 0x4000c\n"
    "v_mfma_f32_16x16x32_bf16 v[108:111], a[136:139], v[152:155], v[108:111]\n"
    "ds_read_b128 v[208:211], v27 offset:24576\n"
    "s_lshl_b32 s28, s28, 7\n"
    "v_mfma_f32_16x16x32_bf16 v[108:111], a[140:143], v[156:159], v[108:111]\n"
    "ds_read_b128 v[212:215], v28 offset:24576\n"
    "s_add_u32 m0, s79, 81920\n"
    "v_mfma_f32_16x16x32_bf16 v[104:107], a[132:135], v[156:159], v[104:107]\n"
    "ds_read_b128 v[216:219], v27 offset:26624\n"
    "v_mfma_f32_16x16x32_bf16 v[104:107], a[128:131], v[152:155], v[104:107]\n"
    "buffer_load_dwordx4 v14, s[36:39], s28 offen offset:0 lds\n"
    "v_mfma_f32_16x16x32_bf16 v[112:115], a[128:131], v[160:163], v[112:115]\n"
    "ds_read_b128 v[220:223], v28 offset:26624\n"
    "v_mfma_f32_16x16x32_bf16 v[112:115], a[132:135], v[164:167], v[112:115]\n"
    "buffer_load_dwordx4 v15, s[36:39], s28 offen offset:1024 lds\n"
    "v_mfma_f32_16x16x32_bf16 v[116:119], a[140:143], v[164:167], v[116:119]\n"
    "buffer_load_dwordx4 v16, s[36:39], s28 offen offset:2048 lds\n"
    "v_mfma_f32_16x16x32_bf16 v[116:119], a[136:139], v[160:163], v[116:119]\n"
    "v_mfma_f32_16x16x32_bf16 v[124:127], a[136:139], v[168:171], v[124:127]\n"
    "v_mfma_f32_16x16x32_bf16 v[124:127], a[140:143], v[172:175], v[124:127]\n"
    "v_mfma_f32_16x16x32_bf16 v[120:123], a[132:135], v[172:175], v[120:123]\n"
    "v_mfma_f32_16x16x32_bf16 v[120:123], a[128:131], v[168:171], v[120:123]\n"
    "s_waitcnt vmcnt(7) lgkmcnt(0)\n"
    "s_barrier\n"
    "v_mfma_f32_16x16x32_bf16 v[80:83], a[144:147], v[176:179], v[80:83]\n"
    "ds_read_b64_tr_b16 a[176:177], v23 offset:0\n"
    "ds_read_b64_tr_b16 a[178:179], v23 offset:1024\n"
    "v_mfma_f32_16x16x32_bf16 v[80:83], a[148:151], v[180:183], v[80:83]\n"
    "ds_read_b64_tr_b16 a[180:181], v23 offset:8192\n"
    "v_mfma_f32_16x16x32_bf16 v[84:87], a[156:159], v[180:183], v[84:87]\n"
    "ds_read_b64_tr_b16 a[182:183], v23 offset:9216\n"
    "s_bfe_u32 s29, s27, 0x40010\n"
    "v_mfma_f32_16x16x32_bf16 v[84:87], a[152:155], v[176:179], v[84:87]\n"
    "ds_read_b128 v[128:131], v27 offset:49152\n"
    "v_mfma_f32_16x16x32_bf16 v[92:95], a[152:155], v[184:187], v[92:95]\n"
    "ds_read_b128 v[132:135], v28 offset:49152\n"
    "v_mfma_f32_16x16x32_bf16 v[92:95], a[156:159], v[188:191], v[92:95]\n"
    "ds_read_b64_tr_b16 a[184:185], v24 offset:0\n"
    "s_lshl_b32 s29, s29, 7\n"
    "v_mfma_f32_16x16x32_bf16 v[88:91], a[148:151], v[188:191], v[88:91]\n"
    "ds_read_b64_tr_b16 a[186:187], v24 offset:1024\n"
    "v_mfma_f32_16x16x32_bf16 v[88:91], a[144:147], v[184:187], v[88:91]\n"
    "ds_read_b64_tr_b16 a[188:189], v24 offset:8192\n"
    "v_mfma_f32_16x16x32_bf16 v[96:99], a[144:147], v[192:195], v[96:99]\n"
    "ds_read_b64_tr_b16 a[190:191], v24 offset:9216\n"
    "s_add_u32 m0, s79, 32768\n"
    "v_mfma_f32_16x16x32_bf16 v[96:99], a[148:151], v[196:199], v[96:99]\n"
    "ds_read_b128 v[136:139], v27 offset:51200\n"
    "v_mfma_f32_16x16x32_bf16 v[100:103], a[156:159], v[196:199], v[100:103]\n"
    "ds_read_b128 v[140:143], v28 offset:51200\n"
    "v_mfma_f32_16x16x32_bf16 v[100:103], a[152:155], v[192:195], v[100:103]\n"
    "ds_read_b128 v[144:147], v27 offset:53248\n"
    "v_mfma_f32_16x16x32_bf16 v[108:111], a[152:155], v[200:203], v[108:111]\n"
    "ds_read_b128 v[148:151], v28 offset:53248\n"
    "v_mfma_f32_16x16x32_bf16 v[108:111], a[156:159], v[204:207], v[108:111]\n"
    "buffer_load_dwordx4 v14, s[36:39], s29 offen offset:0 lds\n"
    "v_mfma_f32_16x16x32_bf16 v[104:107], a[148:151], v[204:207], v[104:107]\n"
    "ds_read_b128 v[152:155], v27 offset:55296\n"
    "v_mfma_f32_16x16x32_bf16 v[104:107], a[144:147], v[200:203], v[104:107]\n"
    "ds_read_b128 v[156:159], v28 offset:55296\n"
    "v_mfma_f32_16x16x32_bf16 v[112:115], a[144:147], v[208:211], v[112:115]\n"
    "ds_read_b128 v[160:163], v27 offset:57344\n"
    "v_mfma_f32_16x16x32_bf16 v[112:115], a[148:151], v[212:215], v[112:115]\n"
    "buffer_load_dwordx4 v15, s[36:39], s29 offen offset:1024 lds\n"
    "v_mfma_f32_16x16x32_bf16 v[116:119], a[156:159], v[212:215], v[116:119]\n"
    "ds_read_b128 v[164:167], v28 offset:57344\n"
    "v_mfma_f32_16x16x32_bf16 v[116:119], a[152:155], v[208:211], v[116:119]\n"
    "ds_read_b128 v[168:171], v27 offset:59392\n"
    "v_mfma_f32_16x16x32_bf16 v[124:127], a[152:155], v[216:219], v[124:127]\n"
    "ds_read_b128 v[172:175], v28 offset:59392\n"
    "v_mfma_f32_16x16x32_bf16 v[124:127], a[156:159], v[220:223], v[124:127]\n"
    "buffer_load_dwordx4 v16, s[36:39], s29 offen offset:2048 lds\n"
    "v_mfma_f32_16x16x32_bf16 v[120:123], a[148:151], v[220:223], v[120:123]\n"
    "v_mfma_f32_16x16x32_bf16 v[120:123], a[144:147], v[216:219], v[120:123]\n"
    "s_waitcnt vmcnt(3) lgkmcnt(0)\n"
    "s_barrier\n"
    "v_mfma_f32_16x16x32_bf16 v[80:83], a[160:163], v[128:131], v[80:83]\n"
    "ds_read_b64_tr_b16 a[192:193], v21 offset:0\n"
    "ds_read_b64_tr_b16 a[194:195], v21 offset:1024\n"
    "s_bfe_u32 s30, s27, 0x40014\n"
    "v_mfma_f32_16x16x32_bf16 v[80:83], a[164:167], v[132:135], v[80:83]\n"
    "ds_read_b64_tr_b16 a[196:197], v21 offset:8192\n"
    "v_mfma_f32_16x16x32_bf16 v[84:87], a[172:175], v[132:135], v[84:87]\n"
    "ds_read_b64_tr_b16 a[198:199], v21 offset:9216\n"
    "s_lshl_b32 s30, s30, 19\n"
    "v_mfma_f32_16x16x32_bf16 v[84:87], a[168:171], v[128:131], v[84:87]\n"
    "ds_read_b128 v[176:179], v29 offset:16384\n"
    "s_add_u32 m0, s78, 16384\n"
    "v_mfma_f32_16x16x32_bf16 v[92:95], a[168:171], v[136:139], v[92:95]\n"
    "ds_read_b128 v[180:183], v30 offset:16384\n"
    "ds_read_b64_tr_b16 a[200:201], v22 offset:0\n"
    "v_mfma_f32_16x16x32_bf16 v[92:95], a[172:175], v[140:143], v[92:95]\n"
    "buffer_load_dwordx4 v10, s[40:43], s30 offen offset:0 lds\n"
    "v_mfma_f32_16x16x32_bf16 v[88:91], a[164:167], v[140:143], v[88:91]\n"
    "ds_read_b64_tr_b16 a[202:203], v22 offset:1024\n"
    "v_mfma_f32_16x16x32_bf16 v[88:91], a[160:163], v[136:139], v[88:91]\n"
    "buffer_load_dwordx4 v11, s[40:43], s30 offen offset:1024 lds\n"
    "v_mfma_f32_16x16x32_bf16 v[96:99], a[160:163], v[144:147], v[96:99]\n"
    "ds_read_b64_tr_b16 a[204:205], v22 offset:8192\n"
    "ds_read_b64_tr_b16 a[206:207], v22 offset:9216\n"
    "v_mfma_f32_16x16x32_bf16 v[96:99], a[164:167], v[148:151], v[96:99]\n"
    "buffer_load_dwordx4 v12, s[40:43], s30 offen offset:2048 lds\n"
    "v_mfma_f32_16x16x32_bf16 v[100:103], a[172:175], v[148:151], v[100:103]\n"
    "ds_read_b128 v[184:187], v29 offset:18432\n"
    "v_mfma_f32_16x16x32_bf16 v[100:103], a[168:171], v[144:147], v[100:103]\n"
    "buffer_load_dwordx4 v13, s[40:43], s30 offen offset:3072 lds\n"
    "v_mfma_f32_16x16x32_bf16 v[108:111], a[168:171], v[152:155], v[108:111]\n"
    "ds_read_b128 v[188:191], v30 offset:18432\n"
    "v_mfma_f32_16x16x32_bf16 v[108:111], a[172:175], v[156:159], v[108:111]\n"
    "ds_read_b128 v[192:195], v29 offset:20480\n"
    "s_bfe_u32 s31, s27, 0x40014\n"
    "ds_read_b128 v[196:199], v30 offset:20480\n"
    "s_lshl_b32 s31, s31, 7\n"
    "v_mfma_f32_16x16x32_bf16 v[104:107], a[164:167], v[156:159], v[104:107]\n"
    "ds_read_b128 v[200:203], v29 offset:22528\n"
    "v_mfma_f32_16x16x32_bf16 v[104:107], a[160:163], v[152:155], v[104:107]\n"
    "ds_read_b128 v[204:207], v30 offset:22528\n"
    "s_add_u32 m0, s79, 49152\n"
    "v_mfma_f32_16x16x32_bf16 v[112:115], a[160:163], v[160:163], v[112:115]\n"
    "ds_read_b128 v[208:211], v29 offset:24576\n"
    "v_mfma_f32_16x16x32_bf16 v[112:115], a[164:167], v[164:167], v[112:115]\n"
    "buffer_load_dwordx4 v14, s[36:39], s31 offen offset:0 lds\n"
    "ds_read_b128 v[212:215], v30 offset:24576\n"
    "v_mfma_f32_16x16x32_bf16 v[116:119], a[172:175], v[164:167], v[116:119]\n"
    "ds_read_b128 v[216:219], v29 offset:26624\n"
    "v_mfma_f32_16x16x32_bf16 v[116:119], a[168:171], v[160:163], v[116:119]\n"
    "buffer_load_dwordx4 v15, s[36:39], s31 offen offset:1024 lds\n"
    "v_mfma_f32_16x16x32_bf16 v[124:127], a[168:171], v[168:171], v[124:127]\n"
    "ds_read_b128 v[220:223], v30 offset:26624\n"
    "v_mfma_f32_16x16x32_bf16 v[124:127], a[172:175], v[172:175], v[124:127]\n"
    "buffer_load_dwordx4 v16, s[36:39], s31 offen offset:2048 lds\n"
    "v_mfma_f32_16x16x32_bf16 v[120:123], a[164:167], v[172:175], v[120:123]\n"
    "v_mfma_f32_16x16x32_bf16 v[120:123], a[160:163], v[168:171], v[120:123]\n"
    "s_waitcnt vmcnt(7) lgkmcnt(0)\n"
    "s_barrier\n"
    "v_mfma_f32_16x16x32_bf16 v[80:83], a[176:179], v[176:179], v[80:83]\n"
    "ds_read_b128 v[128:131], v27 offset:32768\n"
    "s_bfe_u32 s28, s27, 0x40018\n"
    "v_mfma_f32_16x16x32_bf16 v[80:83], a[180:183], v[180:183], v[80:83]\n"
    "ds_read_b128 v[132:135], v28 offset:32768\n"
    "s_lshl_b32 s28, s28, 19\n"
    "v_mfma_f32_16x16x32_bf16 v[84:87], a[188:191], v[180:183], v[84:87]\n"
    "ds_read_b128 v[136:139], v27 offset:34816\n"
    "s_add_u32 m0, s78, 65536\n"
    "v_mfma_f32_16x16x32_bf16 v[84:87], a[184:187], v[176:179], v[84:87]\n"
    "ds_read_b128 v[140:143], v28 offset:34816\n"
    "v_mfma_f32_16x16x32_bf16 v[92:95], a[184:187], v[184:187], v[92:95]\n"
    "buffer_load_dwordx4 v10, s[40:43], s28 offen offset:0 lds\n"
    "v_mfma_f32_16x16x32_bf16 v[92:95], a[188:191], v[188:191], v[92:95]\n"
    "ds_read_b128 v[144:147], v27 offset:36864\n"
    "v_mfma_f32_16x16x32_bf16 v[88:91], a[180:183], v[188:191], v[88:91]\n"
    "buffer_load_dwordx4 v11, s[40:43], s28 offen offset:1024 lds\n"
    "v_mfma_f32_16x16x32_bf16 v[88:91], a[176:179], v[184:187], v[88:91]\n"
    "ds_read_b128 v[148:151], v28 offset:36864\n"
    "v_mfma_f32_16x16x32_bf16 v[96:99], a[176:179], v[192:195], v[96:99]\n"
    "buffer_load_dwordx4 v12, s[40:43], s28 offen offset:2048 lds\n"
    "v_mfma_f32_16x16x32_bf16 v[96:99], a[180:183], v[196:199], v[96:99]\n"
    "ds_read_b128 v[152:155], v27 offset:38912\n"
    "v_mfma_f32_16x16x32_bf16 v[100:103], a[188:191], v[196:199], v[100:103]\n"
    "buffer_load_dwordx4 v13, s[40:43], s28 offen offset:3072 lds\n"
    "v_mfma_f32_16x16x32_bf16 v[100:103], a[184:187], v[192:195], v[100:103]\n"
    "ds_read_b128 v[156:159], v28 offset:38912\n"
    "s_bfe_u32 s29, s27, 0x40018\n"
    "v_mfma_f32_16x16x32_bf16 v[108:111], a[184:187], v[200:203], v[108:111]\n"
    "ds_read_b128 v[160:163], v27 offset:40960\n"
    "s_lshl_b32 s29, s29, 7\n"
    "v_mfma_f32_16x16x32_bf16 v[108:111], a[188:191], v[204:207], v[108:111]\n"
    "ds_read_b128 v[164:167], v28 offset:40960\n"
    "s_add_u32 m0, s79, 81920\n"
    "v_mfma_f32_16x16x32_bf16 v[104:107], a[180:183], v[204:207], v[104:107]\n"
    "ds_read_b128 v[168:171], v27 offset:43008\n"
    "v_mfma_f32_16x16x32_bf16 v[104:107], a[176:179], v[200:203], v[104:107]\n"
    "buffer_load_dwordx4 v14, s[36:39], s29 offen offset:0 lds\n"
    "v_mfma_f32_16x16x32_bf16 v[112:115], a[176:179], v[208:211], v[112:115]\n"
    "ds_read_b128 v[172:175], v28 offset:43008\n"
    "v_mfma_f32_16x16x32_bf16 v[112:115], a[180:183], v[212:215], v[112:115]\n"
    "buffer_load_dwordx4 v15, s[36:39], s29 offen offset:1024 lds\n"
    "v_mfma_f32_16x16x32_bf16 v[116:119], a[188:191], v[212:215], v[116:119]\n"
    "buffer_load_dwordx4 v16, s[36:39], s29 offen offset:2048 lds\n"
    "v_mfma_f32_16x16x32_bf16 v[116:119], a[184:187], v[208:211], v[116:119]\n"
    "v_mfma_f32_16x16x32_bf16 v[124:127], a[184:187], v[216:219], v[124:127]\n"
    "v_mfma_f32_16x16x32_bf16 v[124:127], a[188:191], v[220:223], v[124:127]\n"
    "v_mfma_f32_16x16x32_bf16 v[120:123], a[180:183], v[220:223], v[120:123]\n"
    "v_mfma_f32_16x16x32_bf16 v[120:123], a[176:179], v[216:219], v[120:123]\n"
    "s_waitcnt vmcnt(7) lgkmcnt(0)\n"
    "s_barrier\n"
    "v_mfma_f32_16x16x32_bf16 v[80:83], a[192:195], v[128:131], v[80:83]\n"
    "ds_read_b64_tr_b16 a[208:209], v21 offset:16384\n"
    "ds_read_b64_tr_b16 a[210:211], v21 offset:17408\n"
    "s_bfe_u32 s30, s27, 0x4001c\n"
    "v_mfma_f32_16x16x32_bf16 v[80:83], a[196:199], v[132:135], v[80:83]\n"
    "ds_read_b64_tr_b16 a[212:213], v21 offset:24576\n"
    "v_mfma_f32_16x16x32_bf16 v[84:87], a[204:207], v[132:135], v[84:87]\n"
    "ds_read_b64_tr_b16 a[214:215], v21 offset:25600\n"
    "s_lshl_b32 s30, s30, 19\n"
    "v_mfma_f32_16x16x32_bf16 v[84:87], a[200:203], v[128:131], v[84:87]\n"
    "ds_read_b128 v[176:179], v27 offset:49152\n"
    "s_add_u32 m0, s78, 0\n"
    "v_mfma_f32_16x16x32_bf16 v[92:95], a[200:203], v[136:139], v[92:95]\n"
    "ds_read_b128 v[180:183], v28 offset:49152\n"
    "ds_read_b64_tr_b16 a[216:217], v22 offset:16384\n"
    "v_mfma_f32_16x16x32_bf16 v[92:95], a[204:207], v[140:143], v[92:95]\n"
    "buffer_load_dwordx4 v10, s[40:43], s30 offen offset:0 lds\n"
    "v_mfma_f32_16x16x32_bf16 v[88:91], a[196:199], v[140:143], v[88:91]\n"
    "ds_read_b64_tr_b16 a[218:219], v22 offset:17408\n"
    "v_mfma_f32_16x16x32_bf16 v[88:91], a[192:195], v[136:139], v[88:91]\n"
    "buffer_load_dwordx4 v11, s[40:43], s30 offen offset:1024 lds\n"
    "v_mfma_f32_16x16x32_bf16 v[96:99], a[192:195], v[144:147], v[96:99]\n"
    "ds_read_b64_tr_b16 a[220:221], v22 offset:24576\n"
    "ds_read_b64_tr_b16 a[222:223], v22 offset:25600\n"
    "v_mfma_f32_16x16x32_bf16 v[96:99], a[196:199], v[148:151], v[96:99]\n"
    "buffer_load_dwordx4 v12, s[40:43], s30 offen offset:2048 lds\n"
    "v_mfma_f32_16x16x32_bf16 v[100:103], a[204:207], v[148:151], v[100:103]\n"
    "ds_read_b128 v[184:187], v27 offset:51200\n"
    "v_mfma_f32_16x16x32_bf16 v[100:103], a[200:203], v[144:147], v[100:103]\n"
    "buffer_load_dwordx4 v13, s[40:43], s30 offen offset:3072 lds\n"
    "v_mfma_f32_16x16x32_bf16 v[108:111], a[200:203], v[152:155], v[108:111]\n"
    "ds_read_b128 v[188:191], v28 offset:51200\n"
    "v_mfma_f32_16x16x32_bf16 v[108:111], a[204:207], v[156:159], v[108:111]\n"
    "ds_read_b128 v[192:195], v27 offset:53248\n"
    "s_bfe_u32 s31, s27, 0x4001c\n"
    "ds_read_b128 v[196:199], v28 offset:53248\n"
    "s_lshl_b32 s31, s31, 7\n"
    "v_mfma_f32_16x16x32_bf16 v[104:107], a[196:199], v[156:159], v[104:107]\n"
    "ds_read_b128 v[200:203], v27 offset:55296\n"
    "v_mfma_f32_16x16x32_bf16 v[104:107], a[192:195], v[152:155], v[104:107]\n"
    "ds_read_b128 v[204:207], v28 offset:55296\n"
    "s_add_u32 m0, s79, 32768\n"
    "v_mfma_f32_16x16x32_bf16 v[112:115], a[192:195], v[160:163], v[112:115]\n"
    "ds_read_b128 v[208:211], v27 offset:57344\n"
    "v_mfma_f32_16x16x32_bf16 v[112:115], a[196:199], v[164:167], v[112:115]\n"
    "buffer_load_dwordx4 v14, s[36:39], s31 offen offset:0 lds\n"
    "ds_read_b128 v[212:215], v28 offset:57344\n"
    "v_mfma_f32_16x16x32_bf16 v[116:119], a[204:207], v[164:167], v[116:119]\n"
    "ds_read_b128 v[216:219], v27 offset:59392\n"
    "v_mfma_f32_16x16x32_bf16 v[116:119], a[200:203], v[160:163], v[116:119]\n"
    "buffer_load_dwordx4 v15, s[36:39], s31 offen offset:1024 lds\n"
    "v_mfma_f32_16x16x32_bf16 v[124:127], a[200:203], v[168:171], v[124:127]\n"
    "ds_read_b128 v[220:223], v28 offset:59392\n"
    "v_mfma_f32_16x16x32_bf16 v[124:127], a[204:207], v[172:175], v[124:127]\n"
    "buffer_load_dwordx4 v16, s[36:39], s31 offen offset:2048 lds\n"
    "v_mfma_f32_16x16x32_bf16 v[120:123], a[196:199], v[172:175], v[120:123]\n"
    "v_mfma_f32_16x16x32_bf16 v[120:123], a[192:195], v[168:171], v[120:123]\n"
    "s_waitcnt vmcnt(7) lgkmcnt(0)\n"
    "s_barrier\n"
    "v_mfma_f32_16x16x32_bf16 v[80:83], a[208:211], v[176:179], v[80:83]\n"
    "ds_read_b64_tr_b16 a[224:225], v23 offset:0\n"
    "ds_read_b64_tr_b16 a[226:227], v23 offset:1024\n"
    "s_bfe_u32 s28, s90, 0x40000\n"
    "v_mfma_f32_16x16x32_bf16 v[80:83], a[212:215], v[180:183], v[80:83]\n"
    "ds_read_b64_tr_b16 a[228:229], v23 offset:8192\n"
    "s_lshl_b32 s28, s28, 7\n"
    "v_mfma_f32_16x16x32_bf16 v[84:87], a[220:223], v[180:183], v[84:87]\n"
    "ds_read_b64_tr_b16 a[230:231], v23 offset:9216\n"
    "v_mfma_f32_16x16x32_bf16 v[84:87], a[216:219], v[176:179], v[84:87]\n"
    "ds_read_b128 v[128:131], v29 offset:16384\n"
    "s_add_u32 m0, s78, 98304\n"
    "ds_read_b128 v[132:135], v30 offset:16384\n"
    "v_mfma_f32_16x16x32_bf16 v[92:95], a[216:219], v[184:187], v[92:95]\n"
    "buffer_load_dwordx4 v17, s[36:39], s28 offen offset:0 lds\n"
    "v_mfma_f32_16x16x32_bf16 v[92:95], a[220:223], v[188:191], v[92:95]\n"
    "ds_read_b64_tr_b16 a[232:233], v24 offset:0\n"
    "v_mfma_f32_16x16x32_bf16 v[88:91], a[212:215], v[188:191], v[88:91]\n"
    "ds_read_b64_tr_b16 a[234:235], v24 offset:1024\n"
    "v_mfma_f32_16x16x32_bf16 v[88:91], a[208:211], v[184:187], v[88:91]\n"
    "buffer_load_dwordx4 v18, s[36:39], s28 offen offset:1024 lds\n"
    "ds_read_b64_tr_b16 a[236:237], v24 offset:8192\n"
    "v_mfma_f32_16x16x32_bf16 v[96:99], a[208:211], v[192:195], v[96:99]\n"
    "buffer_load_dwordx4 v19, s[36:39], s28 offen offset:2048 lds\n"
    "v_mfma_f32_16x16x32_bf16 v[96:99], a[212:215], v[196:199], v[96:99]\n"
    "ds_read_b64_tr_b16 a[238:239], v24 offset:9216\n"
    "v_mfma_f32_16x16x32_bf16 v[100:103], a[220:223], v[196:199], v[100:103]\n"
    "buffer_load_dwordx4 v20, s[36:39], s28 offen offset:3072 lds\n"
    "v_mfma_f32_16x16x32_bf16 v[100:103], a[216:219], v[192:195], v[100:103]\n"
    "ds_read_b128 v[136:139], v29 offset:18432\n"
    "ds_read_b128 v[140:143], v30 offset:18432\n"
    "s_bfe_u32 s29, s90, 0x40010\n"
    "v_mfma_f32_16x16x32_bf16 v[108:111], a[216:219], v[200:203], v[108:111]\n"
    "ds_read_b128 v[144:147], v29 offset:20480\n"
    "s_lshl_b32 s29, s29, 7\n"
    "v_mfma_f32_16x16x32_bf16 v[108:111], a[220:223], v[204:207], v[108:111]\n"
    "ds_read_b128 v[148:151], v30 offset:20480\n"
    "v_mfma_f32_16x16x32_bf16 v[104:107], a[212:215], v[204:207], v[104:107]\n"
    "ds_read_b128 v[152:155], v29 offset:22528\n"
    "s_add_u32 m0, s78, 114688\n"
    "ds_read_b128 v[156:159], v30 offset:22528\n"
    "v_mfma_f32_16x16x32_bf16 v[104:107], a[208:211], v[200:203], v[104:107]\n"
    "buffer_load_dwordx4 v17, s[36:39], s29 offen offset:0 lds\n"
    "v_mfma_f32_16x16x32_bf16 v[112:115], a[208:211], v[208:211], v[112:115]\n"
    "ds_read_b128 v[160:163], v29 offset:24576\n"
    "v_mfma_f32_16x16x32_bf16 v[112:115], a[212:215], v[212:215], v[112:115]\n"
    "ds_read_b128 v[164:167], v30 offset:24576\n"
    "v_mfma_f32_16x16x32_bf16 v[116:119], a[220:223], v[212:215], v[116:119]\n"
    "buffer_load_dwordx4 v18, s[36:39], s29 offen offset:1024 lds\n"
    "ds_read_b128 v[168:171], v29 offset:26624\n"
    "v_mfma_f32_16x16x32_bf16 v[116:119], a[216:219], v[208:211], v[116:119]\n"
    "buffer_load_dwordx4 v19, s[36:39], s29 offen offset:2048 lds\n"
    "Lw3_%=:\n"
    "s_cbranch_execz Lw4_%=\n"
    "v_mfma_f32_16x16x32_bf16 v[124:127], a[216:219], v[216:219], v[124:127]\n"
    "ds_read_b128 v[172:175], v30 offset:26624\n"
    "v_mfma_f32_16x16x32_bf16 v[124:127], a[220:223], v[220:223], v[124:127]\n"
    "buffer_load_dwordx4 v20, s[36:39], s29 offen offset:3072 lds\n"
    "v_mfma_f32_16x16x32_bf16 v[120:123], a[212:215], v[220:223], v[120:123]\n"
    "v_mfma_f32_16x16x32_bf16 v[120:123], a[208:211], v[216:219], v[120:123]\n"
    "s_waitcnt vmcnt(8) lgkmcnt(0)\n"
    "s_barrier\n"
    "v_mfma_f32_16x16x32_bf16 v[80:83], a[224:227], v[128:131], v[80:83]\n"
    "ds_read_b64_tr_b16 a[240:241], v21 offset:0\n"
    "ds_read_b64_tr_b16 a[242:243], v21 offset:1024\n"
    "s_bfe_u32 s30, s91, 0x40000\n"
    "v_mfma_f32_16x16x32_bf16 v[80:83], a[228:231], v[132:135], v[80:83]\n"
    "ds_read_b64_tr_b16 a[244:245], v21 offset:8192\n"
    "s_lshl_b32 s30, s30, 7\n"
    "v_mfma_f32_16x16x32_bf16 v[84:87], a[236:239], v[132:135], v[84:87]\n"
    "ds_read_b64_tr_b16 a[246:247], v21 offset:9216\n"
    "v_mfma_f32_16x16x32_bf16 v[84:87], a[232:235], v[128:131], v[84:87]\n"
    "ds_read_b128 v[176:179], v27 offset:32768\n"
    "s_add_u32 m0, s78, 131072\n"
    "ds_read_b128 v[180:183], v28 offset:32768\n"
    "v_mfma_f32_16x16x32_bf16 v[92:95], a[232:235], v[136:139], v[92:95]\n"
    "buffer_load_dwordx4 v17, s[36:39], s30 offen offset:0 lds\n"
    "v_mfma_f32_16x16x32_bf16 v[92:95], a[236:239], v[140:143], v[92:95]\n"
    "ds_read_b64_tr_b16 a[248:249], v22 offset:0\n"
    "v_mfma_f32_16x16x32_bf16 v[88:91], a[228:231], v[140:143], v[88:91]\n"
    "ds_read_b64_tr_b16 a[250:251], v22 offset:1024\n"
    "v_mfma_f32_16x16x32_bf16 v[88:91], a[224:227], v[136:139], v[88:91]\n"
    "buffer_load_dwordx4 v18, s[36:39], s30 offen offset:1024 lds\n"
    "ds_read_b64_tr_b16 a[252:253], v22 offset:8192\n"
    "v_mfma_f32_16x16x32_bf16 v[96:99], a[224:227], v[144:147], v[96:99]\n"
    "buffer_load_dwordx4 v19, s[36:39], s30 offen offset:2048 lds\n"
    "v_mfma_f32_16x16x32_bf16 v[96:99], a[228:231], v[148:151], v[96:99]\n"
    "ds_read_b64_tr_b16 a[254:255], v22 offset:9216\n"
    "v_mfma_f32_16x16x32_bf16 v[100:103], a[236:239], v[148:151], v[100:103]\n"
    "buffer_load_dwordx4 v20, s[36:39], s30 offen offset:3072 lds\n"
    "v_mfma_f32_16x16x32_bf16 v[100:103], a[232:235], v[144:147], v[100:103]\n"
    "ds_read_b128 v[184:187], v27 offset:34816\n"
    "ds_read_b128 v[188:191], v28 offset:34816\n"
    "s_bfe_u32 s31, s91, 0x40010\n"
    "v_mfma_f32_16x16x32_bf16 v[108:111], a[232:235], v[152:155], v[108:111]\n"
    "ds_read_b128 v[192:195], v27 offset:36864\n"
    "s_lshl_b32 s31, s31, 7\n"
    "v_mfma_f32_16x16x32_bf16 v[108:111], a[236:239], v[156:159], v[108:111]\n"
    "ds_read_b128 v[196:199], v28 offset:36864\n"
    "v_mfma_f32_16x16x32_bf16 v[104:107], a[228:231], v[156:159], v[104:107]\n"
    "ds_read_b128 v[200:203], v27 offset:38912\n"
    "s_add_u32 m0, s78, 147456\n"
    "ds_read_b128 v[204:207], v28 offset:38912\n"
    "v_mfma_f32_16x16x32_bf16 v[104:107], a[224:227], v[152:155], v[104:107]\n"
    "buffer_load_dwordx4 v17, s[36:39], s31 offen offset:0 lds\n"
    "v_mfma_f32_16x16x32_bf16 v[112:115], a[224:227], v[160:163], v[112:115]\n"
    "ds_read_b128 v[208:211], v27 offset:40960\n"
    "v_mfma_f32_16x16x32_bf16 v[112:115], a[228:231], v[164:167], v[112:115]\n"
    "ds_read_b128 v[212:215], v28 offset:40960\n"
    "v_mfma_f32_16x16x32_bf16 v[116:119], a[236:239], v[164:167], v[116:119]\n"
    "buffer_load_dwordx4 v18, s[36:39], s31 offen offset:1024 lds\n"
    "ds_read_b128 v[216:219], v27 offset:43008\n"
    "v_mfma_f32_16x16x32_bf16 v[116:119], a[232:235], v[160:163], v[116:119]\n"
    "buffer_load_dwordx4 v19, s[36:39], s31 offen offset:2048 lds\n"
    "v_mfma_f32_16x16x32_bf16 v[124:127], a[232:235], v[168:171], v[124:127]\n"
    "ds_read_b128 v[220:223], v28 offset:43008\n"
    "v_mfma_f32_16x16x32_bf16 v[124:127], a[236:239], v[172:175], v[124:127]\n"
    "buffer_load_dwordx4 v20, s[36:39], s31 offen offset:3072 lds\n"
    "v_mfma_f32_16x16x32_bf16 v[120:123], a[228:231], v[172:175], v[120:123]\n"
    "v_mfma_f32_16x16x32_bf16 v[120:123], a[224:227], v[168:171], v[120:123]\n"
    "s_waitcnt vmcnt(8) lgkmcnt(0)\n"
    "s_barrier\n"
    "Ljoin2_%=:\n"
    "v_mfma_f32_16x16x32_bf16 v[80:83], a[240:243], v[176:179], v[80:83]\n"
    "ds_read_b128 v[128:131], v29 offset:32768\n"
    "v_mfma_f32_16x16x32_bf16 v[80:83], a[244:247], v[180:183], v[80:83]\n"
    "ds_read_b128 v[132:135], v30 offset:32768\n"
    "v_mfma_f32_16x16x32_bf16 v[84:87], a[252:255], v[180:183], v[84:87]\n"
    "ds_read_b128 v[136:139], v29 offset:36864\n"
    "v_mfma_f32_16x16x32_bf16 v[84:87], a[248:251], v[176:179], v[84:87]\n"
    "ds_read_b128 v[140:143], v30 offset:36864\n"
    "v_mfma_f32_16x16x32_bf16 v[92:95], a[248:251], v[184:187], v[92:95]\n"
    "ds_read_b128 v[144:147], v29 offset:34816\n"
    "v_mfma_f32_16x16x32_bf16 v[92:95], a[252:255], v[188:191], v[92:95]\n"
    "ds_read_b128 v[148:151], v30 offset:34816\n"
    "v_mfma_f32_16x16x32_bf16 v[88:91], a[244:247], v[188:191], v[88:91]\n"
    "ds_read_b128 v[152:155], v29 offset:38912\n"
    "v_mfma_f32_16x16x32_bf16 v[88:91], a[240:243], v[184:187], v[88:91]\n"
    "ds_read_b128 v[156:159], v30 offset:38912\n"
    "v_mfma_f32_16x16x32_bf16 v[96:99], a[240:243], v[192:195], v[96:99]\n"
    "v_mul_f32 v80, s80, v80\n"
    "v_mul_f32 v81, s80, v81\n"
    "v_mfma_f32_16x16x32_bf16 v[96:99], a[244:247], v[196:199], v[96:99]\n"
    "v_mul_f32 v82, s80, v82\n"
    "v_mul_f32 v83, s80, v83\n"
    "v_mfma_f32_16x16x32_bf16 v[100:103], a[252:255], v[196:199], v[100:103]\n"
    "v_cvt_pk_bf16_f32 v232, v80, v81\n"
    "v_cvt_pk_bf16_f32 v233, v82, v83\n"
    "v_mfma_f32_16x16x32_bf16 v[100:103], a[248:251], v[192:195], v[100:103]\n"
    "v_mul_f32 v84, s80, v84\n"
    "v_mul_f32 v85, s80, v85\n"
    "v_mfma_f32_16x16x32_bf16 v[108:111], a[248:251], v[200:203], v[108:111]\n"
    "v_mul_f32 v86, s80, v86\n"
    "v_mul_f32 v87, s80, v87\n"
    "v_mfma_f32_16x16x32_bf16 v[108:111], a[252:255], v[204:207], v[108:111]\n"
    "v_cvt_pk_bf16_f32 v234, v84, v85\n"
    "v_cvt_pk_bf16_f32 v235, v86, v87\n"
    "v_mfma_f32_16x16x32_bf16 v[104:107], a[244:247], v[204:207], v[104:107]\n"
    "v_mul_f32 v88, s80, v88\n"
    "v_mul_f32 v89, s80, v89\n"
    "v_mfma_f32_16x16x32_bf16 v[104:107], a[240:243], v[200:203], v[104:107]\n"
    "v_mul_f32 v90, s80, v90\n"
    "v_permlane16_swap_b32 v232, v234\n"
    "v_mfma_f32_16x16x32_bf16 v[112:115], a[240:243], v[208:211], v[112:115]\n"
    "v_permlane16_swap_b32 v233, v235\n"
    "v_mfma_f32_16x16x32_bf16 v[112:115], a[244:247], v[212:215], v[112:115]\n"
    "buffer_store_dwordx4 v[232:235], v41, s[56:59], s65 offen offset:0 sc1\n"
    "v_mul_f32 v91, s80, v91\n"
    "v_mfma_f32_16x16x32_bf16 v[116:119], a[252:255], v[212:215], v[116:119]\n"
    "v_cvt_pk_bf16_f32 v236, v88, v89\n"
    "v_cvt_pk_bf16_f32 v237, v90, v91\n"
    "v_mfma_f32_16x16x32_bf16 v[116:119], a[248:251], v[208:211], v[116:119]\n"
    "v_mul_f32 v92, s80, v92\n"
    "v_mul_f32 v93, s80, v93\n"
    "v_mfma_f32_16x16x32_bf16 v[124:127], a[248:251], v[216:219], v[124:127]\n"
    "v_mul_f32 v94, s80, v94\n"
    "v_mul_f32 v95, s80, v95\n"
    "v_mfma_f32_16x16x32_bf16 v[124:127], a[252:255], v[220:223], v[124:127]\n"
    "v_cvt_pk_bf16_f32 v238, v92, v93\n"
    "v_cvt_pk_bf16_f32 v239, v94, v95\n"
    "v_mfma_f32_16x16x32_bf16 v[120:123], a[244:247], v[220:223], v[120:123]\n"
    "v_mfma_f32_16x16x32_bf16 v[120:123], a[240:243], v[216:219], v[120:123]\n"
    "v_pk_mul_f32 v[96:97], v[96:97], s[80:81]\n"
    "v_pk_mul_f32 v[98:99], v[98:99], s[80:81]\n"
    "v_cvt_pk_bf16_f32 v240, v96, v97\n"
    "v_permlane16_swap_b32 v236, v238\n"
    "v_permlane16_swap_b32 v237, v239\n"
    "buffer_store_dwordx4 v[236:239], v41, s[56:59], s65 offen offset:1024 sc1\n"
    "v_cvt_pk_bf16_f32 v241, v98, v99\n"
    "v_pk_mul_f32 v[100:101], v[100:101], s[80:81]\n"
    "v_pk_mul_f32 v[102:103], v[102:103], s[80:81]\n"
    "v_cvt_pk_bf16_f32 v242, v100, v101\n"
    "v_cvt_pk_bf16_f32 v243, v102, v103\n"
    "v_pk_mul_f32 v[104:105], v[104:105], s[80:81]\n"
    "v_pk_mul_f32 v[106:107], v[106:107], s[80:81]\n"
    "v_cvt_pk_bf16_f32 v244, v104, v105\n"
    "v_permlane16_swap_b32 v240, v242\n"
    "v_permlane16_swap_b32 v241, v243\n"
    "buffer_store_dwordx4 v[240:243], v41, s[56:59], s66 offen offset:0 sc1\n"
    "v_cvt_pk_bf16_f32 v245, v106, v107\n"
    "v_pk_mul_f32 v[108:109], v[108:109], s[80:81]\n"
    "v_pk_mul_f32 v[110:111], v[110:111], s[80:81]\n"
    "v_cvt_pk_bf16_f32 v246, v108, v109\n"
    "v_cvt_pk_bf16_f32 v247, v110, v111\n"
    "v_pk_mul_f32 v[112:113], v[112:113], s[80:81]\n"
    "v_pk_mul_f32 v[114:115], v[114:115], s[80:81]\n"
    "v_cvt_pk_bf16_f32 v248, v112, v113\n"
    "v_permlane16_swap_b32 v244, v246\n"
    "v_permlane16_swap_b32 v245, v247\n"
    "buffer_store_dwordx4 v[244:247], v41, s[56:59], s66 offen offset:1024 sc1\n"
    "v_cvt_pk_bf16_f32 v249, v114, v115\n"
    "v_pk_mul_f32 v[116:117], v[116:117], s[80:81]\n"
    "v_pk_mul_f32 v[118:119], v[118:119], s[80:81]\n"
    "v_cvt_pk_bf16_f32 v250, v116, v117\n"
    "v_cvt_pk_bf16_f32 v251, v118, v119\n"
    "v_pk_mul_f32 v[120:121], v[120:121], s[80:81]\n"
    "v_pk_mul_f32 v[122:123], v[122:123], s[80:81]\n"
    "v_cvt_pk_bf16_f32 v252, v120, v121\n"
    "v_permlane16_swap_b32 v248, v250\n"
    "v_permlane16_swap_b32 v249, v251\n"
    "buffer_store_dwordx4 v[248:251], v41, s[56:59], s67 offen offset:0 sc1\n"
    "v_cvt_pk_bf16_f32 v253, v122, v123\n"
    "v_pk_mul_f32 v[124:125], v[124:125], s[80:81]\n"
    "v_pk_mul_f32 v[126:127], v[126:127], s[80:81]\n"
    "v_cvt_pk_bf16_f32 v254, v124, v125\n"
    "v_cvt_pk_bf16_f32 v255, v126, v127\n"
    "s_nop 0\n"
    "v_permlane16_swap_b32 v252, v254\n"
    "v_permlane16_swap_b32 v253, v255\n"
    "buffer_store_dwordx4 v[252:255], v41, s[56:59], s67 offen offset:1024 sc1\n"
    "Lcut_%=:\n"
    "v_lshrrev_b32 v5, 4, v1\n"
    "s_waitcnt lgkmcnt(0)\n"
    "v_mfma_f32_16x16x32_bf16 v[64:67], a[0:3], v[128:131], 0\n"
    "ds_read_b128 v[192:195], v29 offset:40960\n"
    "s_lshl_b32 s73, s11, 13\n"
    "v_mfma_f32_16x16x32_bf16 v[64:67], a[4:7], v[132:135], v[64:67]\n"
    "ds_read_b128 v[196:199], v30 offset:40960\n"
    "s_add_u32 s73, s73, 98304\n"
    "v_mfma_f32_16x16x32_bf16 v[64:67], a[16:19], v[136:139], v[64:67]\n"
    "ds_read_b128 v[200:203], v29 offset:45056\n"
    "v_bfe_u32 v4, v1, 2, 3\n"
    "v_mfma_f32_16x16x32_bf16 v[64:67], a[20:23], v[140:143], v[64:67]\n"
    "ds_read_b128 v[204:207], v30 offset:45056\n"
    "s_lshl_b32 s19, s11, 1\n"
    "v_mfma_f32_16x16x32_bf16 v[68:71], a[28:31], v[140:143], 0\n"
    "ds_read_b128 v[208:211], v29 offset:43008\n"
    "v_xor_b32 v4, s19, v4\n"
    "v_mfma_f32_16x16x32_bf16 v[68:71], a[24:27], v[136:139], v[68:71]\n"
    "ds_read_b128 v[212:215], v30 offset:43008\n"
    "v_lshlrev_b32 v4, 5, v4\n"
    "v_mfma_f32_16x16x32_bf16 v[68:71], a[12:15], v[132:135], v[68:71]\n"
    "ds_read_b128 v[216:219], v29 offset:47104\n"
    "v_bfe_u32 v6, v1, 2, 2\n"
    "v_mfma_f32_16x16x32_bf16 v[68:71], a[8:11], v[128:131], v[68:71]\n"
    "ds_read_b128 v[220:223], v30 offset:47104\n"
    "v_lshl_add_u32 v6, v5, 2, v6\n"
    "v_mfma_f32_16x16x32_bf16 v[76:79], a[8:11], v[144:147], 0\n"
    "v_lshlrev_b32 v6, 8, v6\n"
    "v_and_b32 v7, 3, v1\n"
    "v_mfma_f32_16x16x32_bf16 v[76:79], a[12:15], v[148:151], v[76:79]\n"
    "v_lshlrev_b32 v7, 3, v7\n"
    "v_add3_u32 v21, v4, v6, v7\n"
    "v_mfma_f32_16x16x32_bf16 v[76:79], a[24:27], v[152:155], v[76:79]\n"
    "v_xor_b32 v22, 32, v21\n"
    "v_add_u32 v23, 65536, v21\n"
    "v_mfma_f32_16x16x32_bf16 v[76:79], a[28:31], v[156:159], v[76:79]\n"
    "v_add_u32 v24, 65536, v22\n"
    "v_add_u32 v25, 131072, v21\n"
    "v_mfma_f32_16x16x32_bf16 v[72:75], a[20:23], v[156:159], 0\n"
    "v_add_u32 v26, 131072, v22\n"
    "s_lshl_b32 s19, s96, 13\n"
    "v_mfma_f32_16x16x32_bf16 v[72:75], a[16:19], v[152:155], v[72:75]\n"
    "v_xor_b32 v4, v9, v8\n"
    "v_lshlrev_b32 v4, 4, v4\n"
    "v_mfma_f32_16x16x32_bf16 v[72:75], a[4:7], v[148:151], v[72:75]\n"
    "v_lshlrev_b32 v6, 8, v8\n"
    "v_add3_u32 v160, v4, v6, s19\n"
    "v_mfma_f32_16x16x32_bf16 v[72:75], a[0:3], v[144:147], v[72:75]\n"
    "s_waitcnt lgkmcnt(0)\n"
    "v_xor_b32 v161, 64, v160\n"
    "v_mfma_f32_16x16x32_bf16 v[64:67], a[32:35], v[192:195], v[64:67]\n"
    "ds_read_b128 v[128:131], v29 offset:49152\n"
    "v_xor_b32 v162, 128, v160\n"
    "v_mfma_f32_16x16x32_bf16 v[64:67], a[36:39], v[196:199], v[64:67]\n"
    "ds_read_b128 v[132:135], v30 offset:49152\n"
    "v_xor_b32 v163, 192, v160\n"
    "v_mfma_f32_16x16x32_bf16 v[64:67], a[48:51], v[200:203], v[64:67]\n"
    "ds_read_b128 v[136:139], v29 offset:53248\n"
    "v_add_u32 v164, 65536, v160\n"
    "v_mfma_f32_16x16x32_bf16 v[64:67], a[52:55], v[204:207], v[64:67]\n"
    "ds_read_b128 v[140:143], v30 offset:53248\n"
    "v_add_u32 v165, 65536, v161\n"
    "v_mfma_f32_16x16x32_bf16 v[68:71], a[60:63], v[204:207], v[68:71]\n"
    "ds_read_b128 v[144:147], v29 offset:51200\n"
    "v_add_u32 v166, 65536, v162\n"
    "v_mfma_f32_16x16x32_bf16 v[68:71], a[56:59], v[200:203], v[68:71]\n"
    "ds_read_b128 v[148:151], v30 offset:51200\n"
    "v_add_u32 v167, 65536, v163\n"
    "v_mfma_f32_16x16x32_bf16 v[68:71], a[44:47], v[196:199], v[68:71]\n"
    "ds_read_b128 v[152:155], v29 offset:55296\n"
    "v_add_u32 v168, 131072, v160\n"
    "v_mfma_f32_16x16x32_bf16 v[68:71], a[40:43], v[192:195], v[68:71]\n"
    "ds_read_b128 v[156:159], v30 offset:55296\n"
    "v_add_u32 v169, 131072, v161\n"
    "v_mfma_f32_16x16x32_bf16 v[76:79], a[40:43], v[208:211], v[76:79]\n"
    "v_add_u32 v170, 131072, v162\n"
    "v_add_u32 v171, 131072, v163\n"
    "v_mfma_f32_16x16x32_bf16 v[76:79], a[44:47], v[212:215], v[76:79]\n"
    "v_lshrrev_b32 v4, 2, v1\n"
    "v_lshlrev_b32 v4, 7, v4\n"
    "v_mfma_f32_16x16x32_bf16 v[76:79], a[56:59], v[216:219], v[76:79]\n"
    "v_bfe_u32 v6, v1, 3, 2\n"
    "v_lshlrev_b32 v6, 5, v6\n"
    "v_mfma_f32_16x16x32_bf16 v[76:79], a[60:63], v[220:223], v[76:79]\n"
    "v_add3_u32 v4, v4, v6, v7\n"
    "v_add_u32 v172, s19, v4\n"
    "v_mfma_f32_16x16x32_bf16 v[72:75], a[52:55], v[220:223], v[72:75]\n"
    "v_xor_b32 v173, 32, v172\n"
    "v_xor_b32 v174, 64, v172\n"
    "v_mfma_f32_16x16x32_bf16 v[72:75], a[48:51], v[216:219], v[72:75]\n"
    "v_xor_b32 v175, 96, v172\n"
    "v_add_u32 v176, 65536, v172\n"
    "v_mfma_f32_16x16x32_bf16 v[72:75], a[36:39], v[212:215], v[72:75]\n"
    "v_add_u32 v177, 65536, v173\n"
    "v_add_u32 v178, 65536, v174\n"
    "v_mfma_f32_16x16x32_bf16 v[72:75], a[32:35], v[208:211], v[72:75]\n"
    "s_waitcnt lgkmcnt(0)\n"
    "v_add_u32 v179, 65536, v175\n"
    "v_mfma_f32_16x16x32_bf16 v[64:67], a[64:67], v[128:131], v[64:67]\n"
    "ds_read_b128 v[192:195], v29 offset:57344\n"
    "v_add_u32 v180, 131072, v172\n"
    "v_mfma_f32_16x16x32_bf16 v[64:67], a[68:71], v[132:135], v[64:67]\n"
    "ds_read_b128 v[196:199], v30 offset:57344\n"
    "v_add_u32 v181, 131072, v173\n"
    "v_mfma_f32_16x16x32_bf16 v[64:67], a[80:83], v[136:139], v[64:67]\n"
    "ds_read_b128 v[200:203], v29 offset:61440\n"
    "v_add_u32 v182, 131072, v174\n"
    "v_mfma_f32_16x16x32_bf16 v[64:67], a[84:87], v[140:143], v[64:67]\n"
    "ds_read_b128 v[204:207], v30 offset:61440\n"
    "v_add_u32 v183, 131072, v175\n"
    "v_mfma_f32_16x16x32_bf16 v[68:71], a[92:95], v[140:143], v[68:71]\n"
    "ds_read_b128 v[208:211], v29 offset:59392\n"
    "v_add_u32 v46, s73, v41\n"
    "v_mfma_f32_16x16x32_bf16 v[68:71], a[88:91], v[136:139], v[68:71]\n"
    "ds_read_b128 v[212:215], v30 offset:59392\n"
    "s_lshl_b32 s18, s97, 1\n"
    "v_mfma_f32_16x16x32_bf16 v[68:71], a[76:79], v[132:135], v[68:71]\n"
    "ds_read_b128 v[216:219], v29 offset:63488\n"
    "s_add_u32 s18, s18, s96\n"
    "v_mfma_f32_16x16x32_bf16 v[68:71], a[72:75], v[128:131], v[68:71]\n"
    "ds_read_b128 v[220:223], v30 offset:63488\n"
    "s_xor_b32 s18, s18, s95\n"
    "v_mfma_f32_16x16x32_bf16 v[76:79], a[72:75], v[144:147], v[76:79]\n"
    "s_waitcnt vmcnt(0)\n"
    "buffer_store_dwordx4 v[56:59], v42, s[56:59], 0 offen sc1\n"
    "v_mfma_f32_16x16x32_bf16 v[76:79], a[76:79], v[148:151], v[76:79]\n"
    "s_add_u32 s28, s82, 0\n"
    "s_add_u32 m0, s78, 0\n"
    "v_mfma_f32_16x16x32_bf16 v[76:79], a[88:91], v[152:155], v[76:79]\n"
    "buffer_load_dwordx4 v33, s[44:47], s28 offen offset:0 lds\n"
    "s_lshl_b32 s18, s18, 11\n"
    "v_mfma_f32_16x16x32_bf16 v[76:79], a[92:95], v[156:159], v[76:79]\n"
    "buffer_load_dwordx4 v34, s[44:47], s28 offen offset:1024 lds\n"
    "s_add_u32 s18, s18, 98304\n"
    "v_mfma_f32_16x16x32_bf16 v[72:75], a[84:87], v[156:159], v[72:75]\n"
    "buffer_load_dwordx4 v35, s[44:47], s28 offen offset:2048 lds\n"
    "v_add_u32 v44, s18, v41\n"
    "v_mfma_f32_16x16x32_bf16 v[72:75], a[80:83], v[152:155], v[72:75]\n"
    "buffer_load_dwordx4 v36, s[44:47], s28 offen offset:3072 lds\n"
    "s_add_u32 s29, s83, 0\n"
    "v_mfma_f32_16x16x32_bf16 v[72:75], a[68:71], v[148:151], v[72:75]\n"
    "s_add_u32 m0, s78, 16384\n"
    "v_xor_b32 v45, 2048, v44\n"
    "v_mfma_f32_16x16x32_bf16 v[72:75], a[64:67], v[144:147], v[72:75]\n"
    "s_waitcnt vmcnt(11) lgkmcnt(0)\n"
    "s_barrier\n"
    "v_mfma_f32_16x16x32_bf16 v[64:67], a[96:99], v[192:195], v[64:67]\n"
    "ds_read_b128 v[128:131], v31 offset:0\n"
    "v_lshlrev_b32 v4, 1, v5\n"
    "v_mfma_f32_16x16x32_bf16 v[64:67], a[100:103], v[196:199], v[64:67]\n"
    "ds_read_b128 v[132:135], v32 offset:0\n"
    "v_xor_b32 v4, v8, v4\n"
    "v_mfma_f32_16x16x32_bf16 v[64:67], a[112:115], v[200:203], v[64:67]\n"
    "ds_read_b128 v[136:139], v31 offset:4096\n"
    "v_lshlrev_b32 v4, 4, v4\n"
    "v_mfma_f32_16x16x32_bf16 v[64:67], a[116:119], v[204:207], v[64:67]\n"
    "ds_read_b128 v[140:143], v32 offset:4096\n"
    "v_lshlrev_b32 v6, 11, v5\n"
    "v_mfma_f32_16x16x32_bf16 v[68:71], a[124:127], v[204:207], v[68:71]\n"
    "ds_read_b128 v[144:147], v31 offset:2048\n"
    "s_lshl_b32 s19, s11, 15\n"
    "v_mfma_f32_16x16x32_bf16 v[68:71], a[120:123], v[200:203], v[68:71]\n"
    "ds_read_b128 v[148:151], v32 offset:2048\n"
    "v_add3_u32 v10, v4, v6, s19\n"
    "v_mfma_f32_16x16x32_bf16 v[68:71], a[108:111], v[196:199], v[68:71]\n"
    "ds_read_b128 v[152:155], v31 offset:6144\n"
    "v_xor_b32 v6, 128, v10\n"
    "v_mfma_f32_16x16x32_bf16 v[68:71], a[104:107], v[192:195], v[68:71]\n"
    "ds_read_b128 v[156:159], v32 offset:6144\n"
    "v_add_u32 v11, 7168, v6\n"
    "v_mfma_f32_16x16x32_bf16 v[76:79], a[104:107], v[208:211], v[76:79]\n"
    "buffer_load_dwordx4 v33, s[44:47], s29 offen offset:0 lds\n"
    "v_add_u32 v12, 14336, v10\n"
    "v_mfma_f32_16x16x32_bf16 v[76:79], a[108:111], v[212:215], v[76:79]\n"
    "buffer_load_dwordx4 v34, s[44:47], s29 offen offset:1024 lds\n"
    "v_add_u32 v13, 21504, v6\n"
    "v_mfma_f32_16x16x32_bf16 v[76:79], a[120:123], v[216:219], v[76:79]\n"
    "buffer_load_dwordx4 v35, s[44:47], s29 offen offset:2048 lds\n"
    "s_mul_i32 s18, s11, 3\n"
    "v_mfma_f32_16x16x32_bf16 v[76:79], a[124:127], v[220:223], v[76:79]\n"
    "buffer_load_dwordx4 v36, s[44:47], s29 offen offset:3072 lds\n"
    "s_lshl_b32 s20, s15, 2\n"
    "v_mfma_f32_16x16x32_bf16 v[72:75], a[116:119], v[220:223], v[72:75]\n"
    "s_add_u32 s19, s18, 4\n"
    "s_xor_b32 s19, s19, s20\n"
    "v_mfma_f32_16x16x32_bf16 v[72:75], a[112:115], v[216:219], v[72:75]\n"
    "s_lshl_b32 s19, s19, 10\n"
    "s_add_u32 s19, s19, 2048\n"
    "v_mfma_f32_16x16x32_bf16 v[72:75], a[100:103], v[212:215], v[72:75]\n"
    "v_add_u32 v14, s19, v41\n"
    "s_add_u32 s19, s18, 5\n"
    "v_mfma_f32_16x16x32_bf16 v[72:75], a[96:99], v[208:211], v[72:75]\n"
    "s_waitcnt lgkmcnt(0)\n"
    "s_xor_b32 s19, s19, s20\n"
    "v_mfma_f32_16x16x32_bf16 v[64:67], a[128:131], v[128:131], v[64:67]\n"
    "ds_read_b128 v[192:195], v31 offset:8192\n"
    "s_lshl_b32 s19, s19, 10\n"
    "v_mfma_f32_16x16x32_bf16 v[64:67], a[132:135], v[132:135], v[64:67]\n"
    "ds_read_b128 v[196:199], v32 offset:8192\n"
    "s_add_u32 s19, s19, 1024\n"
    "v_mfma_f32_16x16x32_bf16 v[64:67], a[144:147], v[136:139], v[64:67]\n"
    "ds_read_b128 v[200:203], v31 offset:12288\n"
    "v_add_u32 v15, s19, v41\n"
    "v_mfma_f32_16x16x32_bf16 v[64:67], a[148:151], v[140:143], v[64:67]\n"
    "ds_read_b128 v[204:207], v32 offset:12288\n"
    "s_add_u32 s19, s18, 6\n"
    "v_mfma_f32_16x16x32_bf16 v[68:71], a[156:159], v[140:143], v[68:71]\n"
    "ds_read_b128 v[208:211], v31 offset:10240\n"
    "s_xor_b32 s19, s19, s20\n"
    "v_mfma_f32_16x16x32_bf16 v[68:71], a[152:155], v[136:139], v[68:71]\n"
    "ds_read_b128 v[212:215], v32 offset:10240\n"
    "s_lshl_b32 s19, s19, 10\n"
    "v_mfma_f32_16x16x32_bf16 v[68:71], a[140:143], v[132:135], v[68:71]\n"
    "ds_read_b128 v[216:219], v31 offset:14336\n"
    "s_add_u32 s19, s19, 0\n"
    "v_mfma_f32_16x16x32_bf16 v[68:71], a[136:139], v[128:131], v[68:71]\n"
    "ds_read_b128 v[220:223], v32 offset:14336\n"
    "v_add_u32 v16, s19, v41\n"
    "v_mfma_f32_16x16x32_bf16 v[76:79], a[136:139], v[144:147], v[76:79]\n"
    "s_lshl_b32 s19, s15, 12\n"
    "s_add_u32 s19, s19, 2048\n"
    "v_mfma_f32_16x16x32_bf16 v[76:79], a[140:143], v[148:151], v[76:79]\n"
    "v_add_u32 v17, s19, v41\n"
    "s_lshl_b32 s17, s14, 2\n"
    "v_mfma_f32_16x16x32_bf16 v[76:79], a[152:155], v[152:155], v[76:79]\n"
    "s_and_b32 s18, s13, 1\n"
    "s_mul_i32 s18, s18, 0x11111111\n"
    "v_mfma_f32_16x16x32_bf16 v[76:79], a[156:159], v[156:159], v[76:79]\n"
    "s_add_u32 s6, s18, 0xeca86420\n"
    "s_mov_b32 s7, s6\n"
    "v_mfma_f32_16x16x32_bf16 v[72:75], a[148:151], v[156:159], v[72:75]\n"
    "s_lshr_b64 s[6:7], s[6:7], s17\n"
    "s_xor_b32 s7, s6, 0x11111111\n"
    "v_mfma_f32_16x16x32_bf16 v[72:75], a[144:147], v[152:155], v[72:75]\n"
    "s_lshl_b32 s17, s14, 3\n"
    "s_sub_u32 s18, 64, s17\n"
    "v_mfma_f32_16x16x32_bf16 v[72:75], a[132:135], v[148:151], v[72:75]\n"
    "s_mov_b32 s4, 0x76543210\n"
    "s_mov_b32 s5, 0xfedcba98\n"
    "v_mfma_f32_16x16x32_bf16 v[72:75], a[128:131], v[144:147], v[72:75]\n"
    "s_waitcnt lgkmcnt(0)\n"
    "s_lshr_b64 s[26:27], s[4:5], s17\n"
    "v_mfma_f32_16x16x32_bf16 v[64:67], a[160:163], v[192:195], v[64:67]\n"
    "ds_read_b128 v[128:131], v31 offset:16384\n"
    "s_lshl_b64 s[4:5], s[4:5], s18\n"
    "v_mfma_f32_16x16x32_bf16 v[64:67], a[164:167], v[196:199], v[64:67]\n"
    "ds_read_b128 v[132:135], v32 offset:16384\n"
    "s_or_b64 s[26:27], s[26:27], s[4:5]\n"
    "v_mfma_f32_16x16x32_bf16 v[64:67], a[176:179], v[200:203], v[64:67]\n"
    "ds_read_b128 v[136:139], v31 offset:20480\n"
    "s_cmp_lt_u32 s13, 2\n"
    "s_cselect_b64 s[26:27], s[6:7], s[26:27]\n"
    "v_mfma_f32_16x16x32_bf16 v[64:67], a[180:183], v[204:207], v[64:67]\n"
    "ds_read_b128 v[140:143], v32 offset:20480\n"
    "s_lshl_b32 s17, s11, 2\n"
    "v_mfma_f32_16x16x32_bf16 v[68:71], a[188:191], v[204:207], v[68:71]\n"
    "ds_read_b128 v[144:147], v31 offset:18432\n"
    "s_lshr_b64 s[90:91], s[26:27], s17\n"
    "v_mfma_f32_16x16x32_bf16 v[68:71], a[184:187], v[200:203], v[68:71]\n"
    "ds_read_b128 v[148:151], v32 offset:18432\n"
    "v_mfma_f32_16x16x32_bf16 v[68:71], a[172:175], v[196:199], v[68:71]\n"
    "ds_read_b128 v[152:155], v31 offset:22528\n"
    "v_mfma_f32_16x16x32_bf16 v[68:71], a[168:171], v[192:195], v[68:71]\n"
    "ds_read_b128 v[156:159], v32 offset:22528\n"
    "v_mfma_f32_16x16x32_bf16 v[76:79], a[168:171], v[208:211], v[76:79]\n"
    "v_mfma_f32_16x16x32_bf16 v[76:79], a[172:175], v[212:215], v[76:79]\n"
    "v_mfma_f32_16x16x32_bf16 v[76:79], a[184:187], v[216:219], v[76:79]\n"
    "v_mfma_f32_16x16x32_bf16 v[76:79], a[188:191], v[220:223], v[76:79]\n"
    "v_mfma_f32_16x16x32_bf16 v[72:75], a[180:183], v[220:223], v[72:75]\n"
    "v_mfma_f32_16x16x32_bf16 v[72:75], a[176:179], v[216:219], v[72:75]\n"
    "v_mfma_f32_16x16x32_bf16 v[72:75], a[164:167], v[212:215], v[72:75]\n"
    "v_mfma_f32_16x16x32_bf16 v[72:75], a[160:163], v[208:211], v[72:75]\n"
    "s_waitcnt lgkmcnt(0)\n"
    "s_mov_b32 s93, 1048576\n"
    "s_mov_b64 exec, 7\n"
    "Lpq_%=:\n"
    "buffer_load_dwordx2 v[52:53], v43, s[56:59], 0 offen sc1\n"
    "s_sub_u32 s93, s93, 1\n"
    "s_cbranch_scc1 Lpqd_%=\n"
    "s_waitcnt vmcnt(0)\n"
    "v_cmp_ne_u64 vcc, s[24:25], v[52:53]\n"
    "s_cbranch_vccnz Lpq_%=\n"
    "Lpqd_%=:\n"
    "s_waitcnt vmcnt(0)\n"
    "s_mov_b64 exec, -1\n"
    "s_add_u32 m0, s73, 2048\n"
    "s_add_u32 s93, s64, 4096\n"
    "buffer_load_dwordx4 v41, s[56:59], s64 offen offset:0 sc1 lds\n"
    "buffer_load_dwordx4 v41, s[56:59], s64 offen offset:1024 sc1 lds\n"
    "buffer_load_dwordx4 v41, s[56:59], s64 offen offset:2048 sc1 lds\n"
    "buffer_load_dwordx4 v41, s[56:59], s64 offen offset:3072 sc1 lds\n"
    "s_add_u32 m0, s73, 6144\n"
    "s_nop 0\n"
    "buffer_load_dwordx4 v41, s[56:59], s93 offen offset:0 sc1 lds\n"
    "buffer_load_dwordx4 v41, s[56:59], s93 offen offset:1024 sc1 lds\n"
    "s_barrier\n"
    "s_add_u32 s30, s82, 16384\n"
    "s_add_u32 m0, s78, 32768\n"
    "s_nop 0\n"
    "buffer_load_dwordx4 v33, s[44:47], s30 offen offset:0 lds\n"
    "buffer_load_dwordx4 v34, s[44:47], s30 offen offset:1024 lds\n"
    "buffer_load_dwordx4 v35, s[44:47], s30 offen offset:2048 lds\n"
    "buffer_load_dwordx4 v36, s[44:47], s30 offen offset:3072 lds\n"
    "v_mfma_f32_16x16x32_bf16 v[64:67], a[192:195], v[128:131], v[64:67]\n"
    "ds_read_b128 v[192:195], v31 offset:24576\n"
    "v_mfma_f32_16x16x32_bf16 v[64:67], a[196:199], v[132:135], v[64:67]\n"
    "ds_read_b128 v[196:199], v32 offset:24576\n"
    "v_mfma_f32_16x16x32_bf16 v[64:67], a[208:211], v[136:139], v[64:67]\n"
    "ds_read_b128 v[200:203], v31 offset:28672\n"
    "v_mfma_f32_16x16x32_bf16 v[64:67], a[212:215], v[140:143], v[64:67]\n"
    "ds_read_b128 v[204:207], v32 offset:28672\n"
    "v_mfma_f32_16x16x32_bf16 v[68:71], a[220:223], v[140:143], v[68:71]\n"
    "ds_read_b128 v[208:211], v31 offset:26624\n"
    "v_mfma_f32_16x16x32_bf16 v[68:71], a[216:219], v[136:139], v[68:71]\n"
    "ds_read_b128 v[212:215], v32 offset:26624\n"
    "v_mfma_f32_16x16x32_bf16 v[68:71], a[204:207], v[132:135], v[68:71]\n"
    "ds_read_b128 v[216:219], v31 offset:30720\n"
    "v_mfma_f32_16x16x32_bf16 v[68:71], a[200:203], v[128:131], v[68:71]\n"
    "ds_read_b128 v[220:223], v32 offset:30720\n"
    "v_mfma_f32_16x16x32_bf16 v[76:79], a[200:203], v[144:147], v[76:79]\n"
    "v_mfma_f32_16x16x32_bf16 v[76:79], a[204:207], v[148:151], v[76:79]\n"
    "v_mfma_f32_16x16x32_bf16 v[76:79], a[216:219], v[152:155], v[76:79]\n"
    "v_mfma_f32_16x16x32_bf16 v[76:79], a[220:223], v[156:159], v[76:79]\n"
    "v_mfma_f32_16x16x32_bf16 v[72:75], a[212:215], v[156:159], v[72:75]\n"
    "v_mfma_f32_16x16x32_bf16 v[72:75], a[208:211], v[152:155], v[72:75]\n"
    "v_mfma_f32_16x16x32_bf16 v[72:75], a[196:199], v[148:151], v[72:75]\n"
    "v_mfma_f32_16x16x32_bf16 v[72:75], a[192:195], v[144:147], v[72:75]\n"
    "s_waitcnt lgkmcnt(0)\n"
    "v_mfma_f32_16x16x32_bf16 v[64:67], a[224:227], v[192:195], v[64:67]\n"
    "v_mfma_f32_16x16x32_bf16 v[64:67], a[228:231], v[196:199], v[64:67]\n"
    "v_mfma_f32_16x16x32_bf16 v[64:67], a[240:243], v[200:203], v[64:67]\n"
    "v_mfma_f32_16x16x32_bf16 v[64:67], a[244:247], v[204:207], v[64:67]\n"
    "v_mfma_f32_16x16x32_bf16 v[68:71], a[252:255], v[204:207], v[68:71]\n"
    "v_mfma_f32_16x16x32_bf16 v[68:71], a[248:251], v[200:203], v[68:71]\n"
    "v_mfma_f32_16x16x32_bf16 v[68:71], a[236:239], v[196:199], v[68:71]\n"
    "v_mfma_f32_16x16x32_bf16 v[68:71], a[232:235], v[192:195], v[68:71]\n"
    "v_mfma_f32_16x16x32_bf16 v[76:79], a[232:235], v[208:211], v[76:79]\n"
    "v_mfma_f32_16x16x32_bf16 v[76:79], a[236:239], v[212:215], v[76:79]\n"
    "v_mfma_f32_16x16x32_bf16 v[76:79], a[248:251], v[216:219], v[76:79]\n"
    "v_mfma_f32_16x16x32_bf16 v[76:79], a[252:255], v[220:223], v[76:79]\n"
    "v_mfma_f32_16x16x32_bf16 v[72:75], a[244:247], v[220:223], v[72:75]\n"
    "v_mfma_f32_16x16x32_bf16 v[72:75], a[240:243], v[216:219], v[72:75]\n"
    "v_mfma_f32_16x16x32_bf16 v[72:75], a[228:231], v[212:215], v[72:75]\n"
    "v_mfma_f32_16x16x32_bf16 v[72:75], a[224:227], v[208:211], v[72:75]\n"
    "v_pk_mul_f32 v[64:65], v[64:65], s[80:81]\n"
    "v_pk_mul_f32 v[66:67], v[66:67], s[80:81]\n"
    "v_cvt_pk_bf16_f32 v224, v64, v65\n"
    "v_cvt_pk_bf16_f32 v225, v66, v67\n"
    "v_pk_mul_f32 v[68:69], v[68:69], s[80:81]\n"
    "v_pk_mul_f32 v[70:71], v[70:71], s[80:81]\n"
    "v_cvt_pk_bf16_f32 v226, v68, v69\n"
    "v_cvt_pk_bf16_f32 v227, v70, v71\n"
    "v_pk_mul_f32 v[72:73], v[72:73], s[80:81]\n"
    "v_pk_mul_f32 v[74:75], v[74:75], s[80:81]\n"
    "v_cvt_pk_bf16_f32 v228, v72, v73\n"
    "v_cvt_pk_bf16_f32 v229, v74, v75\n"
    "v_pk_mul_f32 v[76:77], v[76:77], s[80:81]\n"
    "v_pk_mul_f32 v[78:79], v[78:79], s[80:81]\n"
    "v_cvt_pk_bf16_f32 v230, v76, v77\n"
    "v_cvt_pk_bf16_f32 v231, v78, v79\n"
    "v_permlane16_swap_b32 v224, v226\n"
    "v_permlane16_swap_b32 v225, v227\n"
    "v_permlane16_swap_b32 v228, v230\n"
    "v_permlane16_swap_b32 v229, v231\n"
    "ds_write_b128 v46, v[224:227] offset:0\n"
    "ds_write_b128 v46, v[228:231] offset:1024\n"
    "ds_read_b128 a[64:67], v160 offset:0\n"
    "ds_read_b128 a[68:71], v161 offset:0\n"
    "ds_read_b128 a[72:75], v162 offset:0\n"
    "ds_read_b128 a[76:79], v163 offset:0\n"
    "ds_read_b128 a[80:83], v160 offset:4096\n"
    "ds_read_b128 a[84:87], v161 offset:4096\n"
    "ds_read_b128 a[88:91], v162 offset:4096\n"
    "ds_read_b128 a[92:95], v163 offset:4096\n"
    "s_waitcnt vmcnt(4) lgkmcnt(0)\n"
    "s_barrier\n"
    "s_add_u32 s31, s84, 0\n"
    "s_add_u32 m0, s78, 49152\n"
    "s_nop 0\n"
    "buffer_load_dwordx4 v37, s[48:51], s31 offen offset:0 lds\n"
    "buffer_load_dwordx4 v38, s[48:51], s31 offen offset:1024 lds\n"
    "buffer_load_dwordx4 v39, s[48:51], s31 offen offset:2048 lds\n"
    "buffer_load_dwordx4 v40, s[48:51], s31 offen offset:3072 lds\n"
    "s_add_u32 s28, s83, 16384\n"
    "s_add_u32 m0, s78, 65536\n"
    "s_nop 0\n"
    "buffer_load_dwordx4 v33, s[44:47], s28 offen offset:0 lds\n"
    "buffer_load_dwordx4 v34, s[44:47], s28 offen offset:1024 lds\n"
    "buffer_load_dwordx4 v35, s[44:47], s28 offen offset:2048 lds\n"
    "buffer_load_dwordx4 v36, s[44:47], s28 offen offset:3072 lds\n"
    "ds_read_b128 a[0:3], v44 offset:0\n"
    "ds_read_b128 a[4:7], v44 offset:8192\n"
    "ds_read_b128 a[8:11], v44 offset:16384\n"
    "ds_read_b128 a[12:15], v44 offset:24576\n"
    "ds_read_b128 a[16:19], v44 offset:1024\n"
    "ds_read_b128 a[20:23], v44 offset:9216\n"
    "ds_read_b128 a[24:27], v44 offset:17408\n"
    "ds_read_b128 a[28:31], v44 offset:25600\n"
    "ds_read_b128 a[32:35], v45 offset:0\n"
    "Lw4_%=:\n"
    "s_cbranch_execz Lw5_%=\n"
    "ds_read_b128 a[36:39], v45 offset:8192\n"
    "ds_read_b128 a[40:43], v45 offset:16384\n"
    "ds_read_b128 a[44:47], v45 offset:24576\n"
    "ds_read_b128 a[48:51], v45 offset:1024\n"
    "ds_read_b128 a[52:55], v45 offset:9216\n"
    "ds_read_b128 a[56:59], v45 offset:17408\n"
    "ds_read_b128 a[60:63], v45 offset:25600\n"
    "Lattn_%=:\n"
    "s_waitcnt lgkmcnt(12)\n"
    "v_mfma_f32_16x16x32_bf16 v[128:131], a[64:67], a[0:3], 0\n"
    "v_mfma_f32_16x16x32_bf16 v[128:131], a[68:71], a[4:7], v[128:131]\n"
    "v_mfma_f32_16x16x32_bf16 v[128:131], a[72:75], a[8:11], v[128:131]\n"
    "v_mfma_f32_16x16x32_bf16 v[128:131], a[76:79], a[12:15], v[128:131]\n"
    "s_waitcnt lgkmcnt(8)\n"
    "v_mfma_f32_16x16x32_bf16 v[132:135], a[64:67], a[16:19], 0\n"
    "v_mfma_f32_16x16x32_bf16 v[132:135], a[68:71], a[20:23], v[132:135]\n"
    "v_mfma_f32_16x16x32_bf16 v[132:135], a[72:75], a[24:27], v[132:135]\n"
    "v_mfma_f32_16x16x32_bf16 v[132:135], a[76:79], a[28:31], v[132:135]\n"
    "s_waitcnt lgkmcnt(4)\n"
    "v_mfma_f32_16x16x32_bf16 v[136:139], a[64:67], a[32:35], 0\n"
    "v_mfma_f32_16x16x32_bf16 v[136:139], a[68:71], a[36:39], v[136:139]\n"
    "v_mfma_f32_16x16x32_bf16 v[136:139], a[72:75], a[40:43], v[136:139]\n"
    "v_mfma_f32_16x16x32_bf16 v[136:139], a[76:79], a[44:47], v[136:139]\n"
    "s_waitcnt lgkmcnt(0)\n"
    "v_mfma_f32_16x16x32_bf16 v[140:143], a[64:67], a[48:51], 0\n"
    "v_mfma_f32_16x16x32_bf16 v[140:143], a[68:71], a[52:55], v[140:143]\n"
    "v_mfma_f32_16x16x32_bf16 v[140:143], a[72:75], a[56:59], v[140:143]\n"
    "v_mfma_f32_16x16x32_bf16 v[140:143], a[76:79], a[60:63], v[140:143]\n"
    "s_barrier\n"
    "s_cmp_lg_u32 s94, 0\n"
    "s_nop 6\n"
    "s_cbranch_scc1 Lhave_%=\n"
    "v_max3_f32 v52, v128, v129, v130\n"
    "v_max_f32 v52, v52, v131\n"
    "v_max3_f32 v53, v132, v133, v134\n"
    "v_max_f32 v53, v53, v135\n"
    "v_max3_f32 v54, v136, v137, v138\n"
    "v_max_f32 v54, v54, v139\n"
    "v_max3_f32 v55, v140, v141, v142\n"
    "v_max_f32 v55, v55, v143\n"
    "v_mov_b32 v56, v52\n"
    "v_mov_b32 v57, v53\n"
    "v_mov_b32 v58, v54\n"
    "v_mov_b32 v59, v55\n"
    "v_permlane16_swap_b32 v52, v56\n"
    "v_permlane16_swap_b32 v53, v57\n"
    "v_permlane16_swap_b32 v54, v58\n"
    "v_permlane16_swap_b32 v55, v59\n"
    "v_max_f32 v52, v52, v56\n"
    "v_max_f32 v53, v53, v57\n"
    "v_max_f32 v54, v54, v58\n"
    "v_max_f32 v55, v55, v59\n"
    "v_mov_b32 v56, v52\n"
    "v_mov_b32 v57, v53\n"
    "v_mov_b32 v58, v54\n"
    "v_mov_b32 v59, v55\n"
    "v_permlane32_swap_b32 v52, v56\n"
    "v_permlane32_swap_b32 v53, v57\n"
    "v_permlane32_swap_b32 v54, v58\n"
    "v_permlane32_swap_b32 v55, v59\n"
    "v_max_f32 v52, v52, v56\n"
    "v_max_f32 v53, v53, v57\n"
    "v_max_f32 v54, v54, v58\n"
    "v_max_f32 v55, v55, v59\n"
    "Lhave_%=:\n"
    "v_sub_f32 v144, 0, v52\n"
    "v_sub_f32 v145, 0, v52\n"
    "v_sub_f32 v146, 0, v52\n"
    "v_sub_f32 v147, 0, v52\n"
    "v_pk_add_f32 v[128:129], v[128:129], v[144:145]\n"
    "v_pk_add_f32 v[130:131], v[130:131], v[144:145]\n"
    "v_sub_f32 v148, 0, v53\n"
    "v_sub_f32 v149, 0, v53\n"
    "v_sub_f32 v150, 0, v53\n"
    "v_sub_f32 v151, 0, v53\n"
    "v_pk_add_f32 v[132:133], v[132:133], v[148:149]\n"
    "v_pk_add_f32 v[134:135], v[134:135], v[148:149]\n"
    "v_sub_f32 v152, 0, v54\n"
    "v_sub_f32 v153, 0, v54\n"
    "v_sub_f32 v154, 0, v54\n"
    "v_sub_f32 v155, 0, v54\n"
    "v_pk_add_f32 v[136:137], v[136:137], v[152:153]\n"
    "v_pk_add_f32 v[138:139], v[138:139], v[152:153]\n"
    "v_sub_f32 v156, 0, v55\n"
    "v_sub_f32 v157, 0, v55\n"
    "v_sub_f32 v158, 0, v55\n"
    "v_sub_f32 v159, 0, v55\n"
    "v_pk_add_f32 v[140:141], v[140:141], v[156:157]\n"
    "v_pk_add_f32 v[142:143], v[142:143], v[156:157]\n"
    "v_exp_f32 v128, v128\n"
    "v_exp_f32 v129, v129\n"
    "v_exp_f32 v130, v130\n"
    "v_exp_f32 v131, v131\n"
    "v_exp_f32 v132, v132\n"
    "v_exp_f32 v133, v133\n"
    "v_exp_f32 v134, v134\n"
    "v_exp_f32 v135, v135\n"
    "v_exp_f32 v136, v136\n"
    "v_exp_f32 v137, v137\n"
    "v_cvt_pk_bf16_f32 v192, v128, v129\n"
    "v_cvt_pk_bf16_f32 v193, v130, v131\n"
    "v_cvt_pk_bf16_f32 v200, v132, v133\n"
    "v_cvt_pk_bf16_f32 v201, v134, v135\n"
    "v_cvt_pk_bf16_f32 v208, v136, v137\n"
    "s_waitcnt lgkmcnt(14)\n"
    "v_mfma_f32_16x16x32_bf16 v[128:131], a[80:83], a[0:3], v[144:147]\n"
    "ds_read_b128 a[64:67], v160 offset:16384\n"
    "v_exp_f32 v138, v138\n"
    "v_mfma_f32_16x16x32_bf16 v[128:131], a[84:87], a[4:7], v[128:131]\n"
    "ds_read_b128 a[68:71], v161 offset:16384\n"
    "v_exp_f32 v139, v139\n"
    "v_mfma_f32_16x16x32_bf16 v[128:131], a[88:91], a[8:11], v[128:131]\n"
    "ds_read_b128 a[72:75], v162 offset:16384\n"
    "v_exp_f32 v140, v140\n"
    "v_cvt_pk_bf16_f32 v209, v138, v139\n"
    "v_mfma_f32_16x16x32_bf16 v[128:131], a[92:95], a[12:15], v[128:131]\n"
    "ds_read_b128 a[76:79], v163 offset:16384\n"
    "v_exp_f32 v141, v141\n"
    "v_mfma_f32_16x16x32_bf16 v[132:135], a[80:83], a[16:19], v[148:151]\n"
    "v_cvt_pk_bf16_f32 v216, v140, v141\n"
    "v_exp_f32 v142, v142\n"
    "v_mfma_f32_16x16x32_bf16 v[132:135], a[84:87], a[20:23], v[132:135]\n"
    "v_exp_f32 v143, v143\n"
    "v_mfma_f32_16x16x32_bf16 v[132:135], a[88:91], a[24:27], v[132:135]\n"
    "v_exp_f32 v128, v128\n"
    "v_cvt_pk_bf16_f32 v217, v142, v143\n"
    "v_mfma_f32_16x16x32_bf16 v[132:135], a[92:95], a[28:31], v[132:135]\n"
    "v_exp_f32 v129, v129\n"
    "v_mfma_f32_16x16x32_bf16 v[136:139], a[80:83], a[32:35], v[152:155]\n"
    "s_add_u32 s29, s85, 0\n"
    "v_cvt_pk_bf16_f32 v194, v128, v129\n"
    "v_exp_f32 v130, v130\n"
    "v_mfma_f32_16x16x32_bf16 v[136:139], a[84:87], a[36:39], v[136:139]\n"
    "s_add_u32 m0, s78, 81920\n"
    "v_exp_f32 v131, v131\n"
    "v_mfma_f32_16x16x32_bf16 v[136:139], a[88:91], a[40:43], v[136:139]\n"
    "buffer_load_dwordx4 v37, s[48:51], s29 offen offset:0 lds\n"
    "v_exp_f32 v132, v132\n"
    "v_cvt_pk_bf16_f32 v195, v130, v131\n"
    "v_mfma_f32_16x16x32_bf16 v[136:139], a[92:95], a[44:47], v[136:139]\n"
    "buffer_load_dwordx4 v38, s[48:51], s29 offen offset:1024 lds\n"
    "v_exp_f32 v133, v133\n"
    "v_mfma_f32_16x16x32_bf16 v[140:143], a[80:83], a[48:51], v[156:159]\n"
    "buffer_load_dwordx4 v39, s[48:51], s29 offen offset:2048 lds\n"
    "v_cvt_pk_bf16_f32 v202, v132, v133\n"
    "v_exp_f32 v134, v134\n"
    "v_mfma_f32_16x16x32_bf16 v[140:143], a[84:87], a[52:55], v[140:143]\n"
    "buffer_load_dwordx4 v40, s[48:51], s29 offen offset:3072 lds\n"
    "v_exp_f32 v135, v135\n"
    "v_mfma_f32_16x16x32_bf16 v[140:143], a[88:91], a[56:59], v[140:143]\n"
    "v_exp_f32 v136, v136\n"
    "v_cvt_pk_bf16_f32 v203, v134, v135\n"
    "v_mfma_f32_16x16x32_bf16 v[140:143], a[92:95], a[60:63], v[140:143]\n"
    "v_exp_f32 v137, v137\n"
    "s_waitcnt lgkmcnt(0)\n"
    "v_mfma_f32_16x16x32_bf16 v[128:131], a[64:67], a[0:3], v[144:147]\n"
    "ds_read_b128 a[80:83], v160 offset:20480\n"
    "v_cvt_pk_bf16_f32 v210, v136, v137\n"
    "v_exp_f32 v138, v138\n"
    "v_mfma_f32_16x16x32_bf16 v[128:131], a[68:71], a[4:7], v[128:131]\n"
    "ds_read_b128 a[84:87], v161 offset:20480\n"
    "v_exp_f32 v139, v139\n"
    "v_mfma_f32_16x16x32_bf16 v[128:131], a[72:75], a[8:11], v[128:131]\n"
    "ds_read_b128 a[88:91], v162 offset:20480\n"
    "v_exp_f32 v140, v140\n"
    "v_cvt_pk_bf16_f32 v211, v138, v139\n"
    "v_mfma_f32_16x16x32_bf16 v[128:131], a[76:79], a[12:15], v[128:131]\n"
    "ds_read_b128 a[92:95], v163 offset:20480\n"
    "v_exp_f32 v141, v141\n"
    "v_mfma_f32_16x16x32_bf16 v[132:135], a[64:67], a[16:19], v[148:151]\n"
    "v_cvt_pk_bf16_f32 v218, v140, v141\n"
    "v_exp_f32 v142, v142\n"
    "v_mfma_f32_16x16x32_bf16 v[132:135], a[68:71], a[20:23], v[132:135]\n"
    "v_exp_f32 v143, v143\n"
    "v_mfma_f32_16x16x32_bf16 v[132:135], a[72:75], a[24:27], v[132:135]\n"
    "v_exp_f32 v128, v128\n"
    "v_cvt_pk_bf16_f32 v219, v142, v143\n"
    "v_mfma_f32_16x16x32_bf16 v[132:135], a[76:79], a[28:31], v[132:135]\n"
    "v_exp_f32 v129, v129\n"
    "v_mfma_f32_16x16x32_bf16 v[136:139], a[64:67], a[32:35], v[152:155]\n"
    "s_add_u32 s30, s82, 32768\n"
    "v_cvt_pk_bf16_f32 v196, v128, v129\n"
    "v_exp_f32 v130, v130\n"
    "v_mfma_f32_16x16x32_bf16 v[136:139], a[68:71], a[36:39], v[136:139]\n"
    "s_add_u32 m0, s78, 131072\n"
    "v_exp_f32 v131, v131\n"
    "v_mfma_f32_16x16x32_bf16 v[136:139], a[72:75], a[40:43], v[136:139]\n"
    "buffer_load_dwordx4 v33, s[44:47], s30 offen offset:0 lds\n"
    "v_exp_f32 v132, v132\n"
    "v_cvt_pk_bf16_f32 v197, v130, v131\n"
    "v_mfma_f32_16x16x32_bf16 v[136:139], a[76:79], a[44:47], v[136:139]\n"
    "buffer_load_dwordx4 v34, s[44:47], s30 offen offset:1024 lds\n"
    "v_exp_f32 v133, v133\n"
    "v_mfma_f32_16x16x32_bf16 v[140:143], a[64:67], a[48:51], v[156:159]\n"
    "buffer_load_dwordx4 v35, s[44:47], s30 offen offset:2048 lds\n"
    "v_cvt_pk_bf16_f32 v204, v132, v133\n"
    "v_exp_f32 v134, v134\n"
    "v_mfma_f32_16x16x32_bf16 v[140:143], a[68:71], a[52:55], v[140:143]\n"
    "buffer_load_dwordx4 v36, s[44:47], s30 offen offset:3072 lds\n"
    "v_exp_f32 v135, v135\n"
    "v_mfma_f32_16x16x32_bf16 v[140:143], a[72:75], a[56:59], v[140:143]\n"
    "v_exp_f32 v136, v136\n"
    "v_cvt_pk_bf16_f32 v205, v134, v135\n"
    "v_mfma_f32_16x16x32_bf16 v[140:143], a[76:79], a[60:63], v[140:143]\n"
    "v_exp_f32 v137, v137\n"
    "s_waitcnt vmcnt(12) lgkmcnt(0)\n"
    "s_barrier\n"
    "v_mfma_f32_16x16x32_bf16 v[128:131], a[80:83], a[0:3], v[144:147]\n"
    "ds_read_b128 a[64:67], v160 offset:32768\n"
    "v_cvt_pk_bf16_f32 v212, v136, v137\n"
    "v_exp_f32 v138, v138\n"
    "v_mfma_f32_16x16x32_bf16 v[128:131], a[84:87], a[4:7], v[128:131]\n"
    "ds_read_b128 a[68:71], v161 offset:32768\n"
    "v_exp_f32 v139, v139\n"
    "v_mfma_f32_16x16x32_bf16 v[128:131], a[88:91], a[8:11], v[128:131]\n"
    "ds_read_b128 a[72:75], v162 offset:32768\n"
    "v_exp_f32 v140, v140\n"
    "v_cvt_pk_bf16_f32 v213, v138, v139\n"
    "v_mfma_f32_16x16x32_bf16 v[128:131], a[92:95], a[12:15], v[128:131]\n"
    "ds_read_b128 a[76:79], v163 offset:32768\n"
    "v_exp_f32 v141, v141\n"
    "v_mfma_f32_16x16x32_bf16 v[132:135], a[80:83], a[16:19], v[148:151]\n"
    "ds_read_b64_tr_b16 a[140:141], v172 offset:49152\n"
    "v_cvt_pk_bf16_f32 v220, v140, v141\n"
    "v_exp_f32 v142, v142\n"
    "v_mfma_f32_16x16x32_bf16 v[132:135], a[84:87], a[20:23], v[132:135]\n"
    "ds_read_b64_tr_b16 a[142:143], v172 offset:51200\n"
    "v_exp_f32 v143, v143\n"
    "v_mfma_f32_16x16x32_bf16 v[132:135], a[88:91], a[24:27], v[132:135]\n"
    "ds_read_b64_tr_b16 a[144:145], v172 offset:53248\n"
    "v_exp_f32 v128, v128\n"
    "v_cvt_pk_bf16_f32 v221, v142, v143\n"
    "v_mfma_f32_16x16x32_bf16 v[132:135], a[92:95], a[28:31], v[132:135]\n"
    "ds_read_b64_tr_b16 a[146:147], v172 offset:55296\n"
    "v_exp_f32 v129, v129\n"
    "v_mfma_f32_16x16x32_bf16 v[136:139], a[80:83], a[32:35], v[152:155]\n"
    "ds_read_b64_tr_b16 a[148:149], v173 offset:49152\n"
    "v_cvt_pk_bf16_f32 v198, v128, v129\n"
    "v_exp_f32 v130, v130\n"
    "v_mfma_f32_16x16x32_bf16 v[136:139], a[84:87], a[36:39], v[136:139]\n"
    "ds_read_b64_tr_b16 a[150:151], v173 offset:51200\n"
    "v_exp_f32 v131, v131\n"
    "v_mfma_f32_16x16x32_bf16 v[136:139], a[88:91], a[40:43], v[136:139]\n"
    "ds_read_b64_tr_b16 a[152:153], v173 offset:53248\n"
    "s_add_u32 s31, s84, 16384\n"
    "v_exp_f32 v132, v132\n"
    "v_cvt_pk_bf16_f32 v199, v130, v131\n"
    "v_mfma_f32_16x16x32_bf16 v[136:139], a[92:95], a[44:47], v[136:139]\n"
    "ds_read_b64_tr_b16 a[154:155], v173 offset:55296\n"
    "s_add_u32 m0, s78, 147456\n"
    "v_exp_f32 v133, v133\n"
    "v_mfma_f32_16x16x32_bf16 v[140:143], a[80:83], a[48:51], v[156:159]\n"
    "buffer_load_dwordx4 v37, s[48:51], s31 offen offset:0 lds\n"
    "v_cvt_pk_bf16_f32 v206, v132, v133\n"
    "v_exp_f32 v134, v134\n"
    "v_mfma_f32_16x16x32_bf16 v[140:143], a[84:87], a[52:55], v[140:143]\n"
    "buffer_load_dwordx4 v38, s[48:51], s31 offen offset:1024 lds\n"
    "v_exp_f32 v135, v135\n"
    "v_mfma_f32_16x16x32_bf16 v[140:143], a[88:91], a[56:59], v[140:143]\n"
    "buffer_load_dwordx4 v39, s[48:51], s31 offen offset:2048 lds\n"
    "v_exp_f32 v136, v136\n"
    "v_cvt_pk_bf16_f32 v207, v134, v135\n"
    "v_mfma_f32_16x16x32_bf16 v[140:143], a[92:95], a[60:63], v[140:143]\n"
    "buffer_load_dwordx4 v40, s[48:51], s31 offen offset:3072 lds\n"
    "v_exp_f32 v137, v137\n"
    "s_waitcnt lgkmcnt(4)\n"
    "v_mfma_f32_16x16x32_bf16 v[128:131], a[64:67], a[0:3], v[144:147]\n"
    "v_cvt_pk_bf16_f32 v214, v136, v137\n"
    "v_exp_f32 v138, v138\n"
    "v_mfma_f32_16x16x32_bf16 v[128:131], a[68:71], a[4:7], v[128:131]\n"
    "ds_read_b128 a[80:83], v160 offset:36864\n"
    "v_exp_f32 v139, v139\n"
    "v_mfma_f32_16x16x32_bf16 v[128:131], a[72:75], a[8:11], v[128:131]\n"
    "v_exp_f32 v140, v140\n"
    "v_mfma_f32_16x16x32_bf16 v[128:131], a[76:79], a[12:15], v[128:131]\n"
    "ds_read_b128 a[84:87], v161 offset:36864\n"
    "v_cvt_pk_bf16_f32 v215, v138, v139\n"
    "v_mfma_f32_16x16x32_bf16 v[64:67], a[140:143], v[192:195], 0\n"
    "v_exp_f32 v141, v141\n"
    "v_mfma_f32_16x16x32_bf16 v[64:67], a[144:147], v[196:199], v[64:67]\n"
    "ds_read_b128 a[88:91], v162 offset:36864\n"
    "v_exp_f32 v142, v142\n"
    "v_mfma_f32_16x16x32_bf16 v[68:71], a[144:147], v[204:207], 0\n"
    "v_exp_f32 v143, v143\n"
    "v_mfma_f32_16x16x32_bf16 v[68:71], a[140:143], v[200:203], v[68:71]\n"
    "ds_read_b128 a[92:95], v163 offset:36864\n"
    "v_cvt_pk_bf16_f32 v222, v140, v141\n"
    "v_cvt_pk_bf16_f32 v223, v142, v143\n"
    "v_mfma_f32_16x16x32_bf16 v[132:135], a[64:67], a[16:19], v[148:151]\n"
    "v_exp_f32 v128, v128\n"
    "v_mfma_f32_16x16x32_bf16 v[132:135], a[68:71], a[20:23], v[132:135]\n"
    "ds_read_b64_tr_b16 a[156:157], v174 offset:49152\n"
    "v_mfma_f32_16x16x32_bf16 v[132:135], a[72:75], a[24:27], v[132:135]\n"
    "v_exp_f32 v129, v129\n"
    "v_mfma_f32_16x16x32_bf16 v[132:135], a[76:79], a[28:31], v[132:135]\n"
    "ds_read_b64_tr_b16 a[158:159], v174 offset:51200\n"
    "v_cvt_pk_bf16_f32 v224, v128, v129\n"
    "v_mfma_f32_16x16x32_bf16 a[192:195], a[140:143], v[208:211], 0\n"
    "v_exp_f32 v130, v130\n"
    "v_mfma_f32_16x16x32_bf16 a[192:195], a[144:147], v[212:215], a[192:195]\n"
    "ds_read_b64_tr_b16 a[160:161], v174 offset:53248\n"
    "s_waitcnt lgkmcnt(7)\n"
    "v_mfma_f32_16x16x32_bf16 a[196:199], a[144:147], v[220:223], 0\n"
    "v_exp_f32 v131, v131\n"
    "v_mfma_f32_16x16x32_bf16 a[196:199], a[140:143], v[216:219], a[196:199]\n"
    "ds_read_b64_tr_b16 a[162:163], v174 offset:55296\n"
    "v_cvt_pk_bf16_f32 v225, v130, v131\n"
    "v_mfma_f32_16x16x32_bf16 v[136:139], a[64:67], a[32:35], v[152:155]\n"
    "v_exp_f32 v132, v132\n"
    "v_mfma_f32_16x16x32_bf16 v[136:139], a[68:71], a[36:39], v[136:139]\n"
    "ds_read_b64_tr_b16 a[164:165], v175 offset:49152\n"
    "s_add_u32 s28, s83, 32768\n"
    "v_mfma_f32_16x16x32_bf16 v[136:139], a[72:75], a[40:43], v[136:139]\n"
    "v_exp_f32 v133, v133\n"
    "v_mfma_f32_16x16x32_bf16 v[136:139], a[76:79], a[44:47], v[136:139]\n"
    "ds_read_b64_tr_b16 a[166:167], v175 offset:51200\n"
    "v_cvt_pk_bf16_f32 v232, v132, v133\n"
    "v_mfma_f32_16x16x32_bf16 v[72:75], a[148:151], v[192:195], 0\n"
    "v_exp_f32 v134, v134\n"
    "v_mfma_f32_16x16x32_bf16 v[72:75], a[152:155], v[196:199], v[72:75]\n"
    "ds_read_b64_tr_b16 a[168:169], v175 offset:53248\n"
    "s_add_u32 m0, s78, 98304\n"
    "v_mfma_f32_16x16x32_bf16 v[76:79], a[152:155], v[204:207], 0\n"
    "v_exp_f32 v135, v135\n"
    "v_mfma_f32_16x16x32_bf16 v[76:79], a[148:151], v[200:203], v[76:79]\n"
    "ds_read_b64_tr_b16 a[170:171], v175 offset:55296\n"
    "v_cvt_pk_bf16_f32 v233, v134, v135\n"
    "v_mfma_f32_16x16x32_bf16 v[140:143], a[64:67], a[48:51], v[156:159]\n"
    "v_exp_f32 v136, v136\n"
    "v_mfma_f32_16x16x32_bf16 v[140:143], a[68:71], a[52:55], v[140:143]\n"
    "buffer_load_dwordx4 v33, s[44:47], s28 offen offset:0 lds\n"
    "v_mfma_f32_16x16x32_bf16 v[140:143], a[72:75], a[56:59], v[140:143]\n"
    "v_exp_f32 v137, v137\n"
    "v_mfma_f32_16x16x32_bf16 v[140:143], a[76:79], a[60:63], v[140:143]\n"
    "buffer_load_dwordx4 v34, s[44:47], s28 offen offset:1024 lds\n"
    "v_cvt_pk_bf16_f32 v240, v136, v137\n"
    "v_mfma_f32_16x16x32_bf16 a[200:203], a[148:151], v[208:211], 0\n"
    "v_exp_f32 v138, v138\n"
    "v_mfma_f32_16x16x32_bf16 a[200:203], a[152:155], v[212:215], a[200:203]\n"
    "buffer_load_dwordx4 v35, s[44:47], s28 offen offset:2048 lds\n"
    "v_mfma_f32_16x16x32_bf16 a[204:207], a[152:155], v[220:223], 0\n"
    "v_exp_f32 v139, v139\n"
    "v_mfma_f32_16x16x32_bf16 a[204:207], a[148:151], v[216:219], a[204:207]\n"
    "buffer_load_dwordx4 v36, s[44:47], s28 offen offset:3072 lds\n"
    "v_cvt_pk_bf16_f32 v241, v138, v139\n"
    "v_mfma_f32_16x16x32_bf16 a[172:175], v[60:63], v[192:195], 0\n"
    "v_mfma_f32_16x16x32_bf16 a[172:175], v[60:63], v[196:199], a[172:175]\n"
    "s_waitcnt vmcnt(12) lgkmcnt(0)\n"
    "s_barrier\n"
    "v_mfma_f32_16x16x32_bf16 v[128:131], a[80:83], a[0:3], v[144:147]\n"
    "v_exp_f32 v140, v140\n"
    "v_mfma_f32_16x16x32_bf16 v[128:131], a[84:87], a[4:7], v[128:131]\n"
    "ds_read_b128 a[64:67], v164 offset:0\n"
    "s_lshl_b32 s18, s97, 1\n"
    "v_mfma_f32_16x16x32_bf16 v[128:131], a[88:91], a[8:11], v[128:131]\n"
    "v_exp_f32 v141, v141\n"
    "v_mfma_f32_16x16x32_bf16 v[128:131], a[92:95], a[12:15], v[128:131]\n"
    "ds_read_b128 a[68:71], v165 offset:0\n"
    "v_cvt_pk_bf16_f32 v248, v140, v141\n"
    "v_mfma_f32_16x16x32_bf16 v[80:83], a[156:159], v[192:195], 0\n"
    "v_exp_f32 v142, v142\n"
    "v_mfma_f32_16x16x32_bf16 v[80:83], a[160:163], v[196:199], v[80:83]\n"
    "ds_read_b128 a[72:75], v166 offset:0\n"
    "s_add_u32 s17, s96, s18\n"
    "v_mfma_f32_16x16x32_bf16 v[84:87], a[160:163], v[204:207], 0\n"
    "v_exp_f32 v143, v143\n"
    "v_mfma_f32_16x16x32_bf16 v[84:87], a[156:159], v[200:203], v[84:87]\n"
    "ds_read_b128 a[76:79], v167 offset:0\n"
    "v_cvt_pk_bf16_f32 v249, v142, v143\n"
    "v_mfma_f32_16x16x32_bf16 v[132:135], a[80:83], a[16:19], v[148:151]\n"
    "v_exp_f32 v128, v128\n"
    "v_mfma_f32_16x16x32_bf16 v[132:135], a[84:87], a[20:23], v[132:135]\n"
    "ds_read_b64_tr_b16 a[140:141], v176 offset:16384\n"
    "s_lshl_b32 s18, s23, 2\n"
    "v_mfma_f32_16x16x32_bf16 v[132:135], a[88:91], a[24:27], v[132:135]\n"
    "v_exp_f32 v129, v129\n"
    "v_mfma_f32_16x16x32_bf16 v[132:135], a[92:95], a[28:31], v[132:135]\n"
    "ds_read_b64_tr_b16 a[142:143], v176 offset:18432\n"
    "v_cvt_pk_bf16_f32 v226, v128, v129\n"
    "v_mfma_f32_16x16x32_bf16 a[208:211], a[156:159], v[208:211], 0\n"
    "v_exp_f32 v130, v130\n"
    "v_mfma_f32_16x16x32_bf16 a[208:211], a[160:163], v[212:215], a[208:211]\n"
    "ds_read_b64_tr_b16 a[144:145], v176 offset:20480\n"
    "s_waitcnt lgkmcnt(7)\n"
    "v_mfma_f32_16x16x32_bf16 a[212:215], a[160:163], v[220:223], 0\n"
    "v_exp_f32 v131, v131\n"
    "v_mfma_f32_16x16x32_bf16 a[212:215], a[156:159], v[216:219], a[212:215]\n"
    "ds_read_b64_tr_b16 a[146:147], v176 offset:22528\n"
    "v_cvt_pk_bf16_f32 v227, v130, v131\n"
    "v_mfma_f32_16x16x32_bf16 v[136:139], a[80:83], a[32:35], v[152:155]\n"
    "v_exp_f32 v132, v132\n"
    "v_mfma_f32_16x16x32_bf16 v[136:139], a[84:87], a[36:39], v[136:139]\n"
    "ds_read_b64_tr_b16 a[148:149], v177 offset:16384\n"
    "s_add_u32 s17, s17, s18\n"
    "v_mfma_f32_16x16x32_bf16 v[136:139], a[88:91], a[40:43], v[136:139]\n"
    "v_exp_f32 v133, v133\n"
    "v_mfma_f32_16x16x32_bf16 v[136:139], a[92:95], a[44:47], v[136:139]\n"
    "ds_read_b64_tr_b16 a[150:151], v177 offset:18432\n"
    "v_cvt_pk_bf16_f32 v234, v132, v133\n"
    "v_mfma_f32_16x16x32_bf16 v[88:91], a[164:167], v[192:195], 0\n"
    "v_exp_f32 v134, v134\n"
    "v_mfma_f32_16x16x32_bf16 v[88:91], a[168:171], v[196:199], v[88:91]\n"
    "ds_read_b64_tr_b16 a[152:153], v177 offset:20480\n"
    "s_lshl_b32 s17, s17, 15\n"
    "v_mfma_f32_16x16x32_bf16 v[92:95], a[168:171], v[204:207], 0\n"
    "v_exp_f32 v135, v135\n"
    "v_mfma_f32_16x16x32_bf16 v[92:95], a[164:167], v[200:203], v[92:95]\n"
    "ds_read_b64_tr_b16 a[154:155], v177 offset:22528\n"
    "v_cvt_pk_bf16_f32 v235, v134, v135\n"
    "v_mfma_f32_16x16x32_bf16 v[140:143], a[80:83], a[48:51], v[156:159]\n"
    "v_exp_f32 v136, v136\n"
    "v_mfma_f32_16x16x32_bf16 v[140:143], a[84:87], a[52:55], v[140:143]\n"
    "s_lshl_b32 s18, s95, 12\n"
    "s_add_u32 s17, s17, s18\n"
    "v_mfma_f32_16x16x32_bf16 v[140:143], a[88:91], a[56:59], v[140:143]\n"
    "v_exp_f32 v137, v137\n"
    "v_mfma_f32_16x16x32_bf16 v[140:143], a[92:95], a[60:63], v[140:143]\n"
    "v_cvt_pk_bf16_f32 v242, v136, v137\n"
    "s_add_u32 s17, s17, s92\n"
    "v_mfma_f32_16x16x32_bf16 a[216:219], a[164:167], v[208:211], 0\n"
    "v_exp_f32 v138, v138\n"
    "v_mfma_f32_16x16x32_bf16 a[216:219], a[168:171], v[212:215], a[216:219]\n"
    "s_add_u32 s17, s17, 2048\n"
    "s_lshl_b32 s18, s82, 1\n"
    "v_mfma_f32_16x16x32_bf16 a[220:223], a[168:171], v[220:223], 0\n"
    "v_exp_f32 v139, v139\n"
    "v_mfma_f32_16x16x32_bf16 a[220:223], a[164:167], v[216:219], a[220:223]\n"
    "v_cvt_pk_bf16_f32 v243, v138, v139\n"
    "s_add_u32 s64, s17, s18\n"
    "v_mfma_f32_16x16x32_bf16 a[176:179], v[60:63], v[200:203], 0\n"
    "s_xor_b32 s18, s18, 16384\n"
    "s_add_u32 s65, s17, s18\n"
    "v_mfma_f32_16x16x32_bf16 a[176:179], v[60:63], v[204:207], a[176:179]\n"
    "s_waitcnt lgkmcnt(4)\n"
    "s_lshl_b32 s21, s13, 12\n"
    "v_mfma_f32_16x16x32_bf16 v[128:131], a[64:67], a[0:3], v[144:147]\n"
    "v_exp_f32 v140, v140\n"
    "v_mfma_f32_16x16x32_bf16 v[128:131], a[68:71], a[4:7], v[128:131]\n"
    "ds_read_b128 a[80:83], v164 offset:4096\n"
    "s_add_u32 s21, s21, s8\n"
    "v_mfma_f32_16x16x32_bf16 v[128:131], a[72:75], a[8:11], v[128:131]\n"
    "v_exp_f32 v141, v141\n"
    "v_mfma_f32_16x16x32_bf16 v[128:131], a[76:79], a[12:15], v[128:131]\n"
    "ds_read_b128 a[84:87], v165 offset:4096\n"
    "v_cvt_pk_bf16_f32 v250, v140, v141\n"
    "s_nop 1\n"
    "v_mfma_f32_16x16x32_bf16 v[96:99], a[140:143], v[192:195], 0\n"
    "v_exp_f32 v142, v142\n"
    "v_mfma_f32_16x16x32_bf16 v[96:99], a[144:147], v[196:199], v[96:99]\n"
    "ds_read_b128 a[88:91], v166 offset:4096\n"
    "s_add_u32 s21, s21, 1900544\n"
    "v_mfma_f32_16x16x32_bf16 v[100:103], a[144:147], v[204:207], 0\n"
    "v_exp_f32 v143, v143\n"
    "v_mfma_f32_16x16x32_bf16 v[100:103], a[140:143], v[200:203], v[100:103]\n"
    "ds_read_b128 a[92:95], v167 offset:4096\n"
    "v_cvt_pk_bf16_f32 v251, v142, v143\n"
    "s_nop 1\n"
    "v_mfma_f32_16x16x32_bf16 v[132:135], a[64:67], a[16:19], v[148:151]\n"
    "v_exp_f32 v128, v128\n"
    "v_mfma_f32_16x16x32_bf16 v[132:135], a[68:71], a[20:23], v[132:135]\n"
    "ds_read_b64_tr_b16 a[156:157], v178 offset:16384\n"
    "s_lshl_b32 s22, s11, 7\n"
    "v_mfma_f32_16x16x32_bf16 v[132:135], a[72:75], a[24:27], v[132:135]\n"
    "v_exp_f32 v129, v129\n"
    "v_mfma_f32_16x16x32_bf16 v[132:135], a[76:79], a[28:31], v[132:135]\n"
    "ds_read_b64_tr_b16 a[158:159], v178 offset:18432\n"
    "v_cvt_pk_bf16_f32 v228, v128, v129\n"
    "s_nop 1\n"
    "v_mfma_f32_16x16x32_bf16 a[224:227], a[140:143], v[208:211], 0\n"
    "v_exp_f32 v130, v130\n"
    "v_mfma_f32_16x16x32_bf16 a[224:227], a[144:147], v[212:215], a[224:227]\n"
    "ds_read_b64_tr_b16 a[160:161], v178 offset:20480\n"
    "s_waitcnt lgkmcnt(7)\n"
    "v_mfma_f32_16x16x32_bf16 a[228:231], a[144:147], v[220:223], 0\n"
    "v_exp_f32 v131, v131\n"
    "v_mfma_f32_16x16x32_bf16 a[228:231], a[140:143], v[216:219], a[228:231]\n"
    "ds_read_b64_tr_b16 a[162:163], v178 offset:22528\n"
    "v_cvt_pk_bf16_f32 v229, v130, v131\n"
    "s_nop 1\n"
    "v_mfma_f32_16x16x32_bf16 v[136:139], a[64:67], a[32:35], v[152:155]\n"
    "v_exp_f32 v132, v132\n"
    "v_mfma_f32_16x16x32_bf16 v[136:139], a[68:71], a[36:39], v[136:139]\n"
    "ds_read_b64_tr_b16 a[164:165], v179 offset:16384\n"
    "s_add_u32 s29, s85, 16384\n"
    "v_mfma_f32_16x16x32_bf16 v[136:139], a[72:75], a[40:43], v[136:139]\n"
    "v_exp_f32 v133, v133\n"
    "v_mfma_f32_16x16x32_bf16 v[136:139], a[76:79], a[44:47], v[136:139]\n"
    "ds_read_b64_tr_b16 a[166:167], v179 offset:18432\n"
    "v_cvt_pk_bf16_f32 v236, v132, v133\n"
    "s_nop 1\n"
    "v_mfma_f32_16x16x32_bf16 v[104:107], a[148:151], v[192:195], 0\n"
    "v_exp_f32 v134, v134\n"
    "v_mfma_f32_16x16x32_bf16 v[104:107], a[152:155], v[196:199], v[104:107]\n"
    "ds_read_b64_tr_b16 a[168:169], v179 offset:20480\n"
    "s_add_u32 m0, s78, 114688\n"
    "v_mfma_f32_16x16x32_bf16 v[108:111], a[152:155], v[204:207], 0\n"
    "v_exp_f32 v135, v135\n"
    "v_mfma_f32_16x16x32_bf16 v[108:111], a[148:151], v[200:203], v[108:111]\n"
    "ds_read_b64_tr_b16 a[170:171], v179 offset:22528\n"
    "v_cvt_pk_bf16_f32 v237, v134, v135\n"
    "s_nop 1\n"
    "v_mfma_f32_16x16x32_bf16 v[140:143], a[64:67], a[48:51], v[156:159]\n"
    "v_exp_f32 v136, v136\n"
    "v_mfma_f32_16x16x32_bf16 v[140:143], a[68:71], a[52:55], v[140:143]\n"
    "buffer_load_dwordx4 v37, s[48:51], s29 offen offset:0 lds\n"
    "s_add_u32 s21, s21, s22\n"
    "v_mfma_f32_16x16x32_bf16 v[140:143], a[72:75], a[56:59], v[140:143]\n"
    "v_exp_f32 v137, v137\n"
    "v_mfma_f32_16x16x32_bf16 v[140:143], a[76:79], a[60:63], v[140:143]\n"
    "buffer_load_dwordx4 v38, s[48:51], s29 offen offset:1024 lds\n"
    "v_cvt_pk_bf16_f32 v244, v136, v137\n"
    "s_nop 1\n"
    "v_mfma_f32_16x16x32_bf16 a[232:235], a[148:151], v[208:211], 0\n"
    "v_exp_f32 v138, v138\n"
    "v_mfma_f32_16x16x32_bf16 a[232:235], a[152:155], v[212:215], a[232:235]\n"
    "buffer_load_dwordx4 v39, s[48:51], s29 offen offset:2048 lds\n"
    "v_and_b32 v4, 7, v1\n"
    "v_mfma_f32_16x16x32_bf16 a[236:239], a[152:155], v[220:223], 0\n"
    "v_exp_f32 v139, v139\n"
    "v_mfma_f32_16x16x32_bf16 a[236:239], a[148:151], v[216:219], a[236:239]\n"
    "buffer_load_dwordx4 v40, s[48:51], s29 offen offset:3072 lds\n"
    "v_cvt_pk_bf16_f32 v245, v138, v139\n"
    "s_nop 1\n"
    "v_mfma_f32_16x16x32_bf16 a[180:183], v[60:63], v[208:211], 0\n"
    "v_lshlrev_b32 v4, 9, v4\n"
    "v_add_u32 v43, s21, v4\n"
    "v_mfma_f32_16x16x32_bf16 a[180:183], v[60:63], v[212:215], a[180:183]\n"
    "s_waitcnt vmcnt(8) lgkmcnt(0)\n"
    "s_barrier\n"
    "v_mfma_f32_16x16x32_bf16 v[128:131], a[80:83], a[0:3], v[144:147]\n"
    "v_exp_f32 v140, v140\n"
    "v_mfma_f32_16x16x32_bf16 v[128:131], a[84:87], a[4:7], v[128:131]\n"
    "ds_read_b128 a[64:67], v168 offset:0\n"
    "s_lshl_b32 s22, s14, 9\n"
    "s_nop 1\n"
    "v_mfma_f32_16x16x32_bf16 v[128:131], a[88:91], a[8:11], v[128:131]\n"
    "v_exp_f32 v141, v141\n"
    "v_mfma_f32_16x16x32_bf16 v[128:131], a[92:95], a[12:15], v[128:131]\n"
    "ds_read_b128 a[68:71], v169 offset:0\n"
    "v_cvt_pk_bf16_f32 v252, v140, v141\n"
    "v_mfma_f32_16x16x32_bf16 v[112:115], a[156:159], v[192:195], 0\n"
    "v_exp_f32 v142, v142\n"
    "v_mfma_f32_16x16x32_bf16 v[112:115], a[160:163], v[196:199], v[112:115]\n"
    "ds_read_b128 a[72:75], v170 offset:0\n"
    "s_add_u32 s21, s21, s22\n"
    "s_nop 1\n"
    "v_mfma_f32_16x16x32_bf16 v[116:119], a[160:163], v[204:207], 0\n"
    "v_exp_f32 v143, v143\n"
    "v_mfma_f32_16x16x32_bf16 v[116:119], a[156:159], v[200:203], v[116:119]\n"
    "ds_read_b128 a[76:79], v171 offset:0\n"
    "v_cvt_pk_bf16_f32 v253, v142, v143\n"
    "v_mfma_f32_16x16x32_bf16 v[132:135], a[80:83], a[16:19], v[148:151]\n"
    "v_exp_f32 v128, v128\n"
    "v_mfma_f32_16x16x32_bf16 v[132:135], a[84:87], a[20:23], v[132:135]\n"
    "ds_read_b64_tr_b16 a[140:141], v180 offset:16384\n"
    "v_mov_b32 v42, s21\n"
    "s_nop 1\n"
    "v_mfma_f32_16x16x32_bf16 v[132:135], a[88:91], a[24:27], v[132:135]\n"
    "v_exp_f32 v129, v129\n"
    "v_mfma_f32_16x16x32_bf16 v[132:135], a[92:95], a[28:31], v[132:135]\n"
    "ds_read_b64_tr_b16 a[142:143], v180 offset:18432\n"
    "v_cvt_pk_bf16_f32 v230, v128, v129\n"
    "v_mfma_f32_16x16x32_bf16 a[240:243], a[156:159], v[208:211], 0\n"
    "v_exp_f32 v130, v130\n"
    "v_mfma_f32_16x16x32_bf16 a[240:243], a[160:163], v[212:215], a[240:243]\n"
    "ds_read_b64_tr_b16 a[144:145], v180 offset:20480\n"
    "s_waitcnt lgkmcnt(7)\n"
    "s_nop 1\n"
    "v_mfma_f32_16x16x32_bf16 a[244:247], a[160:163], v[220:223], 0\n"
    "v_exp_f32 v131, v131\n"
    "v_mfma_f32_16x16x32_bf16 a[244:247], a[156:159], v[216:219], a[244:247]\n"
    "ds_read_b64_tr_b16 a[146:147], v180 offset:22528\n"
    "v_cvt_pk_bf16_f32 v231, v130, v131\n"
    "v_mfma_f32_16x16x32_bf16 v[136:139], a[80:83], a[32:35], v[152:155]\n"
    "v_exp_f32 v132, v132\n"
    "v_mfma_f32_16x16x32_bf16 v[136:139], a[84:87], a[36:39], v[136:139]\n"
    "ds_read_b64_tr_b16 a[148:149], v181 offset:16384\n"
    "s_add_u32 s30, s82, 49152\n"
    "s_nop 1\n"
    "v_mfma_f32_16x16x32_bf16 v[136:139], a[88:91], a[40:43], v[136:139]\n"
    "v_exp_f32 v133, v133\n"
    "v_mfma_f32_16x16x32_bf16 v[136:139], a[92:95], a[44:47], v[136:139]\n"
    "ds_read_b64_tr_b16 a[150:151], v181 offset:18432\n"
    "v_cvt_pk_bf16_f32 v238, v132, v133\n"
    "v_mfma_f32_16x16x32_bf16 v[120:123], a[164:167], v[192:195], 0\n"
    "v_exp_f32 v134, v134\n"
    "v_mfma_f32_16x16x32_bf16 v[120:123], a[168:171], v[196:199], v[120:123]\n"
    "ds_read_b64_tr_b16 a[152:153], v181 offset:20480\n"
    "s_add_u32 m0, s78, 0\n"
    "s_nop 1\n"
    "v_mfma_f32_16x16x32_bf16 v[124:127], a[168:171], v[204:207], 0\n"
    "v_exp_f32 v135, v135\n"
    "v_mfma_f32_16x16x32_bf16 v[124:127], a[164:167], v[200:203], v[124:127]\n"
    "ds_read_b64_tr_b16 a[154:155], v181 offset:22528\n"
    "v_cvt_pk_bf16_f32 v239, v134, v135\n"
    "v_mfma_f32_16x16x32_bf16 v[140:143], a[80:83], a[48:51], v[156:159]\n"
    "v_exp_f32 v136, v136\n"
    "v_mfma_f32_16x16x32_bf16 v[140:143], a[84:87], a[52:55], v[140:143]\n"
    "buffer_load_dwordx4 v33, s[44:47], s30 offen offset:0 lds\n"
    "v_and_b32 v6, 7, v1\n"
    "s_nop 1\n"
    "v_mfma_f32_16x16x32_bf16 v[140:143], a[88:91], a[56:59], v[140:143]\n"
    "v_exp_f32 v137, v137\n"
    "v_mfma_f32_16x16x32_bf16 v[140:143], a[92:95], a[60:63], v[140:143]\n"
    "buffer_load_dwordx4 v34, s[44:47], s30 offen offset:1024 lds\n"
    "v_cvt_pk_bf16_f32 v246, v136, v137\n"
    "v_mfma_f32_16x16x32_bf16 a[248:251], a[164:167], v[208:211], 0\n"
    "v_exp_f32 v138, v138\n"
    "v_mfma_f32_16x16x32_bf16 a[248:251], a[168:171], v[212:215], a[248:251]\n"
    "buffer_load_dwordx4 v35, s[44:47], s30 offen offset:2048 lds\n"
    "v_lshl_add_u32 v42, v6, 4, v42\n"
    "s_nop 1\n"
    "v_mfma_f32_16x16x32_bf16 a[252:255], a[168:171], v[220:223], 0\n"
    "v_exp_f32 v139, v139\n"
    "v_mfma_f32_16x16x32_bf16 a[252:255], a[164:167], v[216:219], a[252:255]\n"
    "buffer_load_dwordx4 v36, s[44:47], s30 offen offset:3072 lds\n"
    "v_cvt_pk_bf16_f32 v247, v138, v139\n"
    "v_mfma_f32_16x16x32_bf16 a[184:187], v[60:63], v[216:219], 0\n"
    "v_sub_u32 v6, 7, v1\n"
    "v_ashrrev_i32 v6, 31, v6\n"
    "v_mfma_f32_16x16x32_bf16 a[184:187], v[60:63], v[220:223], a[184:187]\n"
    "s_waitcnt lgkmcnt(4)\n"
    "v_lshl_or_b32 v42, v6, 8, v42\n"
    "s_nop 1\n"
    "v_mfma_f32_16x16x32_bf16 v[128:131], a[64:67], a[0:3], v[144:147]\n"
    "v_exp_f32 v140, v140\n"
    "v_mfma_f32_16x16x32_bf16 v[128:131], a[68:71], a[4:7], v[128:131]\n"
    "ds_read_b128 a[80:83], v168 offset:4096\n"
    "v_mov_b32 v27, v41\n"
    "v_mfma_f32_16x16x32_bf16 v[128:131], a[72:75], a[8:11], v[128:131]\n"
    "v_exp_f32 v141, v141\n"
    "v_mfma_f32_16x16x32_bf16 v[128:131], a[76:79], a[12:15], v[128:131]\n"
    "Lw5_%=:\n"
    "s_cbranch_execz Lret0_%=\n"
    "ds_read_b128 a[84:87], v169 offset:4096\n"
    "v_cvt_pk_bf16_f32 v254, v140, v141\n"
    "s_nop 1\n"
    "v_mfma_f32_16x16x32_bf16 v[64:67], a[140:143], v[224:227], v[64:67]\n"
    "v_exp_f32 v142, v142\n"
    "v_mfma_f32_16x16x32_bf16 v[64:67], a[144:147], v[228:231], v[64:67]\n"
    "ds_read_b128 a[88:91], v170 offset:4096\n"
    "v_add_u32 v29, 65536, v41\n"
    "v_mfma_f32_16x16x32_bf16 v[68:71], a[144:147], v[236:239], v[68:71]\n"
    "v_exp_f32 v143, v143\n"
    "v_mfma_f32_16x16x32_bf16 v[68:71], a[140:143], v[232:235], v[68:71]\n"
    "ds_read_b128 a[92:95], v171 offset:4096\n"
    "v_cvt_pk_bf16_f32 v255, v142, v143\n"
    "s_nop 1\n"
    "v_mfma_f32_16x16x32_bf16 v[132:135], a[64:67], a[16:19], v[148:151]\n"
    "v_exp_f32 v128, v128\n"
    "v_mfma_f32_16x16x32_bf16 v[132:135], a[68:71], a[20:23], v[132:135]\n"
    "ds_read_b64_tr_b16 a[156:157], v182 offset:16384\n"
    "v_add_u32 v31, 131072, v41\n"
    "v_mfma_f32_16x16x32_bf16 v[132:135], a[72:75], a[24:27], v[132:135]\n"
    "v_exp_f32 v129, v129\n"
    "v_mfma_f32_16x16x32_bf16 v[132:135], a[76:79], a[28:31], v[132:135]\n"
    "ds_read_b64_tr_b16 a[158:159], v182 offset:18432\n"
    "v_cvt_pk_bf16_f32 v192, v128, v129\n"
    "s_nop 1\n"
    "v_mfma_f32_16x16x32_bf16 a[192:195], a[140:143], v[240:243], a[192:195]\n"
    "v_exp_f32 v130, v130\n"
    "v_mfma_f32_16x16x32_bf16 a[192:195], a[144:147], v[244:247], a[192:195]\n"
    "ds_read_b64_tr_b16 a[160:161], v182 offset:20480\n"
    "s_waitcnt lgkmcnt(7)\n"
    "v_mfma_f32_16x16x32_bf16 a[196:199], a[144:147], v[252:255], a[196:199]\n"
    "v_exp_f32 v131, v131\n"
    "v_mfma_f32_16x16x32_bf16 a[196:199], a[140:143], v[248:251], a[196:199]\n"
    "ds_read_b64_tr_b16 a[162:163], v182 offset:22528\n"
    "v_cvt_pk_bf16_f32 v193, v130, v131\n"
    "s_nop 1\n"
    "v_mfma_f32_16x16x32_bf16 v[136:139], a[64:67], a[32:35], v[152:155]\n"
    "v_exp_f32 v132, v132\n"
    "v_mfma_f32_16x16x32_bf16 v[136:139], a[68:71], a[36:39], v[136:139]\n"
    "ds_read_b64_tr_b16 a[164:165], v183 offset:16384\n"
    "s_add_u32 s31, s84, 32768\n"
    "v_mfma_f32_16x16x32_bf16 v[136:139], a[72:75], a[40:43], v[136:139]\n"
    "v_exp_f32 v133, v133\n"
    "v_mfma_f32_16x16x32_bf16 v[136:139], a[76:79], a[44:47], v[136:139]\n"
    "ds_read_b64_tr_b16 a[166:167], v183 offset:18432\n"
    "v_cvt_pk_bf16_f32 v200, v132, v133\n"
    "s_nop 1\n"
    "v_mfma_f32_16x16x32_bf16 v[72:75], a[148:151], v[224:227], v[72:75]\n"
    "v_exp_f32 v134, v134\n"
    "v_mfma_f32_16x16x32_bf16 v[72:75], a[152:155], v[228:231], v[72:75]\n"
    "ds_read_b64_tr_b16 a[168:169], v183 offset:20480\n"
    "s_add_u32 m0, s78, 16384\n"
    "v_mfma_f32_16x16x32_bf16 v[76:79], a[152:155], v[236:239], v[76:79]\n"
    "v_exp_f32 v135, v135\n"
    "v_mfma_f32_16x16x32_bf16 v[76:79], a[148:151], v[232:235], v[76:79]\n"
    "ds_read_b64_tr_b16 a[170:171], v183 offset:22528\n"
    "v_cvt_pk_bf16_f32 v201, v134, v135\n"
    "s_nop 1\n"
    "v_mfma_f32_16x16x32_bf16 v[140:143], a[64:67], a[48:51], v[156:159]\n"
    "v_exp_f32 v136, v136\n"
    "v_mfma_f32_16x16x32_bf16 v[140:143], a[68:71], a[52:55], v[140:143]\n"
    "buffer_load_dwordx4 v37, s[48:51], s31 offen offset:0 lds\n"
    "v_mov_b32 v56, s24\n"
    "v_mfma_f32_16x16x32_bf16 v[140:143], a[72:75], a[56:59], v[140:143]\n"
    "v_exp_f32 v137, v137\n"
    "v_mfma_f32_16x16x32_bf16 v[140:143], a[76:79], a[60:63], v[140:143]\n"
    "buffer_load_dwordx4 v38, s[48:51], s31 offen offset:1024 lds\n"
    "v_cvt_pk_bf16_f32 v208, v136, v137\n"
    "s_nop 1\n"
    "v_mfma_f32_16x16x32_bf16 a[200:203], a[148:151], v[240:243], a[200:203]\n"
    "v_exp_f32 v138, v138\n"
    "v_mfma_f32_16x16x32_bf16 a[200:203], a[152:155], v[244:247], a[200:203]\n"
    "buffer_load_dwordx4 v39, s[48:51], s31 offen offset:2048 lds\n"
    "v_mov_b32 v57, s25\n"
    "v_mfma_f32_16x16x32_bf16 a[204:207], a[152:155], v[252:255], a[204:207]\n"
    "v_exp_f32 v139, v139\n"
    "v_mfma_f32_16x16x32_bf16 a[204:207], a[148:151], v[248:251], a[204:207]\n"
    "buffer_load_dwordx4 v40, s[48:51], s31 offen offset:3072 lds\n"
    "v_cvt_pk_bf16_f32 v209, v138, v139\n"
    "s_nop 1\n"
    "v_mfma_f32_16x16x32_bf16 a[172:175], v[60:63], v[224:227], a[172:175]\n"
    "v_mov_b32 v58, s24\n"
    "v_mov_b32 v59, s25\n"
    "v_mfma_f32_16x16x32_bf16 a[172:175], v[60:63], v[228:231], a[172:175]\n"
    "s_waitcnt vmcnt(8) lgkmcnt(0)\n"
    "s_barrier\n"
    "v_mfma_f32_16x16x32_bf16 v[128:131], a[80:83], a[0:3], v[144:147]\n"
    "v_exp_f32 v140, v140\n"
    "v_mfma_f32_16x16x32_bf16 v[128:131], a[84:87], a[4:7], v[128:131]\n"
    "ds_read_b128 a[64:67], v164 offset:32768\n"
    "s_lshl_b32 s19, s97, 13\n"
    "s_nop 1\n"
    "v_mfma_f32_16x16x32_bf16 v[128:131], a[88:91], a[8:11], v[128:131]\n"
    "v_exp_f32 v141, v141\n"
    "v_mfma_f32_16x16x32_bf16 v[128:131], a[92:95], a[12:15], v[128:131]\n"
    "ds_read_b128 a[68:71], v165 offset:32768\n"
    "v_cvt_pk_bf16_f32 v216, v140, v141\n"
    "v_mfma_f32_16x16x32_bf16 v[80:83], a[156:159], v[224:227], v[80:83]\n"
    "v_exp_f32 v142, v142\n"
    "v_mfma_f32_16x16x32_bf16 v[80:83], a[160:163], v[228:231], v[80:83]\n"
    "ds_read_b128 a[72:75], v166 offset:32768\n"
    "s_mov_b32 s17, 81920\n"
    "s_nop 1\n"
    "v_mfma_f32_16x16x32_bf16 v[84:87], a[160:163], v[236:239], v[84:87]\n"
    "v_exp_f32 v143, v143\n"
    "v_mfma_f32_16x16x32_bf16 v[84:87], a[156:159], v[232:235], v[84:87]\n"
    "ds_read_b128 a[76:79], v167 offset:32768\n"
    "v_cvt_pk_bf16_f32 v217, v142, v143\n"
    "v_mfma_f32_16x16x32_bf16 v[132:135], a[80:83], a[16:19], v[148:151]\n"
    "v_exp_f32 v128, v128\n"
    "v_mfma_f32_16x16x32_bf16 v[132:135], a[84:87], a[20:23], v[132:135]\n"
    "ds_read_b64_tr_b16 a[140:141], v176 offset:49152\n"
    "s_nop 2\n"
    "v_mfma_f32_16x16x32_bf16 v[132:135], a[88:91], a[24:27], v[132:135]\n"
    "v_exp_f32 v129, v129\n"
    "v_mfma_f32_16x16x32_bf16 v[132:135], a[92:95], a[28:31], v[132:135]\n"
    "ds_read_b64_tr_b16 a[142:143], v176 offset:51200\n"
    "v_cvt_pk_bf16_f32 v194, v128, v129\n"
    "v_mfma_f32_16x16x32_bf16 a[208:211], a[156:159], v[240:243], a[208:211]\n"
    "v_exp_f32 v130, v130\n"
    "v_mfma_f32_16x16x32_bf16 a[208:211], a[160:163], v[244:247], a[208:211]\n"
    "ds_read_b64_tr_b16 a[144:145], v176 offset:53248\n"
    "s_waitcnt lgkmcnt(7)\n"
    "s_nop 1\n"
    "v_mfma_f32_16x16x32_bf16 a[212:215], a[160:163], v[252:255], a[212:215]\n"
    "v_exp_f32 v131, v131\n"
    "v_mfma_f32_16x16x32_bf16 a[212:215], a[156:159], v[248:251], a[212:215]\n"
    "ds_read_b64_tr_b16 a[146:147], v176 offset:55296\n"
    "v_cvt_pk_bf16_f32 v195, v130, v131\n"
    "v_mfma_f32_16x16x32_bf16 v[136:139], a[80:83], a[32:35], v[152:155]\n"
    "v_exp_f32 v132, v132\n"
    "v_mfma_f32_16x16x32_bf16 v[136:139], a[84:87], a[36:39], v[136:139]\n"
    "ds_read_b64_tr_b16 a[148:149], v177 offset:49152\n"
    "s_add_u32 s28, s83, 49152\n"
    "s_nop 1\n"
    "v_mfma_f32_16x16x32_bf16 v[136:139], a[88:91], a[40:43], v[136:139]\n"
    "v_exp_f32 v133, v133\n"
    "v_mfma_f32_16x16x32_bf16 v[136:139], a[92:95], a[44:47], v[136:139]\n"
    "ds_read_b64_tr_b16 a[150:151], v177 offset:51200\n"
    "v_cvt_pk_bf16_f32 v202, v132, v133\n"
    "v_mfma_f32_16x16x32_bf16 v[88:91], a[164:167], v[224:227], v[88:91]\n"
    "v_exp_f32 v134, v134\n"
    "v_mfma_f32_16x16x32_bf16 v[88:91], a[168:171], v[228:231], v[88:91]\n"
    "ds_read_b64_tr_b16 a[152:153], v177 offset:53248\n"
    "s_add_u32 m0, s78, 32768\n"
    "s_nop 1\n"
    "v_mfma_f32_16x16x32_bf16 v[92:95], a[168:171], v[236:239], v[92:95]\n"
    "v_exp_f32 v135, v135\n"
    "v_mfma_f32_16x16x32_bf16 v[92:95], a[164:167], v[232:235], v[92:95]\n"
    "ds_read_b64_tr_b16 a[154:155], v177 offset:55296\n"
    "v_cvt_pk_bf16_f32 v203, v134, v135\n"
    "v_mfma_f32_16x16x32_bf16 v[140:143], a[80:83], a[48:51], v[156:159]\n"
    "v_exp_f32 v136, v136\n"
    "v_mfma_f32_16x16x32_bf16 v[140:143], a[84:87], a[52:55], v[140:143]\n"
    "buffer_load_dwordx4 v33, s[44:47], s28 offen offset:0 lds\n"
    "s_nop 2\n"
    "v_mfma_f32_16x16x32_bf16 v[140:143], a[88:91], a[56:59], v[140:143]\n"
    "v_exp_f32 v137, v137\n"
    "v_mfma_f32_16x16x32_bf16 v[140:143], a[92:95], a[60:63], v[140:143]\n"
    "buffer_load_dwordx4 v34, s[44:47], s28 offen offset:1024 lds\n"
    "v_cvt_pk_bf16_f32 v210, v136, v137\n"
    "v_mfma_f32_16x16x32_bf16 a[216:219], a[164:167], v[240:243], a[216:219]\n"
    "v_exp_f32 v138, v138\n"
    "v_mfma_f32_16x16x32_bf16 a[216:219], a[168:171], v[244:247], a[216:219]\n"
    "buffer_load_dwordx4 v35, s[44:47], s28 offen offset:2048 lds\n"
    "s_nop 2\n"
    "v_mfma_f32_16x16x32_bf16 a[220:223], a[168:171], v[252:255], a[220:223]\n"
    "v_exp_f32 v139, v139\n"
    "v_mfma_f32_16x16x32_bf16 a[220:223], a[164:167], v[248:251], a[220:223]\n"
    "buffer_load_dwordx4 v36, s[44:47], s28 offen offset:3072 lds\n"
    "v_cvt_pk_bf16_f32 v211, v138, v139\n"
    "v_mfma_f32_16x16x32_bf16 a[176:179], v[60:63], v[232:235], a[176:179]\n"
    "s_cmp_eq_u32 s96, 0\n"
    "s_cselect_b32 s17, 65536, s17\n"
    "v_mfma_f32_16x16x32_bf16 a[176:179], v[60:63], v[236:239], a[176:179]\n"
    "s_waitcnt lgkmcnt(4)\n"
    "s_add_u32 s17, s17, s19\n"
    "s_nop 1\n"
    "v_mfma_f32_16x16x32_bf16 v[128:131], a[64:67], a[0:3], v[144:147]\n"
    "v_exp_f32 v140, v140\n"
    "v_mfma_f32_16x16x32_bf16 v[128:131], a[68:71], a[4:7], v[128:131]\n"
    "ds_read_b128 a[80:83], v164 offset:36864\n"
    "v_add_u32 v46, s17, v41\n"
    "v_mfma_f32_16x16x32_bf16 v[128:131], a[72:75], a[8:11], v[128:131]\n"
    "v_exp_f32 v141, v141\n"
    "v_mfma_f32_16x16x32_bf16 v[128:131], a[76:79], a[12:15], v[128:131]\n"
    "ds_read_b128 a[84:87], v165 offset:36864\n"
    "v_cvt_pk_bf16_f32 v218, v140, v141\n"
    "s_nop 1\n"
    "v_mfma_f32_16x16x32_bf16 v[96:99], a[140:143], v[224:227], v[96:99]\n"
    "v_exp_f32 v142, v142\n"
    "v_mfma_f32_16x16x32_bf16 v[96:99], a[144:147], v[228:231], v[96:99]\n"
    "ds_read_b128 a[88:91], v166 offset:36864\n"
    "s_mov_b32 s17, 65536\n"
    "v_mfma_f32_16x16x32_bf16 v[100:103], a[144:147], v[236:239], v[100:103]\n"
    "v_exp_f32 v143, v143\n"
    "v_mfma_f32_16x16x32_bf16 v[100:103], a[140:143], v[232:235], v[100:103]\n"
    "ds_read_b128 a[92:95], v167 offset:36864\n"
    "v_cvt_pk_bf16_f32 v219, v142, v143\n"
    "s_nop 1\n"
    "v_mfma_f32_16x16x32_bf16 v[132:135], a[64:67], a[16:19], v[148:151]\n"
    "v_exp_f32 v128, v128\n"
    "v_mfma_f32_16x16x32_bf16 v[132:135], a[68:71], a[20:23], v[132:135]\n"
    "ds_read_b64_tr_b16 a[156:157], v178 offset:49152\n"
    "v_mfma_f32_16x16x32_bf16 v[132:135], a[72:75], a[24:27], v[132:135]\n"
    "v_exp_f32 v129, v129\n"
    "v_mfma_f32_16x16x32_bf16 v[132:135], a[76:79], a[28:31], v[132:135]\n"
    "ds_read_b64_tr_b16 a[158:159], v178 offset:51200\n"
    "v_cvt_pk_bf16_f32 v196, v128, v129\n"
    "s_nop 1\n"
    "v_mfma_f32_16x16x32_bf16 a[224:227], a[140:143], v[240:243], a[224:227]\n"
    "v_exp_f32 v130, v130\n"
    "v_mfma_f32_16x16x32_bf16 a[224:227], a[144:147], v[244:247], a[224:227]\n"
    "ds_read_b64_tr_b16 a[160:161], v178 offset:53248\n"
    "s_waitcnt lgkmcnt(7)\n"
    "v_mfma_f32_16x16x32_bf16 a[228:231], a[144:147], v[252:255], a[228:231]\n"
    "v_exp_f32 v131, v131\n"
    "v_mfma_f32_16x16x32_bf16 a[228:231], a[140:143], v[248:251], a[228:231]\n"
    "ds_read_b64_tr_b16 a[162:163], v178 offset:55296\n"
    "v_cvt_pk_bf16_f32 v197, v130, v131\n"
    "s_nop 1\n"
    "v_mfma_f32_16x16x32_bf16 v[136:139], a[64:67], a[32:35], v[152:155]\n"
    "v_exp_f32 v132, v132\n"
    "v_mfma_f32_16x16x32_bf16 v[136:139], a[68:71], a[36:39], v[136:139]\n"
    "ds_read_b64_tr_b16 a[164:165], v179 offset:49152\n"
    "s_add_u32 s29, s85, 32768\n"
    "v_mfma_f32_16x16x32_bf16 v[136:139], a[72:75], a[40:43], v[136:139]\n"
    "v_exp_f32 v133, v133\n"
    "v_mfma_f32_16x16x32_bf16 v[136:139], a[76:79], a[44:47], v[136:139]\n"
    "ds_read_b64_tr_b16 a[166:167], v179 offset:51200\n"
    "v_cvt_pk_bf16_f32 v204, v132, v133\n"
    "s_nop 1\n"
    "v_mfma_f32_16x16x32_bf16 v[104:107], a[148:151], v[224:227], v[104:107]\n"
    "v_exp_f32 v134, v134\n"
    "v_mfma_f32_16x16x32_bf16 v[104:107], a[152:155], v[228:231], v[104:107]\n"
    "ds_read_b64_tr_b16 a[168:169], v179 offset:53248\n"
    "s_add_u32 m0, s78, 49152\n"
    "v_mfma_f32_16x16x32_bf16 v[108:111], a[152:155], v[236:239], v[108:111]\n"
    "v_exp_f32 v135, v135\n"
    "v_mfma_f32_16x16x32_bf16 v[108:111], a[148:151], v[232:235], v[108:111]\n"
    "ds_read_b64_tr_b16 a[170:171], v179 offset:55296\n"
    "v_cvt_pk_bf16_f32 v205, v134, v135\n"
    "s_nop 1\n"
    "v_mfma_f32_16x16x32_bf16 v[140:143], a[64:67], a[48:51], v[156:159]\n"
    "v_exp_f32 v136, v136\n"
    "v_mfma_f32_16x16x32_bf16 v[140:143], a[68:71], a[52:55], v[140:143]\n"
    "buffer_load_dwordx4 v37, s[48:51], s29 offen offset:0 lds\n"
    "v_mfma_f32_16x16x32_bf16 v[140:143], a[72:75], a[56:59], v[140:143]\n"
    "v_exp_f32 v137, v137\n"
    "v_mfma_f32_16x16x32_bf16 v[140:143], a[76:79], a[60:63], v[140:143]\n"
    "buffer_load_dwordx4 v38, s[48:51], s29 offen offset:1024 lds\n"
    "v_cvt_pk_bf16_f32 v212, v136, v137\n"
    "s_nop 1\n"
    "v_mfma_f32_16x16x32_bf16 a[232:235], a[148:151], v[240:243], a[232:235]\n"
    "v_exp_f32 v138, v138\n"
    "v_mfma_f32_16x16x32_bf16 a[232:235], a[152:155], v[244:247], a[232:235]\n"
    "buffer_load_dwordx4 v39, s[48:51], s29 offen offset:2048 lds\n"
    "v_mfma_f32_16x16x32_bf16 a[236:239], a[152:155], v[252:255], a[236:239]\n"
    "v_exp_f32 v139, v139\n"
    "v_mfma_f32_16x16x32_bf16 a[236:239], a[148:151], v[248:251], a[236:239]\n"
    "buffer_load_dwordx4 v40, s[48:51], s29 offen offset:3072 lds\n"
    "v_cvt_pk_bf16_f32 v213, v138, v139\n"
    "s_nop 1\n"
    "v_mfma_f32_16x16x32_bf16 a[180:183], v[60:63], v[240:243], a[180:183]\n"
    "s_cmp_eq_u32 s96, 0\n"
    "s_cselect_b32 s17, 81920, s17\n"
    "v_mfma_f32_16x16x32_bf16 a[180:183], v[60:63], v[244:247], a[180:183]\n"
    "s_waitcnt vmcnt(8) lgkmcnt(0)\n"
    "s_barrier\n"
    "v_mfma_f32_16x16x32_bf16 v[128:131], a[80:83], a[0:3], v[144:147]\n"
    "v_exp_f32 v140, v140\n"
    "v_mfma_f32_16x16x32_bf16 v[128:131], a[84:87], a[4:7], v[128:131]\n"
    "ds_read_b128 a[64:67], v160 offset:0\n"
    "s_add_u32 s17, s17, s19\n"
    "s_nop 1\n"
    "v_mfma_f32_16x16x32_bf16 v[128:131], a[88:91], a[8:11], v[128:131]\n"
    "v_exp_f32 v141, v141\n"
    "v_mfma_f32_16x16x32_bf16 v[128:131], a[92:95], a[12:15], v[128:131]\n"
    "ds_read_b128 a[68:71], v161 offset:0\n"
    "v_cvt_pk_bf16_f32 v220, v140, v141\n"
    "v_mfma_f32_16x16x32_bf16 v[112:115], a[156:159], v[224:227], v[112:115]\n"
    "v_exp_f32 v142, v142\n"
    "v_mfma_f32_16x16x32_bf16 v[112:115], a[160:163], v[228:231], v[112:115]\n"
    "ds_read_b128 a[72:75], v162 offset:0\n"
    "v_add_u32 v47, s17, v41\n"
    "s_nop 1\n"
    "v_mfma_f32_16x16x32_bf16 v[116:119], a[160:163], v[236:239], v[116:119]\n"
    "v_exp_f32 v143, v143\n"
    "v_mfma_f32_16x16x32_bf16 v[116:119], a[156:159], v[232:235], v[116:119]\n"
    "ds_read_b128 a[76:79], v163 offset:0\n"
    "v_cvt_pk_bf16_f32 v221, v142, v143\n"
    "v_mfma_f32_16x16x32_bf16 v[132:135], a[80:83], a[16:19], v[148:151]\n"
    "v_exp_f32 v128, v128\n"
    "v_mfma_f32_16x16x32_bf16 v[132:135], a[84:87], a[20:23], v[132:135]\n"
    "ds_read_b64_tr_b16 a[140:141], v172 offset:16384\n"
    "s_mov_b32 s17, 0\n"
    "s_nop 1\n"
    "v_mfma_f32_16x16x32_bf16 v[132:135], a[88:91], a[24:27], v[132:135]\n"
    "v_exp_f32 v129, v129\n"
    "v_mfma_f32_16x16x32_bf16 v[132:135], a[92:95], a[28:31], v[132:135]\n"
    "ds_read_b64_tr_b16 a[142:143], v172 offset:18432\n"
    "v_cvt_pk_bf16_f32 v198, v128, v129\n"
    "v_mfma_f32_16x16x32_bf16 a[240:243], a[156:159], v[240:243], a[240:243]\n"
    "v_exp_f32 v130, v130\n"
    "v_mfma_f32_16x16x32_bf16 a[240:243], a[160:163], v[244:247], a[240:243]\n"
    "ds_read_b64_tr_b16 a[144:145], v172 offset:20480\n"
    "s_waitcnt lgkmcnt(7)\n"
    "s_nop 1\n"
    "v_mfma_f32_16x16x32_bf16 a[244:247], a[160:163], v[252:255], a[244:247]\n"
    "v_exp_f32 v131, v131\n"
    "v_mfma_f32_16x16x32_bf16 a[244:247], a[156:159], v[248:251], a[244:247]\n"
    "ds_read_b64_tr_b16 a[146:147], v172 offset:22528\n"
    "v_cvt_pk_bf16_f32 v199, v130, v131\n"
    "v_mfma_f32_16x16x32_bf16 v[136:139], a[80:83], a[32:35], v[152:155]\n"
    "v_exp_f32 v132, v132\n"
    "v_mfma_f32_16x16x32_bf16 v[136:139], a[84:87], a[36:39], v[136:139]\n"
    "ds_read_b64_tr_b16 a[148:149], v173 offset:16384\n"
    "s_add_u32 s30, s82, 65536\n"
    "s_nop 1\n"
    "v_mfma_f32_16x16x32_bf16 v[136:139], a[88:91], a[40:43], v[136:139]\n"
    "v_exp_f32 v133, v133\n"
    "v_mfma_f32_16x16x32_bf16 v[136:139], a[92:95], a[44:47], v[136:139]\n"
    "ds_read_b64_tr_b16 a[150:151], v173 offset:18432\n"
    "v_cvt_pk_bf16_f32 v206, v132, v133\n"
    "v_mfma_f32_16x16x32_bf16 v[120:123], a[164:167], v[224:227], v[120:123]\n"
    "v_exp_f32 v134, v134\n"
    "v_mfma_f32_16x16x32_bf16 v[120:123], a[168:171], v[228:231], v[120:123]\n"
    "ds_read_b64_tr_b16 a[152:153], v173 offset:20480\n"
    "s_add_u32 m0, s78, 65536\n"
    "s_nop 1\n"
    "v_mfma_f32_16x16x32_bf16 v[124:127], a[168:171], v[236:239], v[124:127]\n"
    "v_exp_f32 v135, v135\n"
    "v_mfma_f32_16x16x32_bf16 v[124:127], a[164:167], v[232:235], v[124:127]\n"
    "ds_read_b64_tr_b16 a[154:155], v173 offset:22528\n"
    "v_cvt_pk_bf16_f32 v207, v134, v135\n"
    "v_mfma_f32_16x16x32_bf16 v[140:143], a[80:83], a[48:51], v[156:159]\n"
    "v_exp_f32 v136, v136\n"
    "v_mfma_f32_16x16x32_bf16 v[140:143], a[84:87], a[52:55], v[140:143]\n"
    "buffer_load_dwordx4 v33, s[44:47], s30 offen offset:0 lds\n"
    "s_nop 2\n"
    "v_mfma_f32_16x16x32_bf16 v[140:143], a[88:91], a[56:59], v[140:143]\n"
    "v_exp_f32 v137, v137\n"
    "v_mfma_f32_16x16x32_bf16 v[140:143], a[92:95], a[60:63], v[140:143]\n"
    "buffer_load_dwordx4 v34, s[44:47], s30 offen offset:1024 lds\n"
    "v_cvt_pk_bf16_f32 v214, v136, v137\n"
    "v_mfma_f32_16x16x32_bf16 a[248:251], a[164:167], v[240:243], a[248:251]\n"
    "v_exp_f32 v138, v138\n"
    "v_mfma_f32_16x16x32_bf16 a[248:251], a[168:171], v[244:247], a[248:251]\n"
    "buffer_load_dwordx4 v35, s[44:47], s30 offen offset:2048 lds\n"
    "s_nop 2\n"
    "v_mfma_f32_16x16x32_bf16 a[252:255], a[168:171], v[252:255], a[252:255]\n"
    "v_exp_f32 v139, v139\n"
    "v_mfma_f32_16x16x32_bf16 a[252:255], a[164:167], v[248:251], a[252:255]\n"
    "buffer_load_dwordx4 v36, s[44:47], s30 offen offset:3072 lds\n"
    "v_cvt_pk_bf16_f32 v215, v138, v139\n"
    "v_mfma_f32_16x16x32_bf16 a[184:187], v[60:63], v[248:251], a[184:187]\n"
    "s_cmp_eq_u32 s96, 0\n"
    "s_cselect_b32 s17, 131072, s17\n"
    "v_mfma_f32_16x16x32_bf16 a[184:187], v[60:63], v[252:255], a[184:187]\n"
    "s_waitcnt lgkmcnt(4)\n"
    "s_add_u32 s17, s17, s19\n"
    "s_nop 1\n"
    "v_mfma_f32_16x16x32_bf16 v[128:131], a[64:67], a[0:3], v[144:147]\n"
    "v_exp_f32 v140, v140\n"
    "v_mfma_f32_16x16x32_bf16 v[128:131], a[68:71], a[4:7], v[128:131]\n"
    "ds_read_b128 a[80:83], v160 offset:4096\n"
    "v_add_u32 v2, s17, v41\n"
    "v_mfma_f32_16x16x32_bf16 v[128:131], a[72:75], a[8:11], v[128:131]\n"
    "v_exp_f32 v141, v141\n"
    "v_mfma_f32_16x16x32_bf16 v[128:131], a[76:79], a[12:15], v[128:131]\n"
    "ds_read_b128 a[84:87], v161 offset:4096\n"
    "v_cvt_pk_bf16_f32 v222, v140, v141\n"
    "s_nop 1\n"
    "v_mfma_f32_16x16x32_bf16 v[64:67], a[140:143], v[192:195], v[64:67]\n"
    "v_exp_f32 v142, v142\n"
    "v_mfma_f32_16x16x32_bf16 v[64:67], a[144:147], v[196:199], v[64:67]\n"
    "ds_read_b128 a[88:91], v162 offset:4096\n"
    "s_mov_b32 s17, 131072\n"
    "v_mfma_f32_16x16x32_bf16 v[68:71], a[144:147], v[204:207], v[68:71]\n"
    "v_exp_f32 v143, v143\n"
    "v_mfma_f32_16x16x32_bf16 v[68:71], a[140:143], v[200:203], v[68:71]\n"
    "ds_read_b128 a[92:95], v163 offset:4096\n"
    "v_cvt_pk_bf16_f32 v223, v142, v143\n"
    "s_nop 1\n"
    "v_mfma_f32_16x16x32_bf16 v[132:135], a[64:67], a[16:19], v[148:151]\n"
    "v_exp_f32 v128, v128\n"
    "v_mfma_f32_16x16x32_bf16 v[132:135], a[68:71], a[20:23], v[132:135]\n"
    "ds_read_b64_tr_b16 a[156:157], v174 offset:16384\n"
    "v_mfma_f32_16x16x32_bf16 v[132:135], a[72:75], a[24:27], v[132:135]\n"
    "v_exp_f32 v129, v129\n"
    "v_mfma_f32_16x16x32_bf16 v[132:135], a[76:79], a[28:31], v[132:135]\n"
    "ds_read_b64_tr_b16 a[158:159], v174 offset:18432\n"
    "v_cvt_pk_bf16_f32 v224, v128, v129\n"
    "s_nop 1\n"
    "v_mfma_f32_16x16x32_bf16 a[192:195], a[140:143], v[208:211], a[192:195]\n"
    "v_exp_f32 v130, v130\n"
    "v_mfma_f32_16x16x32_bf16 a[192:195], a[144:147], v[212:215], a[192:195]\n"
    "ds_read_b64_tr_b16 a[160:161], v174 offset:20480\n"
    "s_waitcnt lgkmcnt(7)\n"
    "v_mfma_f32_16x16x32_bf16 a[196:199], a[144:147], v[220:223], a[196:199]\n"
    "v_exp_f32 v131, v131\n"
    "v_mfma_f32_16x16x32_bf16 a[196:199], a[140:143], v[216:219], a[196:199]\n"
    "ds_read_b64_tr_b16 a[162:163], v174 offset:22528\n"
    "v_cvt_pk_bf16_f32 v225, v130, v131\n"
    "s_nop 1\n"
    "v_mfma_f32_16x16x32_bf16 v[136:139], a[64:67], a[32:35], v[152:155]\n"
    "v_exp_f32 v132, v132\n"
    "v_mfma_f32_16x16x32_bf16 v[136:139], a[68:71], a[36:39], v[136:139]\n"
    "ds_read_b64_tr_b16 a[164:165], v175 offset:16384\n"
    "s_add_u32 s31, s84, 49152\n"
    "v_mfma_f32_16x16x32_bf16 v[136:139], a[72:75], a[40:43], v[136:139]\n"
    "v_exp_f32 v133, v133\n"
    "v_mfma_f32_16x16x32_bf16 v[136:139], a[76:79], a[44:47], v[136:139]\n"
    "ds_read_b64_tr_b16 a[166:167], v175 offset:18432\n"
    "v_cvt_pk_bf16_f32 v232, v132, v133\n"
    "s_nop 1\n"
    "v_mfma_f32_16x16x32_bf16 v[72:75], a[148:151], v[192:195], v[72:75]\n"
    "v_exp_f32 v134, v134\n"
    "v_mfma_f32_16x16x32_bf16 v[72:75], a[152:155], v[196:199], v[72:75]\n"
    "ds_read_b64_tr_b16 a[168:169], v175 offset:20480\n"
    "s_add_u32 m0, s78, 81920\n"
    "v_mfma_f32_16x16x32_bf16 v[76:79], a[152:155], v[204:207], v[76:79]\n"
    "v_exp_f32 v135, v135\n"
    "v_mfma_f32_16x16x32_bf16 v[76:79], a[148:151], v[200:203], v[76:79]\n"
    "ds_read_b64_tr_b16 a[170:171], v175 offset:22528\n"
    "v_cvt_pk_bf16_f32 v233, v134, v135\n"
    "s_nop 1\n"
    "v_mfma_f32_16x16x32_bf16 v[140:143], a[64:67], a[48:51], v[156:159]\n"
    "v_exp_f32 v136, v136\n"
    "v_mfma_f32_16x16x32_bf16 v[140:143], a[68:71], a[52:55], v[140:143]\n"
    "buffer_load_dwordx4 v37, s[48:51], s31 offen offset:0 lds\n"
    "v_mfma_f32_16x16x32_bf16 v[140:143], a[72:75], a[56:59], v[140:143]\n"
    "v_exp_f32 v137, v137\n"
    "v_mfma_f32_16x16x32_bf16 v[140:143], a[76:79], a[60:63], v[140:143]\n"
    "buffer_load_dwordx4 v38, s[48:51], s31 offen offset:1024 lds\n"
    "v_cvt_pk_bf16_f32 v240, v136, v137\n"
    "s_nop 1\n"
    "v_mfma_f32_16x16x32_bf16 a[200:203], a[148:151], v[208:211], a[200:203]\n"
    "v_exp_f32 v138, v138\n"
    "v_mfma_f32_16x16x32_bf16 a[200:203], a[152:155], v[212:215], a[200:203]\n"
    "buffer_load_dwordx4 v39, s[48:51], s31 offen offset:2048 lds\n"
    "v_mfma_f32_16x16x32_bf16 a[204:207], a[152:155], v[220:223], a[204:207]\n"
    "v_exp_f32 v139, v139\n"
    "v_mfma_f32_16x16x32_bf16 a[204:207], a[148:151], v[216:219], a[204:207]\n"
    "buffer_load_dwordx4 v40, s[48:51], s31 offen offset:3072 lds\n"
    "v_cvt_pk_bf16_f32 v241, v138, v139\n"
    "s_nop 1\n"
    "v_mfma_f32_16x16x32_bf16 a[172:175], v[60:63], v[192:195], a[172:175]\n"
    "s_cmp_eq_u32 s96, 0\n"
    "s_cselect_b32 s17, 0, s17\n"
    "v_mfma_f32_16x16x32_bf16 a[172:175], v[60:63], v[196:199], a[172:175]\n"
    "s_waitcnt vmcnt(8) lgkmcnt(0)\n"
    "s_barrier\n"
    "v_mfma_f32_16x16x32_bf16 v[128:131], a[80:83], a[0:3], v[144:147]\n"
    "v_exp_f32 v140, v140\n"
    "v_mfma_f32_16x16x32_bf16 v[128:131], a[84:87], a[4:7], v[128:131]\n"
    "ds_read_b128 a[64:67], v160 offset:32768\n"
    "s_add_u32 s17, s17, s19\n"
    "s_nop 1\n"
    "v_mfma_f32_16x16x32_bf16 v[128:131], a[88:91], a[8:11], v[128:131]\n"
    "v_exp_f32 v141, v141\n"
    "v_mfma_f32_16x16x32_bf16 v[128:131], a[92:95], a[12:15], v[128:131]\n"
    "ds_read_b128 a[68:71], v161 offset:32768\n"
    "v_cvt_pk_bf16_f32 v248, v140, v141\n"
    "v_mfma_f32_16x16x32_bf16 v[80:83], a[156:159], v[192:195], v[80:83]\n"
    "v_exp_f32 v142, v142\n"
    "v_mfma_f32_16x16x32_bf16 v[80:83], a[160:163], v[196:199], v[80:83]\n"
    "ds_read_b128 a[72:75], v162 offset:32768\n"
    "v_add_u32 v3, s17, v41\n"
    "s_nop 1\n"
    "v_mfma_f32_16x16x32_bf16 v[84:87], a[160:163], v[204:207], v[84:87]\n"
    "v_exp_f32 v143, v143\n"
    "v_mfma_f32_16x16x32_bf16 v[84:87], a[156:159], v[200:203], v[84:87]\n"
    "ds_read_b128 a[76:79], v163 offset:32768\n"
    "v_cvt_pk_bf16_f32 v249, v142, v143\n"
    "v_mfma_f32_16x16x32_bf16 v[132:135], a[80:83], a[16:19], v[148:151]\n"
    "v_exp_f32 v128, v128\n"
    "v_mfma_f32_16x16x32_bf16 v[132:135], a[84:87], a[20:23], v[132:135]\n"
    "ds_read_b64_tr_b16 a[140:141], v172 offset:49152\n"
    "s_lshr_b32 s19, s78, 2\n"
    "s_nop 1\n"
    "v_mfma_f32_16x16x32_bf16 v[132:135], a[88:91], a[24:27], v[132:135]\n"
    "v_exp_f32 v129, v129\n"
    "v_mfma_f32_16x16x32_bf16 v[132:135], a[92:95], a[28:31], v[132:135]\n"
    "ds_read_b64_tr_b16 a[142:143], v172 offset:51200\n"
    "v_cvt_pk_bf16_f32 v226, v128, v129\n"
    "v_mfma_f32_16x16x32_bf16 a[208:211], a[156:159], v[208:211], a[208:211]\n"
    "v_exp_f32 v130, v130\n"
    "v_mfma_f32_16x16x32_bf16 a[208:211], a[160:163], v[212:215], a[208:211]\n"
    "ds_read_b64_tr_b16 a[144:145], v172 offset:53248\n"
    "s_waitcnt lgkmcnt(7)\n"
    "s_nop 1\n"
    "v_mfma_f32_16x16x32_bf16 a[212:215], a[160:163], v[220:223], a[212:215]\n"
    "v_exp_f32 v131, v131\n"
    "v_mfma_f32_16x16x32_bf16 a[212:215], a[156:159], v[216:219], a[212:215]\n"
    "ds_read_b64_tr_b16 a[146:147], v172 offset:55296\n"
    "v_cvt_pk_bf16_f32 v227, v130, v131\n"
    "v_mfma_f32_16x16x32_bf16 v[136:139], a[80:83], a[32:35], v[152:155]\n"
    "v_exp_f32 v132, v132\n"
    "v_mfma_f32_16x16x32_bf16 v[136:139], a[84:87], a[36:39], v[136:139]\n"
    "ds_read_b64_tr_b16 a[148:149], v173 offset:49152\n"
    "s_add_u32 s28, s83, 65536\n"
    "s_nop 1\n"
    "v_mfma_f32_16x16x32_bf16 v[136:139], a[88:91], a[40:43], v[136:139]\n"
    "v_exp_f32 v133, v133\n"
    "v_mfma_f32_16x16x32_bf16 v[136:139], a[92:95], a[44:47], v[136:139]\n"
    "ds_read_b64_tr_b16 a[150:151], v173 offset:51200\n"
    "v_cvt_pk_bf16_f32 v234, v132, v133\n"
    "v_mfma_f32_16x16x32_bf16 v[88:91], a[164:167], v[192:195], v[88:91]\n"
    "v_exp_f32 v134, v134\n"
    "v_mfma_f32_16x16x32_bf16 v[88:91], a[168:171], v[196:199], v[88:91]\n"
    "ds_read_b64_tr_b16 a[152:153], v173 offset:53248\n"
    "s_add_u32 m0, s78, 131072\n"
    "s_nop 1\n"
    "v_mfma_f32_16x16x32_bf16 v[92:95], a[168:171], v[204:207], v[92:95]\n"
    "v_exp_f32 v135, v135\n"
    "v_mfma_f32_16x16x32_bf16 v[92:95], a[164:167], v[200:203], v[92:95]\n"
    "ds_read_b64_tr_b16 a[154:155], v173 offset:55296\n"
    "v_cvt_pk_bf16_f32 v235, v134, v135\n"
    "v_mfma_f32_16x16x32_bf16 v[140:143], a[80:83], a[48:51], v[156:159]\n"
    "v_exp_f32 v136, v136\n"
    "v_mfma_f32_16x16x32_bf16 v[140:143], a[84:87], a[52:55], v[140:143]\n"
    "buffer_load_dwordx4 v33, s[44:47], s28 offen offset:0 lds\n"
    "s_add_u32 s19, s19, 147456\n"
    "s_nop 1\n"
    "v_mfma_f32_16x16x32_bf16 v[140:143], a[88:91], a[56:59], v[140:143]\n"
    "v_exp_f32 v137, v137\n"
    "v_mfma_f32_16x16x32_bf16 v[140:143], a[92:95], a[60:63], v[140:143]\n"
    "buffer_load_dwordx4 v34, s[44:47], s28 offen offset:1024 lds\n"
    "v_cvt_pk_bf16_f32 v242, v136, v137\n"
    "v_mfma_f32_16x16x32_bf16 a[216:219], a[164:167], v[208:211], a[216:219]\n"
    "v_exp_f32 v138, v138\n"
    "v_mfma_f32_16x16x32_bf16 a[216:219], a[168:171], v[212:215], a[216:219]\n"
    "buffer_load_dwordx4 v35, s[44:47], s28 offen offset:2048 lds\n"
    "v_add_u32 v48, s19, v41\n"
    "s_nop 1\n"
    "v_mfma_f32_16x16x32_bf16 a[220:223], a[168:171], v[220:223], a[220:223]\n"
    "v_exp_f32 v139, v139\n"
    "v_mfma_f32_16x16x32_bf16 a[220:223], a[164:167], v[216:219], a[220:223]\n"
    "buffer_load_dwordx4 v36, s[44:47], s28 offen offset:3072 lds\n"
    "v_cvt_pk_bf16_f32 v243, v138, v139\n"
    "v_mfma_f32_16x16x32_bf16 a[176:179], v[60:63], v[200:203], a[176:179]\n"
    "v_xor_b32 v44, 2048, v48\n"
    "s_lshl_b32 s19, s11, 2\n"
    "v_mfma_f32_16x16x32_bf16 a[176:179], v[60:63], v[204:207], a[176:179]\n"
    "s_waitcnt lgkmcnt(4)\n"
    "s_add_u32 s19, s19, 151552\n"
    "s_nop 1\n"
    "v_mfma_f32_16x16x32_bf16 v[128:131], a[64:67], a[0:3], v[144:147]\n"
    "v_exp_f32 v140, v140\n"
    "v_mfma_f32_16x16x32_bf16 v[128:131], a[68:71], a[4:7], v[128:131]\n"
    "ds_read_b128 a[80:83], v160 offset:36864\n"
    "v_mov_b32 v45, s19\n"
    "v_mfma_f32_16x16x32_bf16 v[128:131], a[72:75], a[8:11], v[128:131]\n"
    "v_exp_f32 v141, v141\n"
    "v_mfma_f32_16x16x32_bf16 v[128:131], a[76:79], a[12:15], v[128:131]\n"
    "ds_read_b128 a[84:87], v161 offset:36864\n"
    "v_cvt_pk_bf16_f32 v250, v140, v141\n"
    "s_nop 1\n"
    "v_mfma_f32_16x16x32_bf16 v[96:99], a[140:143], v[192:195], v[96:99]\n"
    "v_exp_f32 v142, v142\n"
    "v_mfma_f32_16x16x32_bf16 v[96:99], a[144:147], v[196:199], v[96:99]\n"
    "ds_read_b128 a[88:91], v162 offset:36864\n"
    "v_mov_b32 v49, 151552\n"
    "v_mfma_f32_16x16x32_bf16 v[100:103], a[144:147], v[204:207], v[100:103]\n"
    "v_exp_f32 v143, v143\n"
    "v_mfma_f32_16x16x32_bf16 v[100:103], a[140:143], v[200:203], v[100:103]\n"
    "ds_read_b128 a[92:95], v163 offset:36864\n"
    "v_cvt_pk_bf16_f32 v251, v142, v143\n"
    "s_nop 1\n"
    "v_mfma_f32_16x16x32_bf16 v[132:135], a[64:67], a[16:19], v[148:151]\n"
    "v_exp_f32 v128, v128\n"
    "v_mfma_f32_16x16x32_bf16 v[132:135], a[68:71], a[20:23], v[132:135]\n"
    "ds_read_b64_tr_b16 a[156:157], v174 offset:49152\n"
    "s_mov_b32 s20, 0x53800000\n"
    "v_mfma_f32_16x16x32_bf16 v[132:135], a[72:75], a[24:27], v[132:135]\n"
    "v_exp_f32 v129, v129\n"
    "v_mfma_f32_16x16x32_bf16 v[132:135], a[76:79], a[28:31], v[132:135]\n"
    "ds_read_b64_tr_b16 a[158:159], v174 offset:51200\n"
    "v_cvt_pk_bf16_f32 v228, v128, v129\n"
    "s_nop 1\n"
    "v_mfma_f32_16x16x32_bf16 a[224:227], a[140:143], v[208:211], a[224:227]\n"
    "v_exp_f32 v130, v130\n"
    "v_mfma_f32_16x16x32_bf16 a[224:227], a[144:147], v[212:215], a[224:227]\n"
    "ds_read_b64_tr_b16 a[160:161], v174 offset:53248\n"
    "s_waitcnt lgkmcnt(7)\n"
    "v_mfma_f32_16x16x32_bf16 a[228:231], a[144:147], v[220:223], a[228:231]\n"
    "v_exp_f32 v131, v131\n"
    "v_mfma_f32_16x16x32_bf16 a[228:231], a[140:143], v[216:219], a[228:231]\n"
    "ds_read_b64_tr_b16 a[162:163], v174 offset:55296\n"
    "v_cvt_pk_bf16_f32 v229, v130, v131\n"
    "s_nop 1\n"
    "v_mfma_f32_16x16x32_bf16 v[136:139], a[64:67], a[32:35], v[152:155]\n"
    "v_exp_f32 v132, v132\n"
    "v_mfma_f32_16x16x32_bf16 v[136:139], a[68:71], a[36:39], v[136:139]\n"
    "ds_read_b64_tr_b16 a[164:165], v175 offset:49152\n"
    "s_add_u32 s29, s85, 49152\n"
    "v_mfma_f32_16x16x32_bf16 v[136:139], a[72:75], a[40:43], v[136:139]\n"
    "v_exp_f32 v133, v133\n"
    "v_mfma_f32_16x16x32_bf16 v[136:139], a[76:79], a[44:47], v[136:139]\n"
    "ds_read_b64_tr_b16 a[166:167], v175 offset:51200\n"
    "v_cvt_pk_bf16_f32 v236, v132, v133\n"
    "s_nop 1\n"
    "v_mfma_f32_16x16x32_bf16 v[104:107], a[148:151], v[192:195], v[104:107]\n"
    "v_exp_f32 v134, v134\n"
    "v_mfma_f32_16x16x32_bf16 v[104:107], a[152:155], v[196:199], v[104:107]\n"
    "ds_read_b64_tr_b16 a[168:169], v175 offset:53248\n"
    "s_add_u32 m0, s78, 147456\n"
    "v_mfma_f32_16x16x32_bf16 v[108:111], a[152:155], v[204:207], v[108:111]\n"
    "v_exp_f32 v135, v135\n"
    "v_mfma_f32_16x16x32_bf16 v[108:111], a[148:151], v[200:203], v[108:111]\n"
    "ds_read_b64_tr_b16 a[170:171], v175 offset:55296\n"
    "v_cvt_pk_bf16_f32 v237, v134, v135\n"
    "s_nop 1\n"
    "v_mfma_f32_16x16x32_bf16 v[140:143], a[64:67], a[48:51], v[156:159]\n"
    "v_exp_f32 v136, v136\n"
    "v_mfma_f32_16x16x32_bf16 v[140:143], a[68:71], a[52:55], v[140:143]\n"
    "buffer_load_dwordx4 v37, s[48:51], s29 offen offset:0 lds\n"
    "Lw6_%=:\n"
    "s_cbranch_execz Lw7_%=\n"
    "v_mfma_f32_16x16x32_bf16 v[140:143], a[72:75], a[56:59], v[140:143]\n"
    "v_exp_f32 v137, v137\n"
    "v_mfma_f32_16x16x32_bf16 v[140:143], a[76:79], a[60:63], v[140:143]\n"
    "buffer_load_dwordx4 v38, s[48:51], s29 offen offset:1024 lds\n"
    "v_cvt_pk_bf16_f32 v244, v136, v137\n"
    "s_nop 1\n"
    "v_mfma_f32_16x16x32_bf16 a[232:235], a[148:151], v[208:211], a[232:235]\n"
    "v_exp_f32 v138, v138\n"
    "v_mfma_f32_16x16x32_bf16 a[232:235], a[152:155], v[212:215], a[232:235]\n"
    "buffer_load_dwordx4 v39, s[48:51], s29 offen offset:2048 lds\n"
    "v_mfma_f32_16x16x32_bf16 a[236:239], a[152:155], v[220:223], a[236:239]\n"
    "v_exp_f32 v139, v139\n"
    "v_mfma_f32_16x16x32_bf16 a[236:239], a[148:151], v[216:219], a[236:239]\n"
    "buffer_load_dwordx4 v40, s[48:51], s29 offen offset:3072 lds\n"
    "v_cvt_pk_bf16_f32 v245, v138, v139\n"
    "s_nop 1\n"
    "v_mfma_f32_16x16x32_bf16 a[180:183], v[60:63], v[208:211], a[180:183]\n"
    "v_mfma_f32_16x16x32_bf16 a[180:183], v[60:63], v[212:215], a[180:183]\n"
    "s_waitcnt vmcnt(8) lgkmcnt(0)\n"
    "s_barrier\n"
    "v_mfma_f32_16x16x32_bf16 v[128:131], a[80:83], a[0:3], v[144:147]\n"
    "v_exp_f32 v140, v140\n"
    "v_mfma_f32_16x16x32_bf16 v[128:131], a[84:87], a[4:7], v[128:131]\n"
    "ds_read_b128 a[64:67], v164 offset:0\n"
    "s_nop 2\n"
    "v_mfma_f32_16x16x32_bf16 v[128:131], a[88:91], a[8:11], v[128:131]\n"
    "v_exp_f32 v141, v141\n"
    "v_mfma_f32_16x16x32_bf16 v[128:131], a[92:95], a[12:15], v[128:131]\n"
    "ds_read_b128 a[68:71], v165 offset:0\n"
    "v_cvt_pk_bf16_f32 v252, v140, v141\n"
    "v_mfma_f32_16x16x32_bf16 v[112:115], a[156:159], v[192:195], v[112:115]\n"
    "v_exp_f32 v142, v142\n"
    "v_mfma_f32_16x16x32_bf16 v[112:115], a[160:163], v[196:199], v[112:115]\n"
    "ds_read_b128 a[72:75], v166 offset:0\n"
    "s_nop 2\n"
    "v_mfma_f32_16x16x32_bf16 v[116:119], a[160:163], v[204:207], v[116:119]\n"
    "v_exp_f32 v143, v143\n"
    "v_mfma_f32_16x16x32_bf16 v[116:119], a[156:159], v[200:203], v[116:119]\n"
    "ds_read_b128 a[76:79], v167 offset:0\n"
    "v_cvt_pk_bf16_f32 v253, v142, v143\n"
    "v_mfma_f32_16x16x32_bf16 v[132:135], a[80:83], a[16:19], v[148:151]\n"
    "v_exp_f32 v128, v128\n"
    "v_mfma_f32_16x16x32_bf16 v[132:135], a[84:87], a[20:23], v[132:135]\n"
    "ds_read_b64_tr_b16 a[140:141], v176 offset:16384\n"
    "s_nop 2\n"
    "v_mfma_f32_16x16x32_bf16 v[132:135], a[88:91], a[24:27], v[132:135]\n"
    "v_exp_f32 v129, v129\n"
    "v_mfma_f32_16x16x32_bf16 v[132:135], a[92:95], a[28:31], v[132:135]\n"
    "ds_read_b64_tr_b16 a[142:143], v176 offset:18432\n"
    "v_cvt_pk_bf16_f32 v230, v128, v129\n"
    "v_mfma_f32_16x16x32_bf16 a[240:243], a[156:159], v[208:211], a[240:243]\n"
    "v_exp_f32 v130, v130\n"
    "v_mfma_f32_16x16x32_bf16 a[240:243], a[160:163], v[212:215], a[240:243]\n"
    "ds_read_b64_tr_b16 a[144:145], v176 offset:20480\n"
    "s_waitcnt lgkmcnt(7)\n"
    "s_nop 1\n"
    "v_mfma_f32_16x16x32_bf16 a[244:247], a[160:163], v[220:223], a[244:247]\n"
    "v_exp_f32 v131, v131\n"
    "v_mfma_f32_16x16x32_bf16 a[244:247], a[156:159], v[216:219], a[244:247]\n"
    "ds_read_b64_tr_b16 a[146:147], v176 offset:22528\n"
    "v_cvt_pk_bf16_f32 v231, v130, v131\n"
    "v_mfma_f32_16x16x32_bf16 v[136:139], a[80:83], a[32:35], v[152:155]\n"
    "v_exp_f32 v132, v132\n"
    "v_mfma_f32_16x16x32_bf16 v[136:139], a[84:87], a[36:39], v[136:139]\n"
    "ds_read_b64_tr_b16 a[148:149], v177 offset:16384\n"
    "s_add_u32 s30, s82, 81920\n"
    "s_nop 1\n"
    "v_mfma_f32_16x16x32_bf16 v[136:139], a[88:91], a[40:43], v[136:139]\n"
    "v_exp_f32 v133, v133\n"
    "v_mfma_f32_16x16x32_bf16 v[136:139], a[92:95], a[44:47], v[136:139]\n"
    "ds_read_b64_tr_b16 a[150:151], v177 offset:18432\n"
    "v_cvt_pk_bf16_f32 v238, v132, v133\n"
    "v_mfma_f32_16x16x32_bf16 v[120:123], a[164:167], v[192:195], v[120:123]\n"
    "v_exp_f32 v134, v134\n"
    "v_mfma_f32_16x16x32_bf16 v[120:123], a[168:171], v[196:199], v[120:123]\n"
    "ds_read_b64_tr_b16 a[152:153], v177 offset:20480\n"
    "s_add_u32 m0, s78, 98304\n"
    "s_nop 1\n"
    "v_mfma_f32_16x16x32_bf16 v[124:127], a[168:171], v[204:207], v[124:127]\n"
    "v_exp_f32 v135, v135\n"
    "v_mfma_f32_16x16x32_bf16 v[124:127], a[164:167], v[200:203], v[124:127]\n"
    "ds_read_b64_tr_b16 a[154:155], v177 offset:22528\n"
    "v_cvt_pk_bf16_f32 v239, v134, v135\n"
    "v_mfma_f32_16x16x32_bf16 v[140:143], a[80:83], a[48:51], v[156:159]\n"
    "v_exp_f32 v136, v136\n"
    "v_mfma_f32_16x16x32_bf16 v[140:143], a[84:87], a[52:55], v[140:143]\n"
    "buffer_load_dwordx4 v33, s[44:47], s30 offen offset:0 lds\n"
    "s_nop 2\n"
    "v_mfma_f32_16x16x32_bf16 v[140:143], a[88:91], a[56:59], v[140:143]\n"
    "v_exp_f32 v137, v137\n"
    "v_mfma_f32_16x16x32_bf16 v[140:143], a[92:95], a[60:63], v[140:143]\n"
    "buffer_load_dwordx4 v34, s[44:47], s30 offen offset:1024 lds\n"
    "v_cvt_pk_bf16_f32 v246, v136, v137\n"
    "v_mfma_f32_16x16x32_bf16 a[248:251], a[164:167], v[208:211], a[248:251]\n"
    "v_exp_f32 v138, v138\n"
    "v_mfma_f32_16x16x32_bf16 a[248:251], a[168:171], v[212:215], a[248:251]\n"
    "buffer_load_dwordx4 v35, s[44:47], s30 offen offset:2048 lds\n"
    "s_nop 2\n"
    "v_mfma_f32_16x16x32_bf16 a[252:255], a[168:171], v[220:223], a[252:255]\n"
    "v_exp_f32 v139, v139\n"
    "v_mfma_f32_16x16x32_bf16 a[252:255], a[164:167], v[216:219], a[252:255]\n"
    "buffer_load_dwordx4 v36, s[44:47], s30 offen offset:3072 lds\n"
    "v_cvt_pk_bf16_f32 v247, v138, v139\n"
    "v_mfma_f32_16x16x32_bf16 a[184:187], v[60:63], v[216:219], a[184:187]\n"
    "v_mfma_f32_16x16x32_bf16 a[184:187], v[60:63], v[220:223], a[184:187]\n"
    "s_waitcnt lgkmcnt(4)\n"
    "s_nop 2\n"
    "v_mfma_f32_16x16x32_bf16 v[128:131], a[64:67], a[0:3], v[144:147]\n"
    "v_exp_f32 v140, v140\n"
    "v_mfma_f32_16x16x32_bf16 v[128:131], a[68:71], a[4:7], v[128:131]\n"
    "ds_read_b128 a[80:83], v164 offset:4096\n"
    "v_mfma_f32_16x16x32_bf16 v[128:131], a[72:75], a[8:11], v[128:131]\n"
    "v_exp_f32 v141, v141\n"
    "v_mfma_f32_16x16x32_bf16 v[128:131], a[76:79], a[12:15], v[128:131]\n"
    "ds_read_b128 a[84:87], v165 offset:4096\n"
    "v_cvt_pk_bf16_f32 v254, v140, v141\n"
    "v_mfma_f32_16x16x32_bf16 v[64:67], a[140:143], v[224:227], v[64:67]\n"
    "v_exp_f32 v142, v142\n"
    "v_mfma_f32_16x16x32_bf16 v[64:67], a[144:147], v[228:231], v[64:67]\n"
    "ds_read_b128 a[88:91], v166 offset:4096\n"
    "v_mfma_f32_16x16x32_bf16 v[68:71], a[144:147], v[236:239], v[68:71]\n"
    "v_exp_f32 v143, v143\n"
    "v_mfma_f32_16x16x32_bf16 v[68:71], a[140:143], v[232:235], v[68:71]\n"
    "ds_read_b128 a[92:95], v167 offset:4096\n"
    "v_cvt_pk_bf16_f32 v255, v142, v143\n"
    "v_mfma_f32_16x16x32_bf16 v[132:135], a[64:67], a[16:19], v[148:151]\n"
    "v_exp_f32 v128, v128\n"
    "v_mfma_f32_16x16x32_bf16 v[132:135], a[68:71], a[20:23], v[132:135]\n"
    "ds_read_b64_tr_b16 a[156:157], v178 offset:16384\n"
    "v_mfma_f32_16x16x32_bf16 v[132:135], a[72:75], a[24:27], v[132:135]\n"
    "v_exp_f32 v129, v129\n"
    "v_mfma_f32_16x16x32_bf16 v[132:135], a[76:79], a[28:31], v[132:135]\n"
    "ds_read_b64_tr_b16 a[158:159], v178 offset:18432\n"
    "v_cvt_pk_bf16_f32 v192, v128, v129\n"
    "v_mfma_f32_16x16x32_bf16 a[192:195], a[140:143], v[240:243], a[192:195]\n"
    "v_exp_f32 v130, v130\n"
    "v_mfma_f32_16x16x32_bf16 a[192:195], a[144:147], v[244:247], a[192:195]\n"
    "ds_read_b64_tr_b16 a[160:161], v178 offset:20480\n"
    "s_waitcnt lgkmcnt(7)\n"
    "v_mfma_f32_16x16x32_bf16 a[196:199], a[144:147], v[252:255], a[196:199]\n"
    "v_exp_f32 v131, v131\n"
    "v_mfma_f32_16x16x32_bf16 a[196:199], a[140:143], v[248:251], a[196:199]\n"
    "ds_read_b64_tr_b16 a[162:163], v178 offset:22528\n"
    "v_cvt_pk_bf16_f32 v193, v130, v131\n"
    "v_mfma_f32_16x16x32_bf16 v[136:139], a[64:67], a[32:35], v[152:155]\n"
    "v_exp_f32 v132, v132\n"
    "v_mfma_f32_16x16x32_bf16 v[136:139], a[68:71], a[36:39], v[136:139]\n"
    "ds_read_b64_tr_b16 a[164:165], v179 offset:16384\n"
    "s_add_u32 s31, s84, 65536\n"
    "v_mfma_f32_16x16x32_bf16 v[136:139], a[72:75], a[40:43], v[136:139]\n"
    "v_exp_f32 v133, v133\n"
    "v_mfma_f32_16x16x32_bf16 v[136:139], a[76:79], a[44:47], v[136:139]\n"
    "ds_read_b64_tr_b16 a[166:167], v179 offset:18432\n"
    "v_cvt_pk_bf16_f32 v200, v132, v133\n"
    "v_mfma_f32_16x16x32_bf16 v[72:75], a[148:151], v[224:227], v[72:75]\n"
    "v_exp_f32 v134, v134\n"
    "v_mfma_f32_16x16x32_bf16 v[72:75], a[152:155], v[228:231], v[72:75]\n"
    "ds_read_b64_tr_b16 a[168:169], v179 offset:20480\n"
    "s_add_u32 m0, s78, 114688\n"
    "v_mfma_f32_16x16x32_bf16 v[76:79], a[152:155], v[236:239], v[76:79]\n"
    "v_exp_f32 v135, v135\n"
    "v_mfma_f32_16x16x32_bf16 v[76:79], a[148:151], v[232:235], v[76:79]\n"
    "ds_read_b64_tr_b16 a[170:171], v179 offset:22528\n"
    "v_cvt_pk_bf16_f32 v201, v134, v135\n"
    "v_mfma_f32_16x16x32_bf16 v[140:143], a[64:67], a[48:51], v[156:159]\n"
    "v_exp_f32 v136, v136\n"
    "v_mfma_f32_16x16x32_bf16 v[140:143], a[68:71], a[52:55], v[140:143]\n"
    "buffer_load_dwordx4 v37, s[48:51], s31 offen offset:0 lds\n"
    "v_mfma_f32_16x16x32_bf16 v[140:143], a[72:75], a[56:59], v[140:143]\n"
    "v_exp_f32 v137, v137\n"
    "v_mfma_f32_16x16x32_bf16 v[140:143], a[76:79], a[60:63], v[140:143]\n"
    "buffer_load_dwordx4 v38, s[48:51], s31 offen offset:1024 lds\n"
    "v_cvt_pk_bf16_f32 v208, v136, v137\n"
    "v_mfma_f32_16x16x32_bf16 a[200:203], a[148:151], v[240:243], a[200:203]\n"
    "v_exp_f32 v138, v138\n"
    "v_mfma_f32_16x16x32_bf16 a[200:203], a[152:155], v[244:247], a[200:203]\n"
    "buffer_load_dwordx4 v39, s[48:51], s31 offen offset:2048 lds\n"
    "v_mfma_f32_16x16x32_bf16 a[204:207], a[152:155], v[252:255], a[204:207]\n"
    "v_exp_f32 v139, v139\n"
    "v_mfma_f32_16x16x32_bf16 a[204:207], a[148:151], v[248:251], a[204:207]\n"
    "buffer_load_dwordx4 v40, s[48:51], s31 offen offset:3072 lds\n"
    "v_cvt_pk_bf16_f32 v209, v138, v139\n"
    "v_mfma_f32_16x16x32_bf16 a[172:175], v[60:63], v[224:227], a[172:175]\n"
    "v_mfma_f32_16x16x32_bf16 a[172:175], v[60:63], v[228:231], a[172:175]\n"
    "s_waitcnt vmcnt(8) lgkmcnt(0)\n"
    "s_barrier\n"
    "v_mfma_f32_16x16x32_bf16 v[128:131], a[80:83], a[0:3], v[144:147]\n"
    "v_exp_f32 v140, v140\n"
    "v_mfma_f32_16x16x32_bf16 v[128:131], a[84:87], a[4:7], v[128:131]\n"
    "ds_read_b128 a[64:67], v168 offset:0\n"
    "v_mfma_f32_16x16x32_bf16 v[128:131], a[88:91], a[8:11], v[128:131]\n"
    "v_exp_f32 v141, v141\n"
    "v_mfma_f32_16x16x32_bf16 v[128:131], a[92:95], a[12:15], v[128:131]\n"
    "ds_read_b128 a[68:71], v169 offset:0\n"
    "v_cvt_pk_bf16_f32 v216, v140, v141\n"
    "v_mfma_f32_16x16x32_bf16 v[80:83], a[156:159], v[224:227], v[80:83]\n"
    "v_exp_f32 v142, v142\n"
    "v_mfma_f32_16x16x32_bf16 v[80:83], a[160:163], v[228:231], v[80:83]\n"
    "ds_read_b128 a[72:75], v170 offset:0\n"
    "v_mfma_f32_16x16x32_bf16 v[84:87], a[160:163], v[236:239], v[84:87]\n"
    "v_exp_f32 v143, v143\n"
    "v_mfma_f32_16x16x32_bf16 v[84:87], a[156:159], v[232:235], v[84:87]\n"
    "ds_read_b128 a[76:79], v171 offset:0\n"
    "v_cvt_pk_bf16_f32 v217, v142, v143\n"
    "v_mfma_f32_16x16x32_bf16 v[132:135], a[80:83], a[16:19], v[148:151]\n"
    "v_exp_f32 v128, v128\n"
    "v_mfma_f32_16x16x32_bf16 v[132:135], a[84:87], a[20:23], v[132:135]\n"
    "ds_read_b64_tr_b16 a[140:141], v180 offset:16384\n"
    "v_mfma_f32_16x16x32_bf16 v[132:135], a[88:91], a[24:27], v[132:135]\n"
    "v_exp_f32 v129, v129\n"
    "v_mfma_f32_16x16x32_bf16 v[132:135], a[92:95], a[28:31], v[132:135]\n"
    "ds_read_b64_tr_b16 a[142:143], v180 offset:18432\n"
    "v_cvt_pk_bf16_f32 v194, v128, v129\n"
    "v_mfma_f32_16x16x32_bf16 a[208:211], a[156:159], v[240:243], a[208:211]\n"
    "v_exp_f32 v130, v130\n"
    "v_mfma_f32_16x16x32_bf16 a[208:211], a[160:163], v[244:247], a[208:211]\n"
    "ds_read_b64_tr_b16 a[144:145], v180 offset:20480\n"
    "s_waitcnt lgkmcnt(7)\n"
    "v_mfma_f32_16x16x32_bf16 a[212:215], a[160:163], v[252:255], a[212:215]\n"
    "v_exp_f32 v131, v131\n"
    "v_mfma_f32_16x16x32_bf16 a[212:215], a[156:159], v[248:251], a[212:215]\n"
    "ds_read_b64_tr_b16 a[146:147], v180 offset:22528\n"
    "v_cvt_pk_bf16_f32 v195, v130, v131\n"
    "v_mfma_f32_16x16x32_bf16 v[136:139], a[80:83], a[32:35], v[152:155]\n"
    "v_exp_f32 v132, v132\n"
    "v_mfma_f32_16x16x32_bf16 v[136:139], a[84:87], a[36:39], v[136:139]\n"
    "ds_read_b64_tr_b16 a[148:149], v181 offset:16384\n"
    "s_add_u32 s28, s83, 81920\n"
    "v_mfma_f32_16x16x32_bf16 v[136:139], a[88:91], a[40:43], v[136:139]\n"
    "v_exp_f32 v133, v133\n"
    "v_mfma_f32_16x16x32_bf16 v[136:139], a[92:95], a[44:47], v[136:139]\n"
    "ds_read_b64_tr_b16 a[150:151], v181 offset:18432\n"
    "v_cvt_pk_bf16_f32 v202, v132, v133\n"
    "v_mfma_f32_16x16x32_bf16 v[88:91], a[164:167], v[224:227], v[88:91]\n"
    "v_exp_f32 v134, v134\n"
    "v_mfma_f32_16x16x32_bf16 v[88:91], a[168:171], v[228:231], v[88:91]\n"
    "ds_read_b64_tr_b16 a[152:153], v181 offset:20480\n"
    "s_add_u32 m0, s78, 0\n"
    "v_mfma_f32_16x16x32_bf16 v[92:95], a[168:171], v[236:239], v[92:95]\n"
    "v_exp_f32 v135, v135\n"
    "v_mfma_f32_16x16x32_bf16 v[92:95], a[164:167], v[232:235], v[92:95]\n"
    "ds_read_b64_tr_b16 a[154:155], v181 offset:22528\n"
    "v_cvt_pk_bf16_f32 v203, v134, v135\n"
    "v_mfma_f32_16x16x32_bf16 v[140:143], a[80:83], a[48:51], v[156:159]\n"
    "v_exp_f32 v136, v136\n"
    "v_mfma_f32_16x16x32_bf16 v[140:143], a[84:87], a[52:55], v[140:143]\n"
    "buffer_load_dwordx4 v33, s[44:47], s28 offen offset:0 lds\n"
    "v_mfma_f32_16x16x32_bf16 v[140:143], a[88:91], a[56:59], v[140:143]\n"
    "v_exp_f32 v137, v137\n"
    "v_mfma_f32_16x16x32_bf16 v[140:143], a[92:95], a[60:63], v[140:143]\n"
    "buffer_load_dwordx4 v34, s[44:47], s28 offen offset:1024 lds\n"
    "v_cvt_pk_bf16_f32 v210, v136, v137\n"
    "v_mfma_f32_16x16x32_bf16 a[216:219], a[164:167], v[240:243], a[216:219]\n"
    "v_exp_f32 v138, v138\n"
    "v_mfma_f32_16x16x32_bf16 a[216:219], a[168:171], v[244:247], a[216:219]\n"
    "buffer_load_dwordx4 v35, s[44:47], s28 offen offset:2048 lds\n"
    "v_mfma_f32_16x16x32_bf16 a[220:223], a[168:171], v[252:255], a[220:223]\n"
    "v_exp_f32 v139, v139\n"
    "v_mfma_f32_16x16x32_bf16 a[220:223], a[164:167], v[248:251], a[220:223]\n"
    "buffer_load_dwordx4 v36, s[44:47], s28 offen offset:3072 lds\n"
    "v_cvt_pk_bf16_f32 v211, v138, v139\n"
    "v_mfma_f32_16x16x32_bf16 a[176:179], v[60:63], v[232:235], a[176:179]\n"
    "v_mfma_f32_16x16x32_bf16 a[176:179], v[60:63], v[236:239], a[176:179]\n"
    "s_waitcnt lgkmcnt(4)\n"
    "v_mfma_f32_16x16x32_bf16 v[128:131], a[64:67], a[0:3], v[144:147]\n"
    "v_exp_f32 v140, v140\n"
    "v_mfma_f32_16x16x32_bf16 v[128:131], a[68:71], a[4:7], v[128:131]\n"
    "ds_read_b128 a[80:83], v168 offset:4096\n"
    "v_mfma_f32_16x16x32_bf16 v[128:131], a[72:75], a[8:11], v[128:131]\n"
    "v_exp_f32 v141, v141\n"
    "v_mfma_f32_16x16x32_bf16 v[128:131], a[76:79], a[12:15], v[128:131]\n"
    "ds_read_b128 a[84:87], v169 offset:4096\n"
    "v_cvt_pk_bf16_f32 v218, v140, v141\n"
    "v_mfma_f32_16x16x32_bf16 v[96:99], a[140:143], v[224:227], v[96:99]\n"
    "v_exp_f32 v142, v142\n"
    "v_mfma_f32_16x16x32_bf16 v[96:99], a[144:147], v[228:231], v[96:99]\n"
    "ds_read_b128 a[88:91], v170 offset:4096\n"
    "v_mfma_f32_16x16x32_bf16 v[100:103], a[144:147], v[236:239], v[100:103]\n"
    "v_exp_f32 v143, v143\n"
    "v_mfma_f32_16x16x32_bf16 v[100:103], a[140:143], v[232:235], v[100:103]\n"
    "ds_read_b128 a[92:95], v171 offset:4096\n"
    "v_cvt_pk_bf16_f32 v219, v142, v143\n"
    "v_mfma_f32_16x16x32_bf16 v[132:135], a[64:67], a[16:19], v[148:151]\n"
    "v_exp_f32 v128, v128\n"
    "v_mfma_f32_16x16x32_bf16 v[132:135], a[68:71], a[20:23], v[132:135]\n"
    "ds_read_b64_tr_b16 a[156:157], v182 offset:16384\n"
    "v_mfma_f32_16x16x32_bf16 v[132:135], a[72:75], a[24:27], v[132:135]\n"
    "v_exp_f32 v129, v129\n"
    "v_mfma_f32_16x16x32_bf16 v[132:135], a[76:79], a[28:31], v[132:135]\n"
    "ds_read_b64_tr_b16 a[158:159], v182 offset:18432\n"
    "v_cvt_pk_bf16_f32 v196, v128, v129\n"
    "v_mfma_f32_16x16x32_bf16 a[224:227], a[140:143], v[240:243], a[224:227]\n"
    "v_exp_f32 v130, v130\n"
    "v_mfma_f32_16x16x32_bf16 a[224:227], a[144:147], v[244:247], a[224:227]\n"
    "ds_read_b64_tr_b16 a[160:161], v182 offset:20480\n"
    "s_waitcnt lgkmcnt(7)\n"
    "v_mfma_f32_16x16x32_bf16 a[228:231], a[144:147], v[252:255], a[228:231]\n"
    "v_exp_f32 v131, v131\n"
    "v_mfma_f32_16x16x32_bf16 a[228:231], a[140:143], v[248:251], a[228:231]\n"
    "ds_read_b64_tr_b16 a[162:163], v182 offset:22528\n"
    "v_cvt_pk_bf16_f32 v197, v130, v131\n"
    "v_mfma_f32_16x16x32_bf16 v[136:139], a[64:67], a[32:35], v[152:155]\n"
    "v_exp_f32 v132, v132\n"
    "v_mfma_f32_16x16x32_bf16 v[136:139], a[68:71], a[36:39], v[136:139]\n"
    "ds_read_b64_tr_b16 a[164:165], v183 offset:16384\n"
    "s_add_u32 s29, s85, 65536\n"
    "v_mfma_f32_16x16x32_bf16 v[136:139], a[72:75], a[40:43], v[136:139]\n"
    "v_exp_f32 v133, v133\n"
    "v_mfma_f32_16x16x32_bf16 v[136:139], a[76:79], a[44:47], v[136:139]\n"
    "ds_read_b64_tr_b16 a[166:167], v183 offset:18432\n"
    "v_cvt_pk_bf16_f32 v204, v132, v133\n"
    "v_mfma_f32_16x16x32_bf16 v[104:107], a[148:151], v[224:227], v[104:107]\n"
    "v_exp_f32 v134, v134\n"
    "v_mfma_f32_16x16x32_bf16 v[104:107], a[152:155], v[228:231], v[104:107]\n"
    "ds_read_b64_tr_b16 a[168:169], v183 offset:20480\n"
    "s_add_u32 m0, s78, 16384\n"
    "v_mfma_f32_16x16x32_bf16 v[108:111], a[152:155], v[236:239], v[108:111]\n"
    "v_exp_f32 v135, v135\n"
    "v_mfma_f32_16x16x32_bf16 v[108:111], a[148:151], v[232:235], v[108:111]\n"
    "ds_read_b64_tr_b16 a[170:171], v183 offset:22528\n"
    "v_cvt_pk_bf16_f32 v205, v134, v135\n"
    "v_mfma_f32_16x16x32_bf16 v[140:143], a[64:67], a[48:51], v[156:159]\n"
    "v_exp_f32 v136, v136\n"
    "v_mfma_f32_16x16x32_bf16 v[140:143], a[68:71], a[52:55], v[140:143]\n"
    "buffer_load_dwordx4 v37, s[48:51], s29 offen offset:0 lds\n"
    "v_mfma_f32_16x16x32_bf16 v[140:143], a[72:75], a[56:59], v[140:143]\n"
    "v_exp_f32 v137, v137\n"
    "v_mfma_f32_16x16x32_bf16 v[140:143], a[76:79], a[60:63], v[140:143]\n"
    "buffer_load_dwordx4 v38, s[48:51], s29 offen offset:1024 lds\n"
    "v_cvt_pk_bf16_f32 v212, v136, v137\n"
    "v_mfma_f32_16x16x32_bf16 a[232:235], a[148:151], v[240:243], a[232:235]\n"
    "v_exp_f32 v138, v138\n"
    "v_mfma_f32_16x16x32_bf16 a[232:235], a[152:155], v[244:247], a[232:235]\n"
    "buffer_load_dwordx4 v39, s[48:51], s29 offen offset:2048 lds\n"
    "v_mfma_f32_16x16x32_bf16 a[236:239], a[152:155], v[252:255], a[236:239]\n"
    "v_exp_f32 v139, v139\n"
    "v_mfma_f32_16x16x32_bf16 a[236:239], a[148:151], v[248:251], a[236:239]\n"
    "buffer_load_dwordx4 v40, s[48:51], s29 offen offset:3072 lds\n"
    "v_cvt_pk_bf16_f32 v213, v138, v139\n"
    "v_mfma_f32_16x16x32_bf16 a[180:183], v[60:63], v[240:243], a[180:183]\n"
    "v_mfma_f32_16x16x32_bf16 a[180:183], v[60:63], v[244:247], a[180:183]\n"
    "s_waitcnt vmcnt(8) lgkmcnt(0)\n"
    "s_barrier\n"
    "v_mfma_f32_16x16x32_bf16 v[128:131], a[80:83], a[0:3], v[144:147]\n"
    "v_exp_f32 v140, v140\n"
    "v_mfma_f32_16x16x32_bf16 v[128:131], a[84:87], a[4:7], v[128:131]\n"
    "ds_read_b128 a[64:67], v164 offset:32768\n"
    "v_mfma_f32_16x16x32_bf16 v[128:131], a[88:91], a[8:11], v[128:131]\n"
    "v_exp_f32 v141, v141\n"
    "v_mfma_f32_16x16x32_bf16 v[128:131], a[92:95], a[12:15], v[128:131]\n"
    "ds_read_b128 a[68:71], v165 offset:32768\n"
    "v_cvt_pk_bf16_f32 v220, v140, v141\n"
    "v_mfma_f32_16x16x32_bf16 v[112:115], a[156:159], v[224:227], v[112:115]\n"
    "v_exp_f32 v142, v142\n"
    "v_mfma_f32_16x16x32_bf16 v[112:115], a[160:163], v[228:231], v[112:115]\n"
    "ds_read_b128 a[72:75], v166 offset:32768\n"
    "v_mfma_f32_16x16x32_bf16 v[116:119], a[160:163], v[236:239], v[116:119]\n"
    "v_exp_f32 v143, v143\n"
    "v_mfma_f32_16x16x32_bf16 v[116:119], a[156:159], v[232:235], v[116:119]\n"
    "ds_read_b128 a[76:79], v167 offset:32768\n"
    "v_cvt_pk_bf16_f32 v221, v142, v143\n"
    "v_mfma_f32_16x16x32_bf16 v[132:135], a[80:83], a[16:19], v[148:151]\n"
    "v_exp_f32 v128, v128\n"
    "v_mfma_f32_16x16x32_bf16 v[132:135], a[84:87], a[20:23], v[132:135]\n"
    "ds_read_b64_tr_b16 a[140:141], v176 offset:49152\n"
    "v_mfma_f32_16x16x32_bf16 v[132:135], a[88:91], a[24:27], v[132:135]\n"
    "v_exp_f32 v129, v129\n"
    "v_mfma_f32_16x16x32_bf16 v[132:135], a[92:95], a[28:31], v[132:135]\n"
    "ds_read_b64_tr_b16 a[142:143], v176 offset:51200\n"
    "v_cvt_pk_bf16_f32 v198, v128, v129\n"
    "v_mfma_f32_16x16x32_bf16 a[240:243], a[156:159], v[240:243], a[240:243]\n"
    "v_exp_f32 v130, v130\n"
    "v_mfma_f32_16x16x32_bf16 a[240:243], a[160:163], v[244:247], a[240:243]\n"
    "ds_read_b64_tr_b16 a[144:145], v176 offset:53248\n"
    "s_waitcnt lgkmcnt(7)\n"
    "v_mfma_f32_16x16x32_bf16 a[244:247], a[160:163], v[252:255], a[244:247]\n"
    "v_exp_f32 v131, v131\n"
    "v_mfma_f32_16x16x32_bf16 a[244:247], a[156:159], v[248:251], a[244:247]\n"
    "ds_read_b64_tr_b16 a[146:147], v176 offset:55296\n"
    "v_cvt_pk_bf16_f32 v199, v130, v131\n"
    "v_mfma_f32_16x16x32_bf16 v[136:139], a[80:83], a[32:35], v[152:155]\n"
    "v_exp_f32 v132, v132\n"
    "v_mfma_f32_16x16x32_bf16 v[136:139], a[84:87], a[36:39], v[136:139]\n"
    "ds_read_b64_tr_b16 a[148:149], v177 offset:49152\n"
    "s_add_u32 s30, s82, 98304\n"
    "v_mfma_f32_16x16x32_bf16 v[136:139], a[88:91], a[40:43], v[136:139]\n"
    "v_exp_f32 v133, v133\n"
    "v_mfma_f32_16x16x32_bf16 v[136:139], a[92:95], a[44:47], v[136:139]\n"
    "ds_read_b64_tr_b16 a[150:151], v177 offset:51200\n"
    "v_cvt_pk_bf16_f32 v206, v132, v133\n"
    "v_mfma_f32_16x16x32_bf16 v[120:123], a[164:167], v[224:227], v[120:123]\n"
    "v_exp_f32 v134, v134\n"
    "v_mfma_f32_16x16x32_bf16 v[120:123], a[168:171], v[228:231], v[120:123]\n"
    "ds_read_b64_tr_b16 a[152:153], v177 offset:53248\n"
    "s_add_u32 m0, s78, 32768\n"
    "v_mfma_f32_16x16x32_bf16 v[124:127], a[168:171], v[236:239], v[124:127]\n"
    "v_exp_f32 v135, v135\n"
    "v_mfma_f32_16x16x32_bf16 v[124:127], a[164:167], v[232:235], v[124:127]\n"
    "ds_read_b64_tr_b16 a[154:155], v177 offset:55296\n"
    "v_cvt_pk_bf16_f32 v207, v134, v135\n"
    "v_mfma_f32_16x16x32_bf16 v[140:143], a[80:83], a[48:51], v[156:159]\n"
    "v_exp_f32 v136, v136\n"
    "v_mfma_f32_16x16x32_bf16 v[140:143], a[84:87], a[52:55], v[140:143]\n"
    "buffer_load_dwordx4 v33, s[44:47], s30 offen offset:0 lds\n"
    "v_mfma_f32_16x16x32_bf16 v[140:143], a[88:91], a[56:59], v[140:143]\n"
    "v_exp_f32 v137, v137\n"
    "v_mfma_f32_16x16x32_bf16 v[140:143], a[92:95], a[60:63], v[140:143]\n"
    "buffer_load_dwordx4 v34, s[44:47], s30 offen offset:1024 lds\n"
    "v_cvt_pk_bf16_f32 v214, v136, v137\n"
    "v_mfma_f32_16x16x32_bf16 a[248:251], a[164:167], v[240:243], a[248:251]\n"
    "v_exp_f32 v138, v138\n"
    "v_mfma_f32_16x16x32_bf16 a[248:251], a[168:171], v[244:247], a[248:251]\n"
    "buffer_load_dwordx4 v35, s[44:47], s30 offen offset:2048 lds\n"
    "v_mfma_f32_16x16x32_bf16 a[252:255], a[168:171], v[252:255], a[252:255]\n"
    "v_exp_f32 v139, v139\n"
    "v_mfma_f32_16x16x32_bf16 a[252:255], a[164:167], v[248:251], a[252:255]\n"
    "buffer_load_dwordx4 v36, s[44:47], s30 offen offset:3072 lds\n"
    "v_cvt_pk_bf16_f32 v215, v138, v139\n"
    "v_mfma_f32_16x16x32_bf16 a[184:187], v[60:63], v[248:251], a[184:187]\n"
    "v_mfma_f32_16x16x32_bf16 a[184:187], v[60:63], v[252:255], a[184:187]\n"
    "s_waitcnt lgkmcnt(4)\n"
    "v_mfma_f32_16x16x32_bf16 v[128:131], a[64:67], a[0:3], v[144:147]\n"
    "v_exp_f32 v140, v140\n"
    "v_mfma_f32_16x16x32_bf16 v[128:131], a[68:71], a[4:7], v[128:131]\n"
    "ds_read_b128 a[80:83], v164 offset:36864\n"
    "v_mfma_f32_16x16x32_bf16 v[128:131], a[72:75], a[8:11], v[128:131]\n"
    "v_exp_f32 v141, v141\n"
    "v_mfma_f32_16x16x32_bf16 v[128:131], a[76:79], a[12:15], v[128:131]\n"
    "ds_read_b128 a[84:87], v165 offset:36864\n"
    "v_cvt_pk_bf16_f32 v222, v140, v141\n"
    "v_mfma_f32_16x16x32_bf16 v[64:67], a[140:143], v[192:195], v[64:67]\n"
    "v_exp_f32 v142, v142\n"
    "v_mfma_f32_16x16x32_bf16 v[64:67], a[144:147], v[196:199], v[64:67]\n"
    "ds_read_b128 a[88:91], v166 offset:36864\n"
    "v_mfma_f32_16x16x32_bf16 v[68:71], a[144:147], v[204:207], v[68:71]\n"
    "v_exp_f32 v143, v143\n"
    "v_mfma_f32_16x16x32_bf16 v[68:71], a[140:143], v[200:203], v[68:71]\n"
    "ds_read_b128 a[92:95], v167 offset:36864\n"
    "v_cvt_pk_bf16_f32 v223, v142, v143\n"
    "v_mfma_f32_16x16x32_bf16 v[132:135], a[64:67], a[16:19], v[148:151]\n"
    "v_exp_f32 v128, v128\n"
    "v_mfma_f32_16x16x32_bf16 v[132:135], a[68:71], a[20:23], v[132:135]\n"
    "ds_read_b64_tr_b16 a[156:157], v178 offset:49152\n"
    "v_mfma_f32_16x16x32_bf16 v[132:135], a[72:75], a[24:27], v[132:135]\n"
    "v_exp_f32 v129, v129\n"
    "v_mfma_f32_16x16x32_bf16 v[132:135], a[76:79], a[28:31], v[132:135]\n"
    "ds_read_b64_tr_b16 a[158:159], v178 offset:51200\n"
    "v_cvt_pk_bf16_f32 v224, v128, v129\n"
    "v_mfma_f32_16x16x32_bf16 a[192:195], a[140:143], v[208:211], a[192:195]\n"
    "v_exp_f32 v130, v130\n"
    "v_mfma_f32_16x16x32_bf16 a[192:195], a[144:147], v[212:215], a[192:195]\n"
    "ds_read_b64_tr_b16 a[160:161], v178 offset:53248\n"
    "s_waitcnt lgkmcnt(7)\n"
    "v_mfma_f32_16x16x32_bf16 a[196:199], a[144:147], v[220:223], a[196:199]\n"
    "v_exp_f32 v131, v131\n"
    "v_mfma_f32_16x16x32_bf16 a[196:199], a[140:143], v[216:219], a[196:199]\n"
    "ds_read_b64_tr_b16 a[162:163], v178 offset:55296\n"
    "v_cvt_pk_bf16_f32 v225, v130, v131\n"
    "v_mfma_f32_16x16x32_bf16 v[136:139], a[64:67], a[32:35], v[152:155]\n"
    "v_exp_f32 v132, v132\n"
    "v_mfma_f32_16x16x32_bf16 v[136:139], a[68:71], a[36:39], v[136:139]\n"
    "ds_read_b64_tr_b16 a[164:165], v179 offset:49152\n"
    "s_add_u32 s31, s84, 81920\n"
    "v_mfma_f32_16x16x32_bf16 v[136:139], a[72:75], a[40:43], v[136:139]\n"
    "v_exp_f32 v133, v133\n"
    "v_mfma_f32_16x16x32_bf16 v[136:139], a[76:79], a[44:47], v[136:139]\n"
    "ds_read_b64_tr_b16 a[166:167], v179 offset:51200\n"
    "v_cvt_pk_bf16_f32 v232, v132, v133\n"
    "v_mfma_f32_16x16x32_bf16 v[72:75], a[148:151], v[192:195], v[72:75]\n"
    "v_exp_f32 v134, v134\n"
    "v_mfma_f32_16x16x32_bf16 v[72:75], a[152:155], v[196:199], v[72:75]\n"
    "ds_read_b64_tr_b16 a[168:169], v179 offset:53248\n"
    "s_add_u32 m0, s78, 49152\n"
    "v_mfma_f32_16x16x32_bf16 v[76:79], a[152:155], v[204:207], v[76:79]\n"
    "v_exp_f32 v135, v135\n"
    "v_mfma_f32_16x16x32_bf16 v[76:79], a[148:151], v[200:203], v[76:79]\n"
    "ds_read_b64_tr_b16 a[170:171], v179 offset:55296\n"
    "v_cvt_pk_bf16_f32 v233, v134, v135\n"
    "v_mfma_f32_16x16x32_bf16 v[140:143], a[64:67], a[48:51], v[156:159]\n"
    "v_exp_f32 v136, v136\n"
    "v_mfma_f32_16x16x32_bf16 v[140:143], a[68:71], a[52:55], v[140:143]\n"
    "buffer_load_dwordx4 v37, s[48:51], s31 offen offset:0 lds\n"
    "v_mfma_f32_16x16x32_bf16 v[140:143], a[72:75], a[56:59], v[140:143]\n"
    "v_exp_f32 v137, v137\n"
    "v_mfma_f32_16x16x32_bf16 v[140:143], a[76:79], a[60:63], v[140:143]\n"
    "buffer_load_dwordx4 v38, s[48:51], s31 offen offset:1024 lds\n"
    "v_cvt_pk_bf16_f32 v240, v136, v137\n"
    "v_mfma_f32_16x16x32_bf16 a[200:203], a[148:151], v[208:211], a[200:203]\n"
    "v_exp_f32 v138, v138\n"
    "v_mfma_f32_16x16x32_bf16 a[200:203], a[152:155], v[212:215], a[200:203]\n"
    "buffer_load_dwordx4 v39, s[48:51], s31 offen offset:2048 lds\n"
    "v_mfma_f32_16x16x32_bf16 a[204:207], a[152:155], v[220:223], a[204:207]\n"
    "v_exp_f32 v139, v139\n"
    "v_mfma_f32_16x16x32_bf16 a[204:207], a[148:151], v[216:219], a[204:207]\n"
    "buffer_load_dwordx4 v40, s[48:51], s31 offen offset:3072 lds\n"
    "v_cvt_pk_bf16_f32 v241, v138, v139\n"
    "v_mfma_f32_16x16x32_bf16 a[172:175], v[60:63], v[192:195], a[172:175]\n"
    "v_mfma_f32_16x16x32_bf16 a[172:175], v[60:63], v[196:199], a[172:175]\n"
    "s_waitcnt vmcnt(8) lgkmcnt(0)\n"
    "s_barrier\n"
    "v_mfma_f32_16x16x32_bf16 v[128:131], a[80:83], a[0:3], v[144:147]\n"
    "v_exp_f32 v140, v140\n"
    "v_mfma_f32_16x16x32_bf16 v[128:131], a[84:87], a[4:7], v[128:131]\n"
    "ds_read_b128 a[64:67], v160 offset:0\n"
    "v_mfma_f32_16x16x32_bf16 v[128:131], a[88:91], a[8:11], v[128:131]\n"
    "v_exp_f32 v141, v141\n"
    "v_mfma_f32_16x16x32_bf16 v[128:131], a[92:95], a[12:15], v[128:131]\n"
    "ds_read_b128 a[68:71], v161 offset:0\n"
    "v_cvt_pk_bf16_f32 v248, v140, v141\n"
    "v_mfma_f32_16x16x32_bf16 v[80:83], a[156:159], v[192:195], v[80:83]\n"
    "v_exp_f32 v142, v142\n"
    "v_mfma_f32_16x16x32_bf16 v[80:83], a[160:163], v[196:199], v[80:83]\n"
    "ds_read_b128 a[72:75], v162 offset:0\n"
    "v_mfma_f32_16x16x32_bf16 v[84:87], a[160:163], v[204:207], v[84:87]\n"
    "v_exp_f32 v143, v143\n"
    "v_mfma_f32_16x16x32_bf16 v[84:87], a[156:159], v[200:203], v[84:87]\n"
    "ds_read_b128 a[76:79], v163 offset:0\n"
    "v_cvt_pk_bf16_f32 v249, v142, v143\n"
    "v_mfma_f32_16x16x32_bf16 v[132:135], a[80:83], a[16:19], v[148:151]\n"
    "v_exp_f32 v128, v128\n"
    "v_mfma_f32_16x16x32_bf16 v[132:135], a[84:87], a[20:23], v[132:135]\n"
    "ds_read_b64_tr_b16 a[140:141], v172 offset:16384\n"
    "v_mfma_f32_16x16x32_bf16 v[132:135], a[88:91], a[24:27], v[132:135]\n"
    "v_exp_f32 v129, v129\n"
    "v_mfma_f32_16x16x32_bf16 v[132:135], a[92:95], a[28:31], v[132:135]\n"
    "ds_read_b64_tr_b16 a[142:143], v172 offset:18432\n"
    "v_cvt_pk_bf16_f32 v226, v128, v129\n"
    "v_mfma_f32_16x16x32_bf16 a[208:211], a[156:159], v[208:211], a[208:211]\n"
    "v_exp_f32 v130, v130\n"
    "v_mfma_f32_16x16x32_bf16 a[208:211], a[160:163], v[212:215], a[208:211]\n"
    "ds_read_b64_tr_b16 a[144:145], v172 offset:20480\n"
    "s_waitcnt lgkmcnt(7)\n"
    "v_mfma_f32_16x16x32_bf16 a[212:215], a[160:163], v[220:223], a[212:215]\n"
    "v_exp_f32 v131, v131\n"
    "v_mfma_f32_16x16x32_bf16 a[212:215], a[156:159], v[216:219], a[212:215]\n"
    "ds_read_b64_tr_b16 a[146:147], v172 offset:22528\n"
    "v_cvt_pk_bf16_f32 v227, v130, v131\n"
    "v_mfma_f32_16x16x32_bf16 v[136:139], a[80:83], a[32:35], v[152:155]\n"
    "v_exp_f32 v132, v132\n"
    "v_mfma_f32_16x16x32_bf16 v[136:139], a[84:87], a[36:39], v[136:139]\n"
    "ds_read_b64_tr_b16 a[148:149], v173 offset:16384\n"
    "s_add_u32 s28, s83, 98304\n"
    "v_mfma_f32_16x16x32_bf16 v[136:139], a[88:91], a[40:43], v[136:139]\n"
    "v_exp_f32 v133, v133\n"
    "v_mfma_f32_16x16x32_bf16 v[136:139], a[92:95], a[44:47], v[136:139]\n"
    "ds_read_b64_tr_b16 a[150:151], v173 offset:18432\n"
    "v_cvt_pk_bf16_f32 v234, v132, v133\n"
    "v_mfma_f32_16x16x32_bf16 v[88:91], a[164:167], v[192:195], v[88:91]\n"
    "v_exp_f32 v134, v134\n"
    "v_mfma_f32_16x16x32_bf16 v[88:91], a[168:171], v[196:199], v[88:91]\n"
    "ds_read_b64_tr_b16 a[152:153], v173 offset:20480\n"
    "s_add_u32 m0, s78, 65536\n"
    "v_mfma_f32_16x16x32_bf16 v[92:95], a[168:171], v[204:207], v[92:95]\n"
    "v_exp_f32 v135, v135\n"
    "v_mfma_f32_16x16x32_bf16 v[92:95], a[164:167], v[200:203], v[92:95]\n"
    "ds_read_b64_tr_b16 a[154:155], v173 offset:22528\n"
    "v_cvt_pk_bf16_f32 v235, v134, v135\n"
    "v_mfma_f32_16x16x32_bf16 v[140:143], a[80:83], a[48:51], v[156:159]\n"
    "v_exp_f32 v136, v136\n"
    "v_mfma_f32_16x16x32_bf16 v[140:143], a[84:87], a[52:55], v[140:143]\n"
    "buffer_load_dwordx4 v33, s[44:47], s28 offen offset:0 lds\n"
    "v_mfma_f32_16x16x32_bf16 v[140:143], a[88:91], a[56:59], v[140:143]\n"
    "v_exp_f32 v137, v137\n"
    "v_mfma_f32_16x16x32_bf16 v[140:143], a[92:95], a[60:63], v[140:143]\n"
    "buffer_load_dwordx4 v34, s[44:47], s28 offen offset:1024 lds\n"
    "v_cvt_pk_bf16_f32 v242, v136, v137\n"
    "v_mfma_f32_16x16x32_bf16 a[216:219], a[164:167], v[208:211], a[216:219]\n"
    "v_exp_f32 v138, v138\n"
    "v_mfma_f32_16x16x32_bf16 a[216:219], a[168:171], v[212:215], a[216:219]\n"
    "buffer_load_dwordx4 v35, s[44:47], s28 offen offset:2048 lds\n"
    "v_mfma_f32_16x16x32_bf16 a[220:223], a[168:171], v[220:223], a[220:223]\n"
    "v_exp_f32 v139, v139\n"
    "v_mfma_f32_16x16x32_bf16 a[220:223], a[164:167], v[216:219], a[220:223]\n"
    "buffer_load_dwordx4 v36, s[44:47], s28 offen offset:3072 lds\n"
    "v_cvt_pk_bf16_f32 v243, v138, v139\n"
    "v_mfma_f32_16x16x32_bf16 a[176:179], v[60:63], v[200:203], a[176:179]\n"
    "v_mfma_f32_16x16x32_bf16 a[176:179], v[60:63], v[204:207], a[176:179]\n"
    "s_waitcnt lgkmcnt(4)\n"
    "v_mfma_f32_16x16x32_bf16 v[128:131], a[64:67], a[0:3], v[144:147]\n"
    "v_exp_f32 v140, v140\n"
    "v_mfma_f32_16x16x32_bf16 v[128:131], a[68:71], a[4:7], v[128:131]\n"
    "ds_read_b128 a[80:83], v160 offset:4096\n"
    "Lw7_%=:\n"
    "s_cbranch_execz Lw8_%=\n"
    "v_mfma_f32_16x16x32_bf16 v[128:131], a[72:75], a[8:11], v[128:131]\n"
    "v_exp_f32 v141, v141\n"
    "v_mfma_f32_16x16x32_bf16 v[128:131], a[76:79], a[12:15], v[128:131]\n"
    "ds_read_b128 a[84:87], v161 offset:4096\n"
    "v_cvt_pk_bf16_f32 v250, v140, v141\n"
    "v_mfma_f32_16x16x32_bf16 v[96:99], a[140:143], v[192:195], v[96:99]\n"
    "v_exp_f32 v142, v142\n"
    "v_mfma_f32_16x16x32_bf16 v[96:99], a[144:147], v[196:199], v[96:99]\n"
    "ds_read_b128 a[88:91], v162 offset:4096\n"
    "v_mfma_f32_16x16x32_bf16 v[100:103], a[144:147], v[204:207], v[100:103]\n"
    "v_exp_f32 v143, v143\n"
    "v_mfma_f32_16x16x32_bf16 v[100:103], a[140:143], v[200:203], v[100:103]\n"
    "ds_read_b128 a[92:95], v163 offset:4096\n"
    "v_cvt_pk_bf16_f32 v251, v142, v143\n"
    "v_mfma_f32_16x16x32_bf16 v[132:135], a[64:67], a[16:19], v[148:151]\n"
    "v_exp_f32 v128, v128\n"
    "v_mfma_f32_16x16x32_bf16 v[132:135], a[68:71], a[20:23], v[132:135]\n"
    "ds_read_b64_tr_b16 a[156:157], v174 offset:16384\n"
    "v_mfma_f32_16x16x32_bf16 v[132:135], a[72:75], a[24:27], v[132:135]\n"
    "v_exp_f32 v129, v129\n"
    "v_mfma_f32_16x16x32_bf16 v[132:135], a[76:79], a[28:31], v[132:135]\n"
    "ds_read_b64_tr_b16 a[158:159], v174 offset:18432\n"
    "v_cvt_pk_bf16_f32 v228, v128, v129\n"
    "v_mfma_f32_16x16x32_bf16 a[224:227], a[140:143], v[208:211], a[224:227]\n"
    "v_exp_f32 v130, v130\n"
    "v_mfma_f32_16x16x32_bf16 a[224:227], a[144:147], v[212:215], a[224:227]\n"
    "ds_read_b64_tr_b16 a[160:161], v174 offset:20480\n"
    "s_waitcnt lgkmcnt(7)\n"
    "v_mfma_f32_16x16x32_bf16 a[228:231], a[144:147], v[220:223], a[228:231]\n"
    "v_exp_f32 v131, v131\n"
    "v_mfma_f32_16x16x32_bf16 a[228:231], a[140:143], v[216:219], a[228:231]\n"
    "ds_read_b64_tr_b16 a[162:163], v174 offset:22528\n"
    "v_cvt_pk_bf16_f32 v229, v130, v131\n"
    "v_mfma_f32_16x16x32_bf16 v[136:139], a[64:67], a[32:35], v[152:155]\n"
    "v_exp_f32 v132, v132\n"
    "v_mfma_f32_16x16x32_bf16 v[136:139], a[68:71], a[36:39], v[136:139]\n"
    "ds_read_b64_tr_b16 a[164:165], v175 offset:16384\n"
    "s_add_u32 s29, s85, 81920\n"
    "v_mfma_f32_16x16x32_bf16 v[136:139], a[72:75], a[40:43], v[136:139]\n"
    "v_exp_f32 v133, v133\n"
    "v_mfma_f32_16x16x32_bf16 v[136:139], a[76:79], a[44:47], v[136:139]\n"
    "ds_read_b64_tr_b16 a[166:167], v175 offset:18432\n"
    "v_cvt_pk_bf16_f32 v236, v132, v133\n"
    "v_mfma_f32_16x16x32_bf16 v[104:107], a[148:151], v[192:195], v[104:107]\n"
    "v_exp_f32 v134, v134\n"
    "v_mfma_f32_16x16x32_bf16 v[104:107], a[152:155], v[196:199], v[104:107]\n"
    "ds_read_b64_tr_b16 a[168:169], v175 offset:20480\n"
    "s_add_u32 m0, s78, 81920\n"
    "v_mfma_f32_16x16x32_bf16 v[108:111], a[152:155], v[204:207], v[108:111]\n"
    "v_exp_f32 v135, v135\n"
    "v_mfma_f32_16x16x32_bf16 v[108:111], a[148:151], v[200:203], v[108:111]\n"
    "ds_read_b64_tr_b16 a[170:171], v175 offset:22528\n"
    "v_cvt_pk_bf16_f32 v237, v134, v135\n"
    "v_mfma_f32_16x16x32_bf16 v[140:143], a[64:67], a[48:51], v[156:159]\n"
    "v_exp_f32 v136, v136\n"
    "v_mfma_f32_16x16x32_bf16 v[140:143], a[68:71], a[52:55], v[140:143]\n"
    "buffer_load_dwordx4 v37, s[48:51], s29 offen offset:0 lds\n"
    "v_mfma_f32_16x16x32_bf16 v[140:143], a[72:75], a[56:59], v[140:143]\n"
    "v_exp_f32 v137, v137\n"
    "v_mfma_f32_16x16x32_bf16 v[140:143], a[76:79], a[60:63], v[140:143]\n"
    "buffer_load_dwordx4 v38, s[48:51], s29 offen offset:1024 lds\n"
    "v_cvt_pk_bf16_f32 v244, v136, v137\n"
    "v_mfma_f32_16x16x32_bf16 a[232:235], a[148:151], v[208:211], a[232:235]\n"
    "v_exp_f32 v138, v138\n"
    "v_mfma_f32_16x16x32_bf16 a[232:235], a[152:155], v[212:215], a[232:235]\n"
    "buffer_load_dwordx4 v39, s[48:51], s29 offen offset:2048 lds\n"
    "v_mfma_f32_16x16x32_bf16 a[236:239], a[152:155], v[220:223], a[236:239]\n"
    "v_exp_f32 v139, v139\n"
    "v_mfma_f32_16x16x32_bf16 a[236:239], a[148:151], v[216:219], a[236:239]\n"
    "buffer_load_dwordx4 v40, s[48:51], s29 offen offset:3072 lds\n"
    "v_cvt_pk_bf16_f32 v245, v138, v139\n"
    "v_mfma_f32_16x16x32_bf16 a[180:183], v[60:63], v[208:211], a[180:183]\n"
    "v_mfma_f32_16x16x32_bf16 a[180:183], v[60:63], v[212:215], a[180:183]\n"
    "s_waitcnt vmcnt(8) lgkmcnt(0)\n"
    "s_barrier\n"
    "v_mfma_f32_16x16x32_bf16 v[128:131], a[80:83], a[0:3], v[144:147]\n"
    "v_exp_f32 v140, v140\n"
    "v_mfma_f32_16x16x32_bf16 v[128:131], a[84:87], a[4:7], v[128:131]\n"
    "ds_read_b128 a[64:67], v160 offset:32768\n"
    "v_mfma_f32_16x16x32_bf16 v[128:131], a[88:91], a[8:11], v[128:131]\n"
    "v_exp_f32 v141, v141\n"
    "v_mfma_f32_16x16x32_bf16 v[128:131], a[92:95], a[12:15], v[128:131]\n"
    "ds_read_b128 a[68:71], v161 offset:32768\n"
    "v_cvt_pk_bf16_f32 v252, v140, v141\n"
    "v_mfma_f32_16x16x32_bf16 v[112:115], a[156:159], v[192:195], v[112:115]\n"
    "v_exp_f32 v142, v142\n"
    "v_mfma_f32_16x16x32_bf16 v[112:115], a[160:163], v[196:199], v[112:115]\n"
    "ds_read_b128 a[72:75], v162 offset:32768\n"
    "v_mfma_f32_16x16x32_bf16 v[116:119], a[160:163], v[204:207], v[116:119]\n"
    "v_exp_f32 v143, v143\n"
    "v_mfma_f32_16x16x32_bf16 v[116:119], a[156:159], v[200:203], v[116:119]\n"
    "ds_read_b128 a[76:79], v163 offset:32768\n"
    "v_cvt_pk_bf16_f32 v253, v142, v143\n"
    "v_mfma_f32_16x16x32_bf16 v[132:135], a[80:83], a[16:19], v[148:151]\n"
    "v_exp_f32 v128, v128\n"
    "v_mfma_f32_16x16x32_bf16 v[132:135], a[84:87], a[20:23], v[132:135]\n"
    "ds_read_b64_tr_b16 a[140:141], v172 offset:49152\n"
    "v_mfma_f32_16x16x32_bf16 v[132:135], a[88:91], a[24:27], v[132:135]\n"
    "v_exp_f32 v129, v129\n"
    "v_mfma_f32_16x16x32_bf16 v[132:135], a[92:95], a[28:31], v[132:135]\n"
    "ds_read_b64_tr_b16 a[142:143], v172 offset:51200\n"
    "v_cvt_pk_bf16_f32 v230, v128, v129\n"
    "v_mfma_f32_16x16x32_bf16 a[240:243], a[156:159], v[208:211], a[240:243]\n"
    "v_exp_f32 v130, v130\n"
    "v_mfma_f32_16x16x32_bf16 a[240:243], a[160:163], v[212:215], a[240:243]\n"
    "ds_read_b64_tr_b16 a[144:145], v172 offset:53248\n"
    "s_waitcnt lgkmcnt(7)\n"
    "v_mfma_f32_16x16x32_bf16 a[244:247], a[160:163], v[220:223], a[244:247]\n"
    "v_exp_f32 v131, v131\n"
    "v_mfma_f32_16x16x32_bf16 a[244:247], a[156:159], v[216:219], a[244:247]\n"
    "ds_read_b64_tr_b16 a[146:147], v172 offset:55296\n"
    "v_cvt_pk_bf16_f32 v231, v130, v131\n"
    "v_mfma_f32_16x16x32_bf16 v[136:139], a[80:83], a[32:35], v[152:155]\n"
    "v_exp_f32 v132, v132\n"
    "v_mfma_f32_16x16x32_bf16 v[136:139], a[84:87], a[36:39], v[136:139]\n"
    "ds_read_b64_tr_b16 a[148:149], v173 offset:49152\n"
    "s_add_u32 s30, s82, 114688\n"
    "v_mfma_f32_16x16x32_bf16 v[136:139], a[88:91], a[40:43], v[136:139]\n"
    "v_exp_f32 v133, v133\n"
    "v_mfma_f32_16x16x32_bf16 v[136:139], a[92:95], a[44:47], v[136:139]\n"
    "ds_read_b64_tr_b16 a[150:151], v173 offset:51200\n"
    "v_cvt_pk_bf16_f32 v238, v132, v133\n"
    "v_mfma_f32_16x16x32_bf16 v[120:123], a[164:167], v[192:195], v[120:123]\n"
    "v_exp_f32 v134, v134\n"
    "v_mfma_f32_16x16x32_bf16 v[120:123], a[168:171], v[196:199], v[120:123]\n"
    "ds_read_b64_tr_b16 a[152:153], v173 offset:53248\n"
    "s_add_u32 m0, s78, 131072\n"
    "v_mfma_f32_16x16x32_bf16 v[124:127], a[168:171], v[204:207], v[124:127]\n"
    "v_exp_f32 v135, v135\n"
    "v_mfma_f32_16x16x32_bf16 v[124:127], a[164:167], v[200:203], v[124:127]\n"
    "ds_read_b64_tr_b16 a[154:155], v173 offset:55296\n"
    "v_cvt_pk_bf16_f32 v239, v134, v135\n"
    "v_mfma_f32_16x16x32_bf16 v[140:143], a[80:83], a[48:51], v[156:159]\n"
    "v_exp_f32 v136, v136\n"
    "v_mfma_f32_16x16x32_bf16 v[140:143], a[84:87], a[52:55], v[140:143]\n"
    "buffer_load_dwordx4 v33, s[44:47], s30 offen offset:0 lds\n"
    "v_mfma_f32_16x16x32_bf16 v[140:143], a[88:91], a[56:59], v[140:143]\n"
    "v_exp_f32 v137, v137\n"
    "v_mfma_f32_16x16x32_bf16 v[140:143], a[92:95], a[60:63], v[140:143]\n"
    "buffer_load_dwordx4 v34, s[44:47], s30 offen offset:1024 lds\n"
    "v_cvt_pk_bf16_f32 v246, v136, v137\n"
    "v_mfma_f32_16x16x32_bf16 a[248:251], a[164:167], v[208:211], a[248:251]\n"
    "v_exp_f32 v138, v138\n"
    "v_mfma_f32_16x16x32_bf16 a[248:251], a[168:171], v[212:215], a[248:251]\n"
    "buffer_load_dwordx4 v35, s[44:47], s30 offen offset:2048 lds\n"
    "v_mfma_f32_16x16x32_bf16 a[252:255], a[168:171], v[220:223], a[252:255]\n"
    "v_exp_f32 v139, v139\n"
    "v_mfma_f32_16x16x32_bf16 a[252:255], a[164:167], v[216:219], a[252:255]\n"
    "buffer_load_dwordx4 v36, s[44:47], s30 offen offset:3072 lds\n"
    "v_cvt_pk_bf16_f32 v247, v138, v139\n"
    "v_mfma_f32_16x16x32_bf16 a[184:187], v[60:63], v[216:219], a[184:187]\n"
    "v_mfma_f32_16x16x32_bf16 a[184:187], v[60:63], v[220:223], a[184:187]\n"
    "s_waitcnt lgkmcnt(4)\n"
    "v_mfma_f32_16x16x32_bf16 v[128:131], a[64:67], a[0:3], v[144:147]\n"
    "v_exp_f32 v140, v140\n"
    "v_mfma_f32_16x16x32_bf16 v[128:131], a[68:71], a[4:7], v[128:131]\n"
    "ds_read_b128 a[80:83], v160 offset:36864\n"
    "v_mfma_f32_16x16x32_bf16 v[128:131], a[72:75], a[8:11], v[128:131]\n"
    "v_exp_f32 v141, v141\n"
    "v_mfma_f32_16x16x32_bf16 v[128:131], a[76:79], a[12:15], v[128:131]\n"
    "ds_read_b128 a[84:87], v161 offset:36864\n"
    "v_cvt_pk_bf16_f32 v254, v140, v141\n"
    "v_mfma_f32_16x16x32_bf16 v[64:67], a[140:143], v[224:227], v[64:67]\n"
    "v_exp_f32 v142, v142\n"
    "v_mfma_f32_16x16x32_bf16 v[64:67], a[144:147], v[228:231], v[64:67]\n"
    "ds_read_b128 a[88:91], v162 offset:36864\n"
    "v_mfma_f32_16x16x32_bf16 v[68:71], a[144:147], v[236:239], v[68:71]\n"
    "v_exp_f32 v143, v143\n"
    "v_mfma_f32_16x16x32_bf16 v[68:71], a[140:143], v[232:235], v[68:71]\n"
    "ds_read_b128 a[92:95], v163 offset:36864\n"
    "v_cvt_pk_bf16_f32 v255, v142, v143\n"
    "v_mfma_f32_16x16x32_bf16 v[132:135], a[64:67], a[16:19], v[148:151]\n"
    "v_exp_f32 v128, v128\n"
    "v_mfma_f32_16x16x32_bf16 v[132:135], a[68:71], a[20:23], v[132:135]\n"
    "ds_read_b64_tr_b16 a[156:157], v174 offset:49152\n"
    "v_mfma_f32_16x16x32_bf16 v[132:135], a[72:75], a[24:27], v[132:135]\n"
    "v_exp_f32 v129, v129\n"
    "v_mfma_f32_16x16x32_bf16 v[132:135], a[76:79], a[28:31], v[132:135]\n"
    "ds_read_b64_tr_b16 a[158:159], v174 offset:51200\n"
    "v_cvt_pk_bf16_f32 v192, v128, v129\n"
    "v_mfma_f32_16x16x32_bf16 a[192:195], a[140:143], v[240:243], a[192:195]\n"
    "v_exp_f32 v130, v130\n"
    "v_mfma_f32_16x16x32_bf16 a[192:195], a[144:147], v[244:247], a[192:195]\n"
    "ds_read_b64_tr_b16 a[160:161], v174 offset:53248\n"
    "s_waitcnt lgkmcnt(7)\n"
    "v_mfma_f32_16x16x32_bf16 a[196:199], a[144:147], v[252:255], a[196:199]\n"
    "v_exp_f32 v131, v131\n"
    "v_mfma_f32_16x16x32_bf16 a[196:199], a[140:143], v[248:251], a[196:199]\n"
    "ds_read_b64_tr_b16 a[162:163], v174 offset:55296\n"
    "v_cvt_pk_bf16_f32 v193, v130, v131\n"
    "v_mfma_f32_16x16x32_bf16 v[136:139], a[64:67], a[32:35], v[152:155]\n"
    "v_exp_f32 v132, v132\n"
    "v_mfma_f32_16x16x32_bf16 v[136:139], a[68:71], a[36:39], v[136:139]\n"
    "ds_read_b64_tr_b16 a[164:165], v175 offset:49152\n"
    "s_add_u32 s31, s84, 98304\n"
    "v_mfma_f32_16x16x32_bf16 v[136:139], a[72:75], a[40:43], v[136:139]\n"
    "v_exp_f32 v133, v133\n"
    "v_mfma_f32_16x16x32_bf16 v[136:139], a[76:79], a[44:47], v[136:139]\n"
    "ds_read_b64_tr_b16 a[166:167], v175 offset:51200\n"
    "v_cvt_pk_bf16_f32 v200, v132, v133\n"
    "v_mfma_f32_16x16x32_bf16 v[72:75], a[148:151], v[224:227], v[72:75]\n"
    "v_exp_f32 v134, v134\n"
    "v_mfma_f32_16x16x32_bf16 v[72:75], a[152:155], v[228:231], v[72:75]\n"
    "ds_read_b64_tr_b16 a[168:169], v175 offset:53248\n"
    "s_add_u32 m0, s78, 147456\n"
    "v_mfma_f32_16x16x32_bf16 v[76:79], a[152:155], v[236:239], v[76:79]\n"
    "v_exp_f32 v135, v135\n"
    "v_mfma_f32_16x16x32_bf16 v[76:79], a[148:151], v[232:235], v[76:79]\n"
    "ds_read_b64_tr_b16 a[170:171], v175 offset:55296\n"
    "v_cvt_pk_bf16_f32 v201, v134, v135\n"
    "v_mfma_f32_16x16x32_bf16 v[140:143], a[64:67], a[48:51], v[156:159]\n"
    "v_exp_f32 v136, v136\n"
    "v_mfma_f32_16x16x32_bf16 v[140:143], a[68:71], a[52:55], v[140:143]\n"
    "buffer_load_dwordx4 v37, s[48:51], s31 offen offset:0 lds\n"
    "v_mfma_f32_16x16x32_bf16 v[140:143], a[72:75], a[56:59], v[140:143]\n"
    "v_exp_f32 v137, v137\n"
    "v_mfma_f32_16x16x32_bf16 v[140:143], a[76:79], a[60:63], v[140:143]\n"
    "buffer_load_dwordx4 v38, s[48:51], s31 offen offset:1024 lds\n"
    "v_cvt_pk_bf16_f32 v208, v136, v137\n"
    "v_mfma_f32_16x16x32_bf16 a[200:203], a[148:151], v[240:243], a[200:203]\n"
    "v_exp_f32 v138, v138\n"
    "v_mfma_f32_16x16x32_bf16 a[200:203], a[152:155], v[244:247], a[200:203]\n"
    "buffer_load_dwordx4 v39, s[48:51], s31 offen offset:2048 lds\n"
    "v_mfma_f32_16x16x32_bf16 a[204:207], a[152:155], v[252:255], a[204:207]\n"
    "v_exp_f32 v139, v139\n"
    "v_mfma_f32_16x16x32_bf16 a[204:207], a[148:151], v[248:251], a[204:207]\n"
    "buffer_load_dwordx4 v40, s[48:51], s31 offen offset:3072 lds\n"
    "v_cvt_pk_bf16_f32 v209, v138, v139\n"
    "v_mfma_f32_16x16x32_bf16 a[172:175], v[60:63], v[224:227], a[172:175]\n"
    "v_mfma_f32_16x16x32_bf16 a[172:175], v[60:63], v[228:231], a[172:175]\n"
    "s_waitcnt vmcnt(8) lgkmcnt(0)\n"
    "s_barrier\n"
    "v_mfma_f32_16x16x32_bf16 v[128:131], a[80:83], a[0:3], v[144:147]\n"
    "v_exp_f32 v140, v140\n"
    "v_mfma_f32_16x16x32_bf16 v[128:131], a[84:87], a[4:7], v[128:131]\n"
    "ds_read_b128 a[64:67], v164 offset:0\n"
    "v_mfma_f32_16x16x32_bf16 v[128:131], a[88:91], a[8:11], v[128:131]\n"
    "v_exp_f32 v141, v141\n"
    "v_mfma_f32_16x16x32_bf16 v[128:131], a[92:95], a[12:15], v[128:131]\n"
    "ds_read_b128 a[68:71], v165 offset:0\n"
    "v_cvt_pk_bf16_f32 v216, v140, v141\n"
    "v_mfma_f32_16x16x32_bf16 v[80:83], a[156:159], v[224:227], v[80:83]\n"
    "v_exp_f32 v142, v142\n"
    "v_mfma_f32_16x16x32_bf16 v[80:83], a[160:163], v[228:231], v[80:83]\n"
    "ds_read_b128 a[72:75], v166 offset:0\n"
    "v_mfma_f32_16x16x32_bf16 v[84:87], a[160:163], v[236:239], v[84:87]\n"
    "v_exp_f32 v143, v143\n"
    "v_mfma_f32_16x16x32_bf16 v[84:87], a[156:159], v[232:235], v[84:87]\n"
    "ds_read_b128 a[76:79], v167 offset:0\n"
    "v_cvt_pk_bf16_f32 v217, v142, v143\n"
    "v_mfma_f32_16x16x32_bf16 v[132:135], a[80:83], a[16:19], v[148:151]\n"
    "v_exp_f32 v128, v128\n"
    "v_mfma_f32_16x16x32_bf16 v[132:135], a[84:87], a[20:23], v[132:135]\n"
    "ds_read_b64_tr_b16 a[140:141], v176 offset:16384\n"
    "v_mfma_f32_16x16x32_bf16 v[132:135], a[88:91], a[24:27], v[132:135]\n"
    "v_exp_f32 v129, v129\n"
    "v_mfma_f32_16x16x32_bf16 v[132:135], a[92:95], a[28:31], v[132:135]\n"
    "ds_read_b64_tr_b16 a[142:143], v176 offset:18432\n"
    "v_cvt_pk_bf16_f32 v194, v128, v129\n"
    "v_mfma_f32_16x16x32_bf16 a[208:211], a[156:159], v[240:243], a[208:211]\n"
    "v_exp_f32 v130, v130\n"
    "v_mfma_f32_16x16x32_bf16 a[208:211], a[160:163], v[244:247], a[208:211]\n"
    "ds_read_b64_tr_b16 a[144:145], v176 offset:20480\n"
    "s_waitcnt lgkmcnt(7)\n"
    "v_mfma_f32_16x16x32_bf16 a[212:215], a[160:163], v[252:255], a[212:215]\n"
    "v_exp_f32 v131, v131\n"
    "v_mfma_f32_16x16x32_bf16 a[212:215], a[156:159], v[248:251], a[212:215]\n"
    "ds_read_b64_tr_b16 a[146:147], v176 offset:22528\n"
    "v_cvt_pk_bf16_f32 v195, v130, v131\n"
    "v_mfma_f32_16x16x32_bf16 v[136:139], a[80:83], a[32:35], v[152:155]\n"
    "v_exp_f32 v132, v132\n"
    "v_mfma_f32_16x16x32_bf16 v[136:139], a[84:87], a[36:39], v[136:139]\n"
    "ds_read_b64_tr_b16 a[148:149], v177 offset:16384\n"
    "s_add_u32 s28, s83, 114688\n"
    "v_mfma_f32_16x16x32_bf16 v[136:139], a[88:91], a[40:43], v[136:139]\n"
    "v_exp_f32 v133, v133\n"
    "v_mfma_f32_16x16x32_bf16 v[136:139], a[92:95], a[44:47], v[136:139]\n"
    "ds_read_b64_tr_b16 a[150:151], v177 offset:18432\n"
    "v_cvt_pk_bf16_f32 v202, v132, v133\n"
    "v_mfma_f32_16x16x32_bf16 v[88:91], a[164:167], v[224:227], v[88:91]\n"
    "v_exp_f32 v134, v134\n"
    "v_mfma_f32_16x16x32_bf16 v[88:91], a[168:171], v[228:231], v[88:91]\n"
    "ds_read_b64_tr_b16 a[152:153], v177 offset:20480\n"
    "s_add_u32 m0, s78, 98304\n"
    "v_mfma_f32_16x16x32_bf16 v[92:95], a[168:171], v[236:239], v[92:95]\n"
    "v_exp_f32 v135, v135\n"
    "v_mfma_f32_16x16x32_bf16 v[92:95], a[164:167], v[232:235], v[92:95]\n"
    "ds_read_b64_tr_b16 a[154:155], v177 offset:22528\n"
    "v_cvt_pk_bf16_f32 v203, v134, v135\n"
    "v_mfma_f32_16x16x32_bf16 v[140:143], a[80:83], a[48:51], v[156:159]\n"
    "v_exp_f32 v136, v136\n"
    "v_mfma_f32_16x16x32_bf16 v[140:143], a[84:87], a[52:55], v[140:143]\n"
    "buffer_load_dwordx4 v33, s[44:47], s28 offen offset:0 lds\n"
    "v_mfma_f32_16x16x32_bf16 v[140:143], a[88:91], a[56:59], v[140:143]\n"
    "v_exp_f32 v137, v137\n"
    "v_mfma_f32_16x16x32_bf16 v[140:143], a[92:95], a[60:63], v[140:143]\n"
    "buffer_load_dwordx4 v34, s[44:47], s28 offen offset:1024 lds\n"
    "v_cvt_pk_bf16_f32 v210, v136, v137\n"
    "v_mfma_f32_16x16x32_bf16 a[216:219], a[164:167], v[240:243], a[216:219]\n"
    "v_exp_f32 v138, v138\n"
    "v_mfma_f32_16x16x32_bf16 a[216:219], a[168:171], v[244:247], a[216:219]\n"
    "buffer_load_dwordx4 v35, s[44:47], s28 offen offset:2048 lds\n"
    "v_mfma_f32_16x16x32_bf16 a[220:223], a[168:171], v[252:255], a[220:223]\n"
    "v_exp_f32 v139, v139\n"
    "v_mfma_f32_16x16x32_bf16 a[220:223], a[164:167], v[248:251], a[220:223]\n"
    "buffer_load_dwordx4 v36, s[44:47], s28 offen offset:3072 lds\n"
    "v_cvt_pk_bf16_f32 v211, v138, v139\n"
    "v_mfma_f32_16x16x32_bf16 a[176:179], v[60:63], v[232:235], a[176:179]\n"
    "v_mfma_f32_16x16x32_bf16 a[176:179], v[60:63], v[236:239], a[176:179]\n"
    "s_waitcnt lgkmcnt(4)\n"
    "v_mfma_f32_16x16x32_bf16 v[128:131], a[64:67], a[0:3], v[144:147]\n"
    "v_exp_f32 v140, v140\n"
    "v_mfma_f32_16x16x32_bf16 v[128:131], a[68:71], a[4:7], v[128:131]\n"
    "ds_read_b128 a[80:83], v164 offset:4096\n"
    "v_mfma_f32_16x16x32_bf16 v[128:131], a[72:75], a[8:11], v[128:131]\n"
    "v_exp_f32 v141, v141\n"
    "v_mfma_f32_16x16x32_bf16 v[128:131], a[76:79], a[12:15], v[128:131]\n"
    "ds_read_b128 a[84:87], v165 offset:4096\n"
    "v_cvt_pk_bf16_f32 v218, v140, v141\n"
    "v_mfma_f32_16x16x32_bf16 v[96:99], a[140:143], v[224:227], v[96:99]\n"
    "v_exp_f32 v142, v142\n"
    "v_mfma_f32_16x16x32_bf16 v[96:99], a[144:147], v[228:231], v[96:99]\n"
    "ds_read_b128 a[88:91], v166 offset:4096\n"
    "v_mfma_f32_16x16x32_bf16 v[100:103], a[144:147], v[236:239], v[100:103]\n"
    "v_exp_f32 v143, v143\n"
    "v_mfma_f32_16x16x32_bf16 v[100:103], a[140:143], v[232:235], v[100:103]\n"
    "ds_read_b128 a[92:95], v167 offset:4096\n"
    "v_cvt_pk_bf16_f32 v219, v142, v143\n"
    "v_mfma_f32_16x16x32_bf16 v[132:135], a[64:67], a[16:19], v[148:151]\n"
    "v_exp_f32 v128, v128\n"
    "v_mfma_f32_16x16x32_bf16 v[132:135], a[68:71], a[20:23], v[132:135]\n"
    "ds_read_b64_tr_b16 a[156:157], v178 offset:16384\n"
    "v_mfma_f32_16x16x32_bf16 v[132:135], a[72:75], a[24:27], v[132:135]\n"
    "v_exp_f32 v129, v129\n"
    "v_mfma_f32_16x16x32_bf16 v[132:135], a[76:79], a[28:31], v[132:135]\n"
    "ds_read_b64_tr_b16 a[158:159], v178 offset:18432\n"
    "v_cvt_pk_bf16_f32 v196, v128, v129\n"
    "v_mfma_f32_16x16x32_bf16 a[224:227], a[140:143], v[240:243], a[224:227]\n"
    "v_exp_f32 v130, v130\n"
    "v_mfma_f32_16x16x32_bf16 a[224:227], a[144:147], v[244:247], a[224:227]\n"
    "ds_read_b64_tr_b16 a[160:161], v178 offset:20480\n"
    "s_waitcnt lgkmcnt(7)\n"
    "v_mfma_f32_16x16x32_bf16 a[228:231], a[144:147], v[252:255], a[228:231]\n"
    "v_exp_f32 v131, v131\n"
    "v_mfma_f32_16x16x32_bf16 a[228:231], a[140:143], v[248:251], a[228:231]\n"
    "ds_read_b64_tr_b16 a[162:163], v178 offset:22528\n"
    "v_cvt_pk_bf16_f32 v197, v130, v131\n"
    "v_mfma_f32_16x16x32_bf16 v[136:139], a[64:67], a[32:35], v[152:155]\n"
    "v_exp_f32 v132, v132\n"
    "v_mfma_f32_16x16x32_bf16 v[136:139], a[68:71], a[36:39], v[136:139]\n"
    "ds_read_b64_tr_b16 a[164:165], v179 offset:16384\n"
    "s_add_u32 s29, s85, 98304\n"
    "v_mfma_f32_16x16x32_bf16 v[136:139], a[72:75], a[40:43], v[136:139]\n"
    "v_exp_f32 v133, v133\n"
    "v_mfma_f32_16x16x32_bf16 v[136:139], a[76:79], a[44:47], v[136:139]\n"
    "ds_read_b64_tr_b16 a[166:167], v179 offset:18432\n"
    "v_cvt_pk_bf16_f32 v204, v132, v133\n"
    "v_mfma_f32_16x16x32_bf16 v[104:107], a[148:151], v[224:227], v[104:107]\n"
    "v_exp_f32 v134, v134\n"
    "v_mfma_f32_16x16x32_bf16 v[104:107], a[152:155], v[228:231], v[104:107]\n"
    "ds_read_b64_tr_b16 a[168:169], v179 offset:20480\n"
    "s_add_u32 m0, s78, 114688\n"
    "v_mfma_f32_16x16x32_bf16 v[108:111], a[152:155], v[236:239], v[108:111]\n"
    "v_exp_f32 v135, v135\n"
    "v_mfma_f32_16x16x32_bf16 v[108:111], a[148:151], v[232:235], v[108:111]\n"
    "ds_read_b64_tr_b16 a[170:171], v179 offset:22528\n"
    "v_cvt_pk_bf16_f32 v205, v134, v135\n"
    "v_mfma_f32_16x16x32_bf16 v[140:143], a[64:67], a[48:51], v[156:159]\n"
    "v_exp_f32 v136, v136\n"
    "v_mfma_f32_16x16x32_bf16 v[140:143], a[68:71], a[52:55], v[140:143]\n"
    "buffer_load_dwordx4 v37, s[48:51], s29 offen offset:0 lds\n"
    "v_mfma_f32_16x16x32_bf16 v[140:143], a[72:75], a[56:59], v[140:143]\n"
    "v_exp_f32 v137, v137\n"
    "v_mfma_f32_16x16x32_bf16 v[140:143], a[76:79], a[60:63], v[140:143]\n"
    "buffer_load_dwordx4 v38, s[48:51], s29 offen offset:1024 lds\n"
    "v_cvt_pk_bf16_f32 v212, v136, v137\n"
    "v_mfma_f32_16x16x32_bf16 a[232:235], a[148:151], v[240:243], a[232:235]\n"
    "v_exp_f32 v138, v138\n"
    "v_mfma_f32_16x16x32_bf16 a[232:235], a[152:155], v[244:247], a[232:235]\n"
    "buffer_load_dwordx4 v39, s[48:51], s29 offen offset:2048 lds\n"
    "v_mfma_f32_16x16x32_bf16 a[236:239], a[152:155], v[252:255], a[236:239]\n"
    "v_exp_f32 v139, v139\n"
    "v_mfma_f32_16x16x32_bf16 a[236:239], a[148:151], v[248:251], a[236:239]\n"
    "buffer_load_dwordx4 v40, s[48:51], s29 offen offset:3072 lds\n"
    "v_cvt_pk_bf16_f32 v213, v138, v139\n"
    "v_mfma_f32_16x16x32_bf16 a[180:183], v[60:63], v[240:243], a[180:183]\n"
    "v_mfma_f32_16x16x32_bf16 a[180:183], v[60:63], v[244:247], a[180:183]\n"
    "s_waitcnt vmcnt(8) lgkmcnt(0)\n"
    "s_barrier\n"
    "v_mfma_f32_16x16x32_bf16 v[128:131], a[80:83], a[0:3], v[144:147]\n"
    "v_exp_f32 v140, v140\n"
    "v_mfma_f32_16x16x32_bf16 v[128:131], a[84:87], a[4:7], v[128:131]\n"
    "ds_read_b128 a[64:67], v168 offset:0\n"
    "v_mfma_f32_16x16x32_bf16 v[128:131], a[88:91], a[8:11], v[128:131]\n"
    "v_exp_f32 v141, v141\n"
    "v_mfma_f32_16x16x32_bf16 v[128:131], a[92:95], a[12:15], v[128:131]\n"
    "ds_read_b128 a[68:71], v169 offset:0\n"
    "v_cvt_pk_bf16_f32 v220, v140, v141\n"
    "v_mfma_f32_16x16x32_bf16 v[112:115], a[156:159], v[224:227], v[112:115]\n"
    "v_exp_f32 v142, v142\n"
    "v_mfma_f32_16x16x32_bf16 v[112:115], a[160:163], v[228:231], v[112:115]\n"
    "ds_read_b128 a[72:75], v170 offset:0\n"
    "v_mfma_f32_16x16x32_bf16 v[116:119], a[160:163], v[236:239], v[116:119]\n"
    "v_exp_f32 v143, v143\n"
    "v_mfma_f32_16x16x32_bf16 v[116:119], a[156:159], v[232:235], v[116:119]\n"
    "ds_read_b128 a[76:79], v171 offset:0\n"
    "v_cvt_pk_bf16_f32 v221, v142, v143\n"
    "v_mfma_f32_16x16x32_bf16 v[132:135], a[80:83], a[16:19], v[148:151]\n"
    "v_exp_f32 v128, v128\n"
    "v_mfma_f32_16x16x32_bf16 v[132:135], a[84:87], a[20:23], v[132:135]\n"
    "ds_read_b64_tr_b16 a[140:141], v180 offset:16384\n"
    "v_mfma_f32_16x16x32_bf16 v[132:135], a[88:91], a[24:27], v[132:135]\n"
    "v_exp_f32 v129, v129\n"
    "v_mfma_f32_16x16x32_bf16 v[132:135], a[92:95], a[28:31], v[132:135]\n"
    "ds_read_b64_tr_b16 a[142:143], v180 offset:18432\n"
    "v_cvt_pk_bf16_f32 v198, v128, v129\n"
    "v_mfma_f32_16x16x32_bf16 a[240:243], a[156:159], v[240:243], a[240:243]\n"
    "v_exp_f32 v130, v130\n"
    "v_mfma_f32_16x16x32_bf16 a[240:243], a[160:163], v[244:247], a[240:243]\n"
    "ds_read_b64_tr_b16 a[144:145], v180 offset:20480\n"
    "s_waitcnt lgkmcnt(7)\n"
    "v_mfma_f32_16x16x32_bf16 a[244:247], a[160:163], v[252:255], a[244:247]\n"
    "v_exp_f32 v131, v131\n"
    "v_mfma_f32_16x16x32_bf16 a[244:247], a[156:159], v[248:251], a[244:247]\n"
    "ds_read_b64_tr_b16 a[146:147], v180 offset:22528\n"
    "v_cvt_pk_bf16_f32 v199, v130, v131\n"
    "v_mfma_f32_16x16x32_bf16 v[136:139], a[80:83], a[32:35], v[152:155]\n"
    "v_exp_f32 v132, v132\n"
    "v_mfma_f32_16x16x32_bf16 v[136:139], a[84:87], a[36:39], v[136:139]\n"
    "ds_read_b64_tr_b16 a[148:149], v181 offset:16384\n"
    "s_add_u32 s30, s84, 114688\n"
    "v_mfma_f32_16x16x32_bf16 v[136:139], a[88:91], a[40:43], v[136:139]\n"
    "v_exp_f32 v133, v133\n"
    "v_mfma_f32_16x16x32_bf16 v[136:139], a[92:95], a[44:47], v[136:139]\n"
    "ds_read_b64_tr_b16 a[150:151], v181 offset:18432\n"
    "v_cvt_pk_bf16_f32 v206, v132, v133\n"
    "v_mfma_f32_16x16x32_bf16 v[120:123], a[164:167], v[224:227], v[120:123]\n"
    "v_exp_f32 v134, v134\n"
    "v_mfma_f32_16x16x32_bf16 v[120:123], a[168:171], v[228:231], v[120:123]\n"
    "ds_read_b64_tr_b16 a[152:153], v181 offset:20480\n"
    "s_add_u32 m0, s78, 0\n"
    "v_mfma_f32_16x16x32_bf16 v[124:127], a[168:171], v[236:239], v[124:127]\n"
    "v_exp_f32 v135, v135\n"
    "v_mfma_f32_16x16x32_bf16 v[124:127], a[164:167], v[232:235], v[124:127]\n"
    "ds_read_b64_tr_b16 a[154:155], v181 offset:22528\n"
    "v_cvt_pk_bf16_f32 v207, v134, v135\n"
    "v_mfma_f32_16x16x32_bf16 v[140:143], a[80:83], a[48:51], v[156:159]\n"
    "v_exp_f32 v136, v136\n"
    "v_mfma_f32_16x16x32_bf16 v[140:143], a[84:87], a[52:55], v[140:143]\n"
    "buffer_load_dwordx4 v37, s[48:51], s30 offen offset:0 lds\n"
    "v_mfma_f32_16x16x32_bf16 v[140:143], a[88:91], a[56:59], v[140:143]\n"
    "v_exp_f32 v137, v137\n"
    "v_mfma_f32_16x16x32_bf16 v[140:143], a[92:95], a[60:63], v[140:143]\n"
    "buffer_load_dwordx4 v38, s[48:51], s30 offen offset:1024 lds\n"
    "v_cvt_pk_bf16_f32 v214, v136, v137\n"
    "v_mfma_f32_16x16x32_bf16 a[248:251], a[164:167], v[240:243], a[248:251]\n"
    "v_exp_f32 v138, v138\n"
    "v_mfma_f32_16x16x32_bf16 a[248:251], a[168:171], v[244:247], a[248:251]\n"
    "buffer_load_dwordx4 v39, s[48:51], s30 offen offset:2048 lds\n"
    "v_mfma_f32_16x16x32_bf16 a[252:255], a[168:171], v[252:255], a[252:255]\n"
    "v_exp_f32 v139, v139\n"
    "v_mfma_f32_16x16x32_bf16 a[252:255], a[164:167], v[248:251], a[252:255]\n"
    "buffer_load_dwordx4 v40, s[48:51], s30 offen offset:3072 lds\n"
    "v_cvt_pk_bf16_f32 v215, v138, v139\n"
    "v_mfma_f32_16x16x32_bf16 a[184:187], v[60:63], v[248:251], a[184:187]\n"
    "v_mfma_f32_16x16x32_bf16 a[184:187], v[60:63], v[252:255], a[184:187]\n"
    "s_waitcnt lgkmcnt(4)\n"
    "v_mfma_f32_16x16x32_bf16 v[128:131], a[64:67], a[0:3], v[144:147]\n"
    "v_exp_f32 v140, v140\n"
    "v_mfma_f32_16x16x32_bf16 v[128:131], a[68:71], a[4:7], v[128:131]\n"
    "ds_read_b128 a[80:83], v168 offset:4096\n"
    "v_mfma_f32_16x16x32_bf16 v[128:131], a[72:75], a[8:11], v[128:131]\n"
    "v_exp_f32 v141, v141\n"
    "v_mfma_f32_16x16x32_bf16 v[128:131], a[76:79], a[12:15], v[128:131]\n"
    "ds_read_b128 a[84:87], v169 offset:4096\n"
    "v_cvt_pk_bf16_f32 v222, v140, v141\n"
    "v_mfma_f32_16x16x32_bf16 v[64:67], a[140:143], v[192:195], v[64:67]\n"
    "v_exp_f32 v142, v142\n"
    "v_mfma_f32_16x16x32_bf16 v[64:67], a[144:147], v[196:199], v[64:67]\n"
    "ds_read_b128 a[88:91], v170 offset:4096\n"
    "v_mfma_f32_16x16x32_bf16 v[68:71], a[144:147], v[204:207], v[68:71]\n"
    "v_exp_f32 v143, v143\n"
    "v_mfma_f32_16x16x32_bf16 v[68:71], a[140:143], v[200:203], v[68:71]\n"
    "ds_read_b128 a[92:95], v171 offset:4096\n"
    "v_cvt_pk_bf16_f32 v223, v142, v143\n"
    "v_mfma_f32_16x16x32_bf16 v[132:135], a[64:67], a[16:19], v[148:151]\n"
    "v_exp_f32 v128, v128\n"
    "v_mfma_f32_16x16x32_bf16 v[132:135], a[68:71], a[20:23], v[132:135]\n"
    "ds_read_b64_tr_b16 a[156:157], v182 offset:16384\n"
    "v_mfma_f32_16x16x32_bf16 v[132:135], a[72:75], a[24:27], v[132:135]\n"
    "v_exp_f32 v129, v129\n"
    "v_mfma_f32_16x16x32_bf16 v[132:135], a[76:79], a[28:31], v[132:135]\n"
    "ds_read_b64_tr_b16 a[158:159], v182 offset:18432\n"
    "v_cvt_pk_bf16_f32 v224, v128, v129\n"
    "v_mfma_f32_16x16x32_bf16 a[192:195], a[140:143], v[208:211], a[192:195]\n"
    "v_exp_f32 v130, v130\n"
    "v_mfma_f32_16x16x32_bf16 a[192:195], a[144:147], v[212:215], a[192:195]\n"
    "ds_read_b64_tr_b16 a[160:161], v182 offset:20480\n"
    "s_waitcnt lgkmcnt(7)\n"
    "v_mfma_f32_16x16x32_bf16 a[196:199], a[144:147], v[220:223], a[196:199]\n"
    "v_exp_f32 v131, v131\n"
    "v_mfma_f32_16x16x32_bf16 a[196:199], a[140:143], v[216:219], a[196:199]\n"
    "ds_read_b64_tr_b16 a[162:163], v182 offset:22528\n"
    "v_cvt_pk_bf16_f32 v225, v130, v131\n"
    "v_mfma_f32_16x16x32_bf16 v[136:139], a[64:67], a[32:35], v[152:155]\n"
    "v_exp_f32 v132, v132\n"
    "v_mfma_f32_16x16x32_bf16 v[136:139], a[68:71], a[36:39], v[136:139]\n"
    "ds_read_b64_tr_b16 a[164:165], v183 offset:16384\n"
    "s_add_u32 s31, s85, 114688\n"
    "v_mfma_f32_16x16x32_bf16 v[136:139], a[72:75], a[40:43], v[136:139]\n"
    "v_exp_f32 v133, v133\n"
    "v_mfma_f32_16x16x32_bf16 v[136:139], a[76:79], a[44:47], v[136:139]\n"
    "ds_read_b64_tr_b16 a[166:167], v183 offset:18432\n"
    "v_cvt_pk_bf16_f32 v232, v132, v133\n"
    "v_mfma_f32_16x16x32_bf16 v[72:75], a[148:151], v[192:195], v[72:75]\n"
    "v_exp_f32 v134, v134\n"
    "v_mfma_f32_16x16x32_bf16 v[72:75], a[152:155], v[196:199], v[72:75]\n"
    "ds_read_b64_tr_b16 a[168:169], v183 offset:20480\n"
    "s_add_u32 m0, s78, 16384\n"
    "v_mfma_f32_16x16x32_bf16 v[76:79], a[152:155], v[204:207], v[76:79]\n"
    "v_exp_f32 v135, v135\n"
    "v_mfma_f32_16x16x32_bf16 v[76:79], a[148:151], v[200:203], v[76:79]\n"
    "ds_read_b64_tr_b16 a[170:171], v183 offset:22528\n"
    "v_cvt_pk_bf16_f32 v233, v134, v135\n"
    "v_mfma_f32_16x16x32_bf16 v[140:143], a[64:67], a[48:51], v[156:159]\n"
    "v_exp_f32 v136, v136\n"
    "v_mfma_f32_16x16x32_bf16 v[140:143], a[68:71], a[52:55], v[140:143]\n"
    "buffer_load_dwordx4 v37, s[48:51], s31 offen offset:0 lds\n"
    "v_mfma_f32_16x16x32_bf16 v[140:143], a[72:75], a[56:59], v[140:143]\n"
    "v_exp_f32 v137, v137\n"
    "v_mfma_f32_16x16x32_bf16 v[140:143], a[76:79], a[60:63], v[140:143]\n"
    "buffer_load_dwordx4 v38, s[48:51], s31 offen offset:1024 lds\n"
    "v_cvt_pk_bf16_f32 v240, v136, v137\n"
    "v_mfma_f32_16x16x32_bf16 a[200:203], a[148:151], v[208:211], a[200:203]\n"
    "v_exp_f32 v138, v138\n"
    "v_mfma_f32_16x16x32_bf16 a[200:203], a[152:155], v[212:215], a[200:203]\n"
    "buffer_load_dwordx4 v39, s[48:51], s31 offen offset:2048 lds\n"
    "v_mfma_f32_16x16x32_bf16 a[204:207], a[152:155], v[220:223], a[204:207]\n"
    "v_exp_f32 v139, v139\n"
    "v_mfma_f32_16x16x32_bf16 a[204:207], a[148:151], v[216:219], a[204:207]\n"
    "buffer_load_dwordx4 v40, s[48:51], s31 offen offset:3072 lds\n"
    "v_cvt_pk_bf16_f32 v241, v138, v139\n"
    "v_mfma_f32_16x16x32_bf16 a[172:175], v[60:63], v[192:195], a[172:175]\n"
    "v_mfma_f32_16x16x32_bf16 a[172:175], v[60:63], v[196:199], a[172:175]\n"
    "s_waitcnt vmcnt(8) lgkmcnt(0)\n"
    "s_barrier\n"
    "v_mfma_f32_16x16x32_bf16 v[128:131], a[80:83], a[0:3], v[144:147]\n"
    "v_exp_f32 v140, v140\n"
    "v_mfma_f32_16x16x32_bf16 v[128:131], a[84:87], a[4:7], v[128:131]\n"
    "ds_read_b128 a[64:67], v164 offset:32768\n"
    "v_mfma_f32_16x16x32_bf16 v[128:131], a[88:91], a[8:11], v[128:131]\n"
    "v_exp_f32 v141, v141\n"
    "v_mfma_f32_16x16x32_bf16 v[128:131], a[92:95], a[12:15], v[128:131]\n"
    "ds_read_b128 a[68:71], v165 offset:32768\n"
    "v_cvt_pk_bf16_f32 v248, v140, v141\n"
    "v_mfma_f32_16x16x32_bf16 v[80:83], a[156:159], v[192:195], v[80:83]\n"
    "v_exp_f32 v142, v142\n"
    "v_mfma_f32_16x16x32_bf16 v[80:83], a[160:163], v[196:199], v[80:83]\n"
    "ds_read_b128 a[72:75], v166 offset:32768\n"
    "v_mfma_f32_16x16x32_bf16 v[84:87], a[160:163], v[204:207], v[84:87]\n"
    "v_exp_f32 v143, v143\n"
    "v_mfma_f32_16x16x32_bf16 v[84:87], a[156:159], v[200:203], v[84:87]\n"
    "ds_read_b128 a[76:79], v167 offset:32768\n"
    "v_cvt_pk_bf16_f32 v249, v142, v143\n"
    "v_mfma_f32_16x16x32_bf16 v[132:135], a[80:83], a[16:19], v[148:151]\n"
    "v_exp_f32 v128, v128\n"
    "v_mfma_f32_16x16x32_bf16 v[132:135], a[84:87], a[20:23], v[132:135]\n"
    "ds_read_b64_tr_b16 a[140:141], v176 offset:49152\n"
    "v_mfma_f32_16x16x32_bf16 v[132:135], a[88:91], a[24:27], v[132:135]\n"
    "v_exp_f32 v129, v129\n"
    "v_mfma_f32_16x16x32_bf16 v[132:135], a[92:95], a[28:31], v[132:135]\n"
    "ds_read_b64_tr_b16 a[142:143], v176 offset:51200\n"
    "v_cvt_pk_bf16_f32 v226, v128, v129\n"
    "v_mfma_f32_16x16x32_bf16 a[208:211], a[156:159], v[208:211], a[208:211]\n"
    "v_exp_f32 v130, v130\n"
    "v_mfma_f32_16x16x32_bf16 a[208:211], a[160:163], v[212:215], a[208:211]\n"
    "ds_read_b64_tr_b16 a[144:145], v176 offset:53248\n"
    "s_waitcnt lgkmcnt(7)\n"
    "Lw8_%=:\n"
    "s_cbranch_execz Lret0_%=\n"
    "v_mfma_f32_16x16x32_bf16 a[212:215], a[160:163], v[220:223], a[212:215]\n"
    "v_exp_f32 v131, v131\n"
    "v_mfma_f32_16x16x32_bf16 a[212:215], a[156:159], v[216:219], a[212:215]\n"
    "ds_read_b64_tr_b16 a[146:147], v176 offset:55296\n"
    "v_cvt_pk_bf16_f32 v227, v130, v131\n"
    "v_mfma_f32_16x16x32_bf16 v[136:139], a[80:83], a[32:35], v[152:155]\n"
    "v_exp_f32 v132, v132\n"
    "v_mfma_f32_16x16x32_bf16 v[136:139], a[84:87], a[36:39], v[136:139]\n"
    "ds_read_b64_tr_b16 a[148:149], v177 offset:49152\n"
    "v_mfma_f32_16x16x32_bf16 v[136:139], a[88:91], a[40:43], v[136:139]\n"
    "v_exp_f32 v133, v133\n"
    "v_mfma_f32_16x16x32_bf16 v[136:139], a[92:95], a[44:47], v[136:139]\n"
    "ds_read_b64_tr_b16 a[150:151], v177 offset:51200\n"
    "s_bfe_u32 s28, s26, 0x40000\n"
    "v_cvt_pk_bf16_f32 v234, v132, v133\n"
    "v_mfma_f32_16x16x32_bf16 v[88:91], a[164:167], v[192:195], v[88:91]\n"
    "v_exp_f32 v134, v134\n"
    "v_mfma_f32_16x16x32_bf16 v[88:91], a[168:171], v[196:199], v[88:91]\n"
    "ds_read_b64_tr_b16 a[152:153], v177 offset:53248\n"
    "s_lshl_b32 s28, s28, 17\n"
    "v_mfma_f32_16x16x32_bf16 v[92:95], a[168:171], v[204:207], v[92:95]\n"
    "v_exp_f32 v135, v135\n"
    "v_mfma_f32_16x16x32_bf16 v[92:95], a[164:167], v[200:203], v[92:95]\n"
    "ds_read_b64_tr_b16 a[154:155], v177 offset:55296\n"
    "s_add_u32 m0, s78, 32768\n"
    "v_cvt_pk_bf16_f32 v235, v134, v135\n"
    "v_mfma_f32_16x16x32_bf16 v[140:143], a[80:83], a[48:51], v[156:159]\n"
    "v_exp_f32 v136, v136\n"
    "v_mfma_f32_16x16x32_bf16 v[140:143], a[84:87], a[52:55], v[140:143]\n"
    "buffer_load_dwordx4 v10, s[52:55], s28 offen offset:0 lds\n"
    "v_mfma_f32_16x16x32_bf16 v[140:143], a[88:91], a[56:59], v[140:143]\n"
    "v_exp_f32 v137, v137\n"
    "v_mfma_f32_16x16x32_bf16 v[140:143], a[92:95], a[60:63], v[140:143]\n"
    "buffer_load_dwordx4 v11, s[52:55], s28 offen offset:1024 lds\n"
    "v_cvt_pk_bf16_f32 v242, v136, v137\n"
    "v_mfma_f32_16x16x32_bf16 a[216:219], a[164:167], v[208:211], a[216:219]\n"
    "v_exp_f32 v138, v138\n"
    "v_mfma_f32_16x16x32_bf16 a[216:219], a[168:171], v[212:215], a[216:219]\n"
    "buffer_load_dwordx4 v12, s[52:55], s28 offen offset:2048 lds\n"
    "v_mfma_f32_16x16x32_bf16 a[220:223], a[168:171], v[220:223], a[220:223]\n"
    "v_exp_f32 v139, v139\n"
    "v_mfma_f32_16x16x32_bf16 a[220:223], a[164:167], v[216:219], a[220:223]\n"
    "buffer_load_dwordx4 v13, s[52:55], s28 offen offset:3072 lds\n"
    "v_cvt_pk_bf16_f32 v243, v138, v139\n"
    "v_mfma_f32_16x16x32_bf16 a[176:179], v[60:63], v[200:203], a[176:179]\n"
    "v_mfma_f32_16x16x32_bf16 a[176:179], v[60:63], v[204:207], a[176:179]\n"
    "s_waitcnt lgkmcnt(4)\n"
    "v_mfma_f32_16x16x32_bf16 v[128:131], a[64:67], a[0:3], v[144:147]\n"
    "v_exp_f32 v140, v140\n"
    "v_mfma_f32_16x16x32_bf16 v[128:131], a[68:71], a[4:7], v[128:131]\n"
    "ds_read_b128 a[80:83], v164 offset:36864\n"
    "v_mfma_f32_16x16x32_bf16 v[128:131], a[72:75], a[8:11], v[128:131]\n"
    "v_exp_f32 v141, v141\n"
    "v_mfma_f32_16x16x32_bf16 v[128:131], a[76:79], a[12:15], v[128:131]\n"
    "ds_read_b128 a[84:87], v165 offset:36864\n"
    "v_cvt_pk_bf16_f32 v250, v140, v141\n"
    "v_mfma_f32_16x16x32_bf16 v[96:99], a[140:143], v[192:195], v[96:99]\n"
    "v_exp_f32 v142, v142\n"
    "v_mfma_f32_16x16x32_bf16 v[96:99], a[144:147], v[196:199], v[96:99]\n"
    "ds_read_b128 a[88:91], v166 offset:36864\n"
    "v_mfma_f32_16x16x32_bf16 v[100:103], a[144:147], v[204:207], v[100:103]\n"
    "v_exp_f32 v143, v143\n"
    "v_mfma_f32_16x16x32_bf16 v[100:103], a[140:143], v[200:203], v[100:103]\n"
    "ds_read_b128 a[92:95], v167 offset:36864\n"
    "v_cvt_pk_bf16_f32 v251, v142, v143\n"
    "v_mfma_f32_16x16x32_bf16 v[132:135], a[64:67], a[16:19], v[148:151]\n"
    "v_exp_f32 v128, v128\n"
    "v_mfma_f32_16x16x32_bf16 v[132:135], a[68:71], a[20:23], v[132:135]\n"
    "ds_read_b64_tr_b16 a[156:157], v178 offset:49152\n"
    "v_mfma_f32_16x16x32_bf16 v[132:135], a[72:75], a[24:27], v[132:135]\n"
    "v_exp_f32 v129, v129\n"
    "v_mfma_f32_16x16x32_bf16 v[132:135], a[76:79], a[28:31], v[132:135]\n"
    "ds_read_b64_tr_b16 a[158:159], v178 offset:51200\n"
    "v_cvt_pk_bf16_f32 v228, v128, v129\n"
    "v_mfma_f32_16x16x32_bf16 a[224:227], a[140:143], v[208:211], a[224:227]\n"
    "v_exp_f32 v130, v130\n"
    "v_mfma_f32_16x16x32_bf16 a[224:227], a[144:147], v[212:215], a[224:227]\n"
    "ds_read_b64_tr_b16 a[160:161], v178 offset:53248\n"
    "s_waitcnt lgkmcnt(7)\n"
    "v_mfma_f32_16x16x32_bf16 a[228:231], a[144:147], v[220:223], a[228:231]\n"
    "v_exp_f32 v131, v131\n"
    "v_mfma_f32_16x16x32_bf16 a[228:231], a[140:143], v[216:219], a[228:231]\n"
    "ds_read_b64_tr_b16 a[162:163], v178 offset:55296\n"
    "v_cvt_pk_bf16_f32 v229, v130, v131\n"
    "v_mfma_f32_16x16x32_bf16 v[136:139], a[64:67], a[32:35], v[152:155]\n"
    "v_exp_f32 v132, v132\n"
    "v_mfma_f32_16x16x32_bf16 v[136:139], a[68:71], a[36:39], v[136:139]\n"
    "ds_read_b64_tr_b16 a[164:165], v179 offset:49152\n"
    "v_mfma_f32_16x16x32_bf16 v[136:139], a[72:75], a[40:43], v[136:139]\n"
    "v_exp_f32 v133, v133\n"
    "v_mfma_f32_16x16x32_bf16 v[136:139], a[76:79], a[44:47], v[136:139]\n"
    "ds_read_b64_tr_b16 a[166:167], v179 offset:51200\n"
    "s_bfe_u32 s29, s26, 0x40004\n"
    "v_cvt_pk_bf16_f32 v236, v132, v133\n"
    "v_mfma_f32_16x16x32_bf16 v[104:107], a[148:151], v[192:195], v[104:107]\n"
    "v_exp_f32 v134, v134\n"
    "v_mfma_f32_16x16x32_bf16 v[104:107], a[152:155], v[196:199], v[104:107]\n"
    "ds_read_b64_tr_b16 a[168:169], v179 offset:53248\n"
    "s_lshl_b32 s29, s29, 17\n"
    "v_mfma_f32_16x16x32_bf16 v[108:111], a[152:155], v[204:207], v[108:111]\n"
    "v_exp_f32 v135, v135\n"
    "v_mfma_f32_16x16x32_bf16 v[108:111], a[148:151], v[200:203], v[108:111]\n"
    "ds_read_b64_tr_b16 a[170:171], v179 offset:55296\n"
    "s_add_u32 m0, s78, 49152\n"
    "v_cvt_pk_bf16_f32 v237, v134, v135\n"
    "v_mfma_f32_16x16x32_bf16 v[140:143], a[64:67], a[48:51], v[156:159]\n"
    "v_exp_f32 v136, v136\n"
    "v_mfma_f32_16x16x32_bf16 v[140:143], a[68:71], a[52:55], v[140:143]\n"
    "buffer_load_dwordx4 v10, s[52:55], s29 offen offset:0 lds\n"
    "v_mfma_f32_16x16x32_bf16 v[140:143], a[72:75], a[56:59], v[140:143]\n"
    "v_exp_f32 v137, v137\n"
    "v_mfma_f32_16x16x32_bf16 v[140:143], a[76:79], a[60:63], v[140:143]\n"
    "buffer_load_dwordx4 v11, s[52:55], s29 offen offset:1024 lds\n"
    "v_cvt_pk_bf16_f32 v244, v136, v137\n"
    "v_mfma_f32_16x16x32_bf16 a[232:235], a[148:151], v[208:211], a[232:235]\n"
    "v_exp_f32 v138, v138\n"
    "v_mfma_f32_16x16x32_bf16 a[232:235], a[152:155], v[212:215], a[232:235]\n"
    "buffer_load_dwordx4 v12, s[52:55], s29 offen offset:2048 lds\n"
    "v_mfma_f32_16x16x32_bf16 a[236:239], a[152:155], v[220:223], a[236:239]\n"
    "v_exp_f32 v139, v139\n"
    "v_mfma_f32_16x16x32_bf16 a[236:239], a[148:151], v[216:219], a[236:239]\n"
    "buffer_load_dwordx4 v13, s[52:55], s29 offen offset:3072 lds\n"
    "v_cvt_pk_bf16_f32 v245, v138, v139\n"
    "v_mfma_f32_16x16x32_bf16 a[180:183], v[60:63], v[208:211], a[180:183]\n"
    "v_mfma_f32_16x16x32_bf16 a[180:183], v[60:63], v[212:215], a[180:183]\n"
    "s_waitcnt vmcnt(12) lgkmcnt(0)\n"
    "s_barrier\n"
    "v_mfma_f32_16x16x32_bf16 v[128:131], a[80:83], a[0:3], v[144:147]\n"
    "v_exp_f32 v140, v140\n"
    "v_mfma_f32_16x16x32_bf16 v[128:131], a[84:87], a[4:7], v[128:131]\n"
    "ds_read_b64_tr_b16 a[140:141], v172 offset:0\n"
    "v_mfma_f32_16x16x32_bf16 v[128:131], a[88:91], a[8:11], v[128:131]\n"
    "v_exp_f32 v141, v141\n"
    "v_mfma_f32_16x16x32_bf16 v[128:131], a[92:95], a[12:15], v[128:131]\n"
    "ds_read_b64_tr_b16 a[142:143], v172 offset:2048\n"
    "v_cvt_pk_bf16_f32 v252, v140, v141\n"
    "v_mfma_f32_16x16x32_bf16 v[112:115], a[156:159], v[192:195], v[112:115]\n"
    "v_exp_f32 v142, v142\n"
    "v_mfma_f32_16x16x32_bf16 v[112:115], a[160:163], v[196:199], v[112:115]\n"
    "ds_read_b64_tr_b16 a[144:145], v172 offset:4096\n"
    "v_mfma_f32_16x16x32_bf16 v[116:119], a[160:163], v[204:207], v[116:119]\n"
    "v_exp_f32 v143, v143\n"
    "v_mfma_f32_16x16x32_bf16 v[116:119], a[156:159], v[200:203], v[116:119]\n"
    "ds_read_b64_tr_b16 a[146:147], v172 offset:6144\n"
    "v_cvt_pk_bf16_f32 v253, v142, v143\n"
    "v_mfma_f32_16x16x32_bf16 v[132:135], a[80:83], a[16:19], v[148:151]\n"
    "v_exp_f32 v128, v128\n"
    "v_mfma_f32_16x16x32_bf16 v[132:135], a[84:87], a[20:23], v[132:135]\n"
    "v_mfma_f32_16x16x32_bf16 v[132:135], a[88:91], a[24:27], v[132:135]\n"
    "v_exp_f32 v129, v129\n"
    "v_mfma_f32_16x16x32_bf16 v[132:135], a[92:95], a[28:31], v[132:135]\n"
    "v_cvt_pk_bf16_f32 v230, v128, v129\n"
    "v_mfma_f32_16x16x32_bf16 a[240:243], a[156:159], v[208:211], a[240:243]\n"
    "v_exp_f32 v130, v130\n"
    "v_mfma_f32_16x16x32_bf16 a[240:243], a[160:163], v[212:215], a[240:243]\n"
    "s_waitcnt lgkmcnt(4)\n"
    "v_mfma_f32_16x16x32_bf16 a[244:247], a[160:163], v[220:223], a[244:247]\n"
    "v_exp_f32 v131, v131\n"
    "v_mfma_f32_16x16x32_bf16 a[244:247], a[156:159], v[216:219], a[244:247]\n"
    "v_cvt_pk_bf16_f32 v231, v130, v131\n"
    "v_mfma_f32_16x16x32_bf16 v[136:139], a[80:83], a[32:35], v[152:155]\n"
    "v_exp_f32 v132, v132\n"
    "v_mfma_f32_16x16x32_bf16 v[136:139], a[84:87], a[36:39], v[136:139]\n"
    "ds_read_b64_tr_b16 a[148:149], v173 offset:0\n"
    "v_mfma_f32_16x16x32_bf16 v[136:139], a[88:91], a[40:43], v[136:139]\n"
    "v_exp_f32 v133, v133\n"
    "v_mfma_f32_16x16x32_bf16 v[136:139], a[92:95], a[44:47], v[136:139]\n"
    "ds_read_b64_tr_b16 a[150:151], v173 offset:2048\n"
    "s_bfe_u32 s30, s26, 0x40008\n"
    "v_cvt_pk_bf16_f32 v238, v132, v133\n"
    "v_mfma_f32_16x16x32_bf16 v[120:123], a[164:167], v[192:195], v[120:123]\n"
    "v_exp_f32 v134, v134\n"
    "v_mfma_f32_16x16x32_bf16 v[120:123], a[168:171], v[196:199], v[120:123]\n"
    "ds_read_b64_tr_b16 a[152:153], v173 offset:4096\n"
    "s_lshl_b32 s30, s30, 17\n"
    "v_mfma_f32_16x16x32_bf16 v[124:127], a[168:171], v[204:207], v[124:127]\n"
    "v_exp_f32 v135, v135\n"
    "v_mfma_f32_16x16x32_bf16 v[124:127], a[164:167], v[200:203], v[124:127]\n"
    "ds_read_b64_tr_b16 a[154:155], v173 offset:6144\n"
    "s_add_u32 m0, s78, 98304\n"
    "v_cvt_pk_bf16_f32 v239, v134, v135\n"
    "v_mfma_f32_16x16x32_bf16 v[140:143], a[80:83], a[48:51], v[156:159]\n"
    "v_exp_f32 v136, v136\n"
    "v_mfma_f32_16x16x32_bf16 v[140:143], a[84:87], a[52:55], v[140:143]\n"
    "buffer_load_dwordx4 v10, s[52:55], s30 offen offset:0 lds\n"
    "v_mfma_f32_16x16x32_bf16 v[140:143], a[88:91], a[56:59], v[140:143]\n"
    "v_exp_f32 v137, v137\n"
    "v_mfma_f32_16x16x32_bf16 v[140:143], a[92:95], a[60:63], v[140:143]\n"
    "buffer_load_dwordx4 v11, s[52:55], s30 offen offset:1024 lds\n"
    "v_cvt_pk_bf16_f32 v246, v136, v137\n"
    "v_mfma_f32_16x16x32_bf16 a[248:251], a[164:167], v[208:211], a[248:251]\n"
    "v_exp_f32 v138, v138\n"
    "v_mfma_f32_16x16x32_bf16 a[248:251], a[168:171], v[212:215], a[248:251]\n"
    "buffer_load_dwordx4 v12, s[52:55], s30 offen offset:2048 lds\n"
    "v_mfma_f32_16x16x32_bf16 a[252:255], a[168:171], v[220:223], a[252:255]\n"
    "v_exp_f32 v139, v139\n"
    "v_mfma_f32_16x16x32_bf16 a[252:255], a[164:167], v[216:219], a[252:255]\n"
    "buffer_load_dwordx4 v13, s[52:55], s30 offen offset:3072 lds\n"
    "v_cvt_pk_bf16_f32 v247, v138, v139\n"
    "v_mfma_f32_16x16x32_bf16 a[184:187], v[60:63], v[216:219], a[184:187]\n"
    "v_mfma_f32_16x16x32_bf16 a[184:187], v[60:63], v[220:223], a[184:187]\n"
    "s_waitcnt lgkmcnt(4)\n"
    "v_mfma_f32_16x16x32_bf16 v[64:67], a[140:143], v[224:227], v[64:67]\n"
    "v_exp_f32 v140, v140\n"
    "v_mfma_f32_16x16x32_bf16 v[64:67], a[144:147], v[228:231], v[64:67]\n"
    "ds_read_b64_tr_b16 a[156:157], v174 offset:0\n"
    "v_mfma_f32_16x16x32_bf16 v[68:71], a[144:147], v[236:239], v[68:71]\n"
    "v_exp_f32 v141, v141\n"
    "v_mfma_f32_16x16x32_bf16 v[68:71], a[140:143], v[232:235], v[68:71]\n"
    "ds_read_b64_tr_b16 a[158:159], v174 offset:2048\n"
    "v_cvt_pk_bf16_f32 v254, v140, v141\n"
    "v_mfma_f32_16x16x32_bf16 a[172:175], v[60:63], v[224:227], a[172:175]\n"
    "v_exp_f32 v142, v142\n"
    "v_mfma_f32_16x16x32_bf16 a[172:175], v[60:63], v[228:231], a[172:175]\n"
    "ds_read_b64_tr_b16 a[160:161], v174 offset:4096\n"
    "v_mfma_f32_16x16x32_bf16 a[176:179], v[60:63], v[232:235], a[176:179]\n"
    "v_exp_f32 v143, v143\n"
    "v_mfma_f32_16x16x32_bf16 a[176:179], v[60:63], v[236:239], a[176:179]\n"
    "ds_read_b64_tr_b16 a[162:163], v174 offset:6144\n"
    "v_cvt_pk_bf16_f32 v255, v142, v143\n"
    "v_mfma_f32_16x16x32_bf16 a[192:195], a[140:143], v[240:243], a[192:195]\n"
    "v_mfma_f32_16x16x32_bf16 a[192:195], a[144:147], v[244:247], a[192:195]\n"
    "v_mfma_f32_16x16x32_bf16 a[196:199], a[144:147], v[252:255], a[196:199]\n"
    "v_mfma_f32_16x16x32_bf16 a[196:199], a[140:143], v[248:251], a[196:199]\n"
    "v_mfma_f32_16x16x32_bf16 a[180:183], v[60:63], v[240:243], a[180:183]\n"
    "v_mfma_f32_16x16x32_bf16 a[180:183], v[60:63], v[244:247], a[180:183]\n"
    "v_mfma_f32_16x16x32_bf16 a[184:187], v[60:63], v[248:251], a[184:187]\n"
    "v_mfma_f32_16x16x32_bf16 a[184:187], v[60:63], v[252:255], a[184:187]\n"
    "s_waitcnt lgkmcnt(4)\n"
    "s_nop 0\n"
    "ds_write_b128 v46, a[192:195] offset:0\n"
    "v_mfma_f32_16x16x32_bf16 v[72:75], a[148:151], v[224:227], v[72:75]\n"
    "ds_read_b64_tr_b16 a[164:165], v175 offset:0\n"
    "v_mfma_f32_16x16x32_bf16 v[72:75], a[152:155], v[228:231], v[72:75]\n"
    "ds_read_b64_tr_b16 a[166:167], v175 offset:2048\n"
    "v_mfma_f32_16x16x32_bf16 v[76:79], a[152:155], v[236:239], v[76:79]\n"
    "ds_read_b64_tr_b16 a[168:169], v175 offset:4096\n"
    "v_accvgpr_read_b32 v184, a172\n"
    "v_accvgpr_read_b32 v185, a176\n"
    "v_mfma_f32_16x16x32_bf16 v[76:79], a[148:151], v[232:235], v[76:79]\n"
    "ds_read_b64_tr_b16 a[170:171], v175 offset:6144\n"
    "v_mov_b32 v190, v152\n"
    "v_mov_b32 v191, v156\n"
    "v_mfma_f32_16x16x32_bf16 a[200:203], a[148:151], v[240:243], a[200:203]\n"
    "v_accvgpr_read_b32 v188, a180\n"
    "v_accvgpr_read_b32 v189, a184\n"
    "v_mfma_f32_16x16x32_bf16 a[200:203], a[152:155], v[244:247], a[200:203]\n"
    "ds_write_b128 v48, v[188:191]\n"
    "v_mfma_f32_16x16x32_bf16 a[204:207], a[152:155], v[252:255], a[204:207]\n"
    "v_add_f32 v6, v184, v185\n"
    "v_add_f32 v4, v188, v189\n"
    "v_add_f32 v4, v4, v6\n"
    "v_cmp_ngt_f32 vcc, s20, v4\n"
    "s_nop 0\n"
    "s_or_b32 s21, vcc_lo, vcc_hi\n"
    "v_mov_b32 v4, s21\n"
    "v_mfma_f32_16x16x32_bf16 a[204:207], a[148:151], v[248:251], a[204:207]\n"
    "ds_write_b32 v45, v4\n"
    "s_waitcnt vmcnt(12) lgkmcnt(0)\n"
    "s_barrier\n"
    "v_mfma_f32_16x16x32_bf16 v[80:83], a[156:159], v[224:227], v[80:83]\n"
    "ds_read_b64_tr_b16 a[140:141], v172 offset:16384\n"
    "v_mfma_f32_16x16x32_bf16 v[80:83], a[160:163], v[228:231], v[80:83]\n"
    "ds_read_b64_tr_b16 a[142:143], v172 offset:18432\n"
    "v_mfma_f32_16x16x32_bf16 v[84:87], a[160:163], v[236:239], v[84:87]\n"
    "ds_read_b64_tr_b16 a[144:145], v172 offset:20480\n"
    "v_mfma_f32_16x16x32_bf16 v[84:87], a[156:159], v[232:235], v[84:87]\n"
    "ds_read_b64_tr_b16 a[146:147], v172 offset:22528\n"
    "v_mfma_f32_16x16x32_bf16 a[208:211], a[156:159], v[240:243], a[208:211]\n"
    "ds_read_b128 v[128:131], v44\n"
    "v_mfma_f32_16x16x32_bf16 a[208:211], a[160:163], v[244:247], a[208:211]\n"
    "ds_read_b128 v[132:135], v49\n"
    "v_mfma_f32_16x16x32_bf16 a[212:215], a[160:163], v[252:255], a[212:215]\n"
    "ds_write_b128 v46, a[196:199] offset:1024\n"
    "v_mfma_f32_16x16x32_bf16 a[212:215], a[156:159], v[248:251], a[212:215]\n"
    "s_waitcnt lgkmcnt(9)\n"
    "ds_write_b128 v46, a[200:203] offset:2048\n"
    "v_mfma_f32_16x16x32_bf16 v[88:91], a[164:167], v[224:227], v[88:91]\n"
    "ds_read_b64_tr_b16 a[148:149], v173 offset:16384\n"
    "s_waitcnt lgkmcnt(3)\n"
    "v_mfma_f32_16x16x32_bf16 v[88:91], a[168:171], v[228:231], v[88:91]\n"
    "ds_read_b64_tr_b16 a[150:151], v173 offset:18432\n"
    "v_or3_b32 v4, v132, v133, v134\n"
    "v_or_b32 v4, v4, v135\n"
    "v_cmp_lt_u32 vcc, s94, v4\n"
    "v_mfma_f32_16x16x32_bf16 v[92:95], a[168:171], v[236:239], v[92:95]\n"
    "ds_read_b64_tr_b16 a[152:153], v173 offset:20480\n"
    "s_cbranch_vccnz Lredo_%=\n"
    "v_mfma_f32_16x16x32_bf16 v[92:95], a[164:167], v[232:235], v[92:95]\n"
    "ds_read_b64_tr_b16 a[154:155], v173 offset:22528\n"
    "v_min_f32 v136, v144, v130\n"
    "v_mfma_f32_16x16x32_bf16 a[216:219], a[164:167], v[240:243], a[216:219]\n"
    "ds_write_b128 v46, a[204:207] offset:3072\n"
    "v_sub_f32 v138, v136, v144\n"
    "v_mfma_f32_16x16x32_bf16 a[216:219], a[168:171], v[244:247], a[216:219]\n"
    "ds_write_b128 v46, a[208:211] offset:4096\n"
    "v_sub_f32 v140, v136, v130\n"
    "v_mfma_f32_16x16x32_bf16 a[220:223], a[168:171], v[252:255], a[220:223]\n"
    "ds_write_b128 v46, a[212:215] offset:5120\n"
    "v_min_f32 v137, v148, v131\n"
    "v_mfma_f32_16x16x32_bf16 a[220:223], a[164:167], v[248:251], a[220:223]\n"
    "s_waitcnt lgkmcnt(11)\n"
    "v_sub_f32 v139, v137, v148\n"
    "v_mfma_f32_16x16x32_bf16 v[96:99], a[140:143], v[224:227], v[96:99]\n"
    "ds_read_b64_tr_b16 a[156:157], v174 offset:16384\n"
    "v_sub_f32 v141, v137, v131\n"
    "v_mfma_f32_16x16x32_bf16 v[96:99], a[144:147], v[228:231], v[96:99]\n"
    "ds_read_b64_tr_b16 a[158:159], v174 offset:18432\n"
    "v_mfma_f32_16x16x32_bf16 v[100:103], a[144:147], v[236:239], v[100:103]\n"
    "ds_read_b64_tr_b16 a[160:161], v174 offset:20480\n"
    "v_mfma_f32_16x16x32_bf16 v[100:103], a[140:143], v[232:235], v[100:103]\n"
    "ds_read_b64_tr_b16 a[162:163], v174 offset:22528\n"
    "v_mfma_f32_16x16x32_bf16 a[224:227], a[140:143], v[240:243], a[224:227]\n"
    "ds_write_b128 v46, a[216:219] offset:6144\n"
    "v_mfma_f32_16x16x32_bf16 a[224:227], a[144:147], v[244:247], a[224:227]\n"
    "ds_write_b128 v46, a[220:223] offset:7168\n"
    "v_mfma_f32_16x16x32_bf16 a[228:231], a[144:147], v[252:255], a[228:231]\n"
    "v_exp_f32 v138, v138\n"
    "v_mfma_f32_16x16x32_bf16 a[228:231], a[140:143], v[248:251], a[228:231]\n"
    "s_waitcnt lgkmcnt(9)\n"
    "v_mfma_f32_16x16x32_bf16 v[104:107], a[148:151], v[224:227], v[104:107]\n"
    "ds_read_b64_tr_b16 a[164:165], v175 offset:16384\n"
    "v_mfma_f32_16x16x32_bf16 v[104:107], a[152:155], v[228:231], v[104:107]\n"
    "ds_read_b64_tr_b16 a[166:167], v175 offset:18432\n"
    "v_mfma_f32_16x16x32_bf16 v[108:111], a[152:155], v[236:239], v[108:111]\n"
    "ds_read_b64_tr_b16 a[168:169], v175 offset:20480\n"
    "v_mfma_f32_16x16x32_bf16 v[108:111], a[148:151], v[232:235], v[108:111]\n"
    "ds_read_b64_tr_b16 a[170:171], v175 offset:22528\n"
    "v_mfma_f32_16x16x32_bf16 a[232:235], a[148:151], v[240:243], a[232:235]\n"
    "ds_write_b128 v2, a[224:227] offset:0\n"
    "v_mfma_f32_16x16x32_bf16 a[232:235], a[152:155], v[244:247], a[232:235]\n"
    "ds_write_b128 v2, a[228:231] offset:1024\n"
    "v_mfma_f32_16x16x32_bf16 a[236:239], a[152:155], v[252:255], a[236:239]\n"
    "v_exp_f32 v139, v139\n"
    "v_mfma_f32_16x16x32_bf16 a[236:239], a[148:151], v[248:251], a[236:239]\n"
    "s_waitcnt lgkmcnt(8)\n"
    "s_waitcnt lgkmcnt(6)\n"
    "s_barrier\n"
    "v_mfma_f32_16x16x32_bf16 v[112:115], a[156:159], v[224:227], v[112:115]\n"
    "v_exp_f32 v140, v140\n"
    "v_mfma_f32_16x16x32_bf16 v[112:115], a[160:163], v[228:231], v[112:115]\n"
    "v_exp_f32 v141, v141\n"
    "v_mfma_f32_16x16x32_bf16 v[116:119], a[160:163], v[236:239], v[116:119]\n"
    "v_mul_f32 v184, v184, v138\n"
    "v_fmac_f32 v184, v128, v140\n"
    "v_mfma_f32_16x16x32_bf16 v[116:119], a[156:159], v[232:235], v[116:119]\n"
    "ds_write_b128 v2, a[232:235] offset:2048\n"
    "v_mul_f32 v185, v185, v139\n"
    "v_mfma_f32_16x16x32_bf16 a[240:243], a[156:159], v[240:243], a[240:243]\n"
    "v_fmac_f32 v185, v129, v141\n"
    "v_mfma_f32_16x16x32_bf16 a[240:243], a[160:163], v[244:247], a[240:243]\n"
    "ds_write_b128 v2, a[236:239] offset:3072\n"
    "v_mfma_f32_16x16x32_bf16 a[244:247], a[160:163], v[252:255], a[244:247]\n"
    "v_rcp_f32 v184, v184\n"
    "v_mfma_f32_16x16x32_bf16 a[244:247], a[156:159], v[248:251], a[244:247]\n"
    "s_waitcnt lgkmcnt(4)\n"
    "ds_read_b128 v[192:195], v47 offset:0\n"
    "v_mfma_f32_16x16x32_bf16 v[120:123], a[164:167], v[224:227], v[120:123]\n"
    "v_rcp_f32 v185, v185\n"
    "v_mfma_f32_16x16x32_bf16 v[120:123], a[168:171], v[228:231], v[120:123]\n"
    "v_mul_f32 v152, v138, v184\n"
    "v_mul_f32 v156, v140, v184\n"
    "v_mfma_f32_16x16x32_bf16 v[124:127], a[168:171], v[236:239], v[124:127]\n"
    "v_mul_f32 v154, v139, v185\n"
    "v_mul_f32 v158, v141, v185\n"
    "v_mfma_f32_16x16x32_bf16 v[124:127], a[164:167], v[232:235], v[124:127]\n"
    "ds_write_b128 v2, a[240:243] offset:4096\n"
    "v_mfma_f32_16x16x32_bf16 a[248:251], a[164:167], v[240:243], a[248:251]\n"
    "ds_read_b128 v[196:199], v47 offset:1024\n"
    "v_mfma_f32_16x16x32_bf16 a[248:251], a[168:171], v[244:247], a[248:251]\n"
    "ds_write_b128 v2, a[244:247] offset:5120\n"
    "v_mfma_f32_16x16x32_bf16 a[252:255], a[168:171], v[252:255], a[252:255]\n"
    "ds_read_b128 v[200:203], v47 offset:2048\n"
    "v_mfma_f32_16x16x32_bf16 a[252:255], a[164:167], v[248:251], a[252:255]\n"
    "ds_read_b128 v[204:207], v47 offset:3072\n"
    "ds_read_b128 v[208:211], v47 offset:4096\n"
    "ds_read_b128 v[212:215], v47 offset:5120\n"
    "ds_read_b128 v[216:219], v47 offset:6144\n"
    "ds_read_b128 v[220:223], v47 offset:7168\n"
    "ds_write_b128 v2, a[248:251] offset:6144\n"
    "s_nop 1\n"
    "ds_write_b128 v2, a[252:255] offset:7168\n"
    "s_waitcnt lgkmcnt(6)\n"
    "v_pk_mul_f32 v[64:65], v[64:65], v[152:153] op_sel_hi:[1,0]\n"
    "v_pk_mul_f32 v[66:67], v[66:67], v[152:153] op_sel_hi:[1,0]\n"
    "v_pk_fma_f32 v[64:65], v[192:193], v[156:157], v[64:65] op_sel_hi:[1,0,1]\n"
    "v_pk_fma_f32 v[66:67], v[194:195], v[156:157], v[66:67] op_sel_hi:[1,0,1]\n"
    "v_cvt_pk_bf16_f32 v128, v64, v65\n"
    "v_cvt_pk_bf16_f32 v129, v66, v67\n"
    "v_pk_mul_f32 v[72:73], v[72:73], v[152:153] op_sel_hi:[1,0]\n"
    "v_pk_mul_f32 v[74:75], v[74:75], v[152:153] op_sel_hi:[1,0]\n"
    "v_pk_fma_f32 v[72:73], v[200:201], v[156:157], v[72:73] op_sel_hi:[1,0,1]\n"
    "v_pk_fma_f32 v[74:75], v[202:203], v[156:157], v[74:75] op_sel_hi:[1,0,1]\n"
    "v_cvt_pk_bf16_f32 v130, v72, v73\n"
    "v_cvt_pk_bf16_f32 v131, v74, v75\n"
    "buffer_store_dwordx4 v[128:131], v41, s[56:59], s64 offen offset:0 sc1\n"
    "v_pk_mul_f32 v[68:69], v[68:69], v[154:155] op_sel_hi:[1,0]\n"
    "v_pk_mul_f32 v[70:71], v[70:71], v[154:155] op_sel_hi:[1,0]\n"
    "v_pk_fma_f32 v[68:69], v[196:197], v[158:159], v[68:69] op_sel_hi:[1,0,1]\n"
    "v_pk_fma_f32 v[70:71], v[198:199], v[158:159], v[70:71] op_sel_hi:[1,0,1]\n"
    "v_cvt_pk_bf16_f32 v132, v68, v69\n"
    "v_cvt_pk_bf16_f32 v133, v70, v71\n"
    "v_pk_mul_f32 v[76:77], v[76:77], v[154:155] op_sel_hi:[1,0]\n"
    "v_pk_mul_f32 v[78:79], v[78:79], v[154:155] op_sel_hi:[1,0]\n"
    "v_pk_fma_f32 v[76:77], v[204:205], v[158:159], v[76:77] op_sel_hi:[1,0,1]\n"
    "v_pk_fma_f32 v[78:79], v[206:207], v[158:159], v[78:79] op_sel_hi:[1,0,1]\n"
    "v_cvt_pk_bf16_f32 v134, v76, v77\n"
    "v_cvt_pk_bf16_f32 v135, v78, v79\n"
    "buffer_store_dwordx4 v[132:135], v41, s[56:59], s64 offen offset:1024 sc1\n"
    "s_waitcnt vmcnt(2) lgkmcnt(0)\n"
    "s_barrier\n"
    "ds_read_b128 v[224:227], v3 offset:0\n"
    "ds_read_b128 v[228:231], v3 offset:1024\n"
    "ds_read_b128 v[232:235], v3 offset:2048\n"
    "ds_read_b128 v[236:239], v3 offset:3072\n"
    "ds_read_b128 v[240:243], v3 offset:4096\n"
    "ds_read_b128 v[244:247], v3 offset:5120\n"
    "ds_read_b128 v[248:251], v3 offset:6144\n"
    "ds_read_b128 v[252:255], v3 offset:7168\n"
    "v_pk_mul_f32 v[80:81], v[80:81], v[152:153] op_sel_hi:[1,0]\n"
    "v_pk_mul_f32 v[82:83], v[82:83], v[152:153] op_sel_hi:[1,0]\n"
    "v_pk_fma_f32 v[80:81], v[208:209], v[156:157], v[80:81] op_sel_hi:[1,0,1]\n"
    "v_pk_fma_f32 v[82:83], v[210:211], v[156:157], v[82:83] op_sel_hi:[1,0,1]\n"
    "v_cvt_pk_bf16_f32 v136, v80, v81\n"
    "v_cvt_pk_bf16_f32 v137, v82, v83\n"
    "v_pk_mul_f32 v[88:89], v[88:89], v[152:153] op_sel_hi:[1,0]\n"
    "v_pk_mul_f32 v[90:91], v[90:91], v[152:153] op_sel_hi:[1,0]\n"
    "v_pk_fma_f32 v[88:89], v[216:217], v[156:157], v[88:89] op_sel_hi:[1,0,1]\n"
    "v_pk_fma_f32 v[90:91], v[218:219], v[156:157], v[90:91] op_sel_hi:[1,0,1]\n"
    "v_cvt_pk_bf16_f32 v138, v88, v89\n"
    "v_cvt_pk_bf16_f32 v139, v90, v91\n"
    "buffer_store_dwordx4 v[136:139], v41, s[56:59], s64 offen offset:2048 sc1\n"
    "v_pk_mul_f32 v[84:85], v[84:85], v[154:155] op_sel_hi:[1,0]\n"
    "v_pk_mul_f32 v[86:87], v[86:87], v[154:155] op_sel_hi:[1,0]\n"
    "v_pk_fma_f32 v[84:85], v[212:213], v[158:159], v[84:85] op_sel_hi:[1,0,1]\n"
    "v_pk_fma_f32 v[86:87], v[214:215], v[158:159], v[86:87] op_sel_hi:[1,0,1]\n"
    "v_cvt_pk_bf16_f32 v140, v84, v85\n"
    "v_cvt_pk_bf16_f32 v141, v86, v87\n"
    "v_pk_mul_f32 v[92:93], v[92:93], v[154:155] op_sel_hi:[1,0]\n"
    "v_pk_mul_f32 v[94:95], v[94:95], v[154:155] op_sel_hi:[1,0]\n"
    "v_pk_fma_f32 v[92:93], v[220:221], v[158:159], v[92:93] op_sel_hi:[1,0,1]\n"
    "v_pk_fma_f32 v[94:95], v[222:223], v[158:159], v[94:95] op_sel_hi:[1,0,1]\n"
    "v_cvt_pk_bf16_f32 v142, v92, v93\n"
    "v_cvt_pk_bf16_f32 v143, v94, v95\n"
    "buffer_store_dwordx4 v[140:143], v41, s[56:59], s64 offen offset:3072 sc1\n"
    "s_waitcnt lgkmcnt(4)\n"
    "v_pk_mul_f32 v[96:97], v[96:97], v[152:153] op_sel_hi:[1,0]\n"
    "v_pk_mul_f32 v[98:99], v[98:99], v[152:153] op_sel_hi:[1,0]\n"
    "v_pk_fma_f32 v[96:97], v[224:225], v[156:157], v[96:97] op_sel_hi:[1,0,1]\n"
    "v_pk_fma_f32 v[98:99], v[226:227], v[156:157], v[98:99] op_sel_hi:[1,0,1]\n"
    "v_cvt_pk_bf16_f32 v128, v96, v97\n"
    "v_cvt_pk_bf16_f32 v129, v98, v99\n"
    "v_pk_mul_f32 v[104:105], v[104:105], v[152:153] op_sel_hi:[1,0]\n"
    "v_pk_mul_f32 v[106:107], v[106:107], v[152:153] op_sel_hi:[1,0]\n"
    "v_pk_fma_f32 v[104:105], v[232:233], v[156:157], v[104:105] op_sel_hi:[1,0,1]\n"
    "v_pk_fma_f32 v[106:107], v[234:235], v[156:157], v[106:107] op_sel_hi:[1,0,1]\n"
    "v_cvt_pk_bf16_f32 v130, v104, v105\n"
    "v_cvt_pk_bf16_f32 v131, v106, v107\n"
    "buffer_store_dwordx4 v[128:131], v41, s[56:59], s65 offen offset:0 sc1\n"
    "v_pk_mul_f32 v[100:101], v[100:101], v[154:155] op_sel_hi:[1,0]\n"
    "v_pk_mul_f32 v[102:103], v[102:103], v[154:155] op_sel_hi:[1,0]\n"
    "v_pk_fma_f32 v[100:101], v[228:229], v[158:159], v[100:101] op_sel_hi:[1,0,1]\n"
    "v_pk_fma_f32 v[102:103], v[230:231], v[158:159], v[102:103] op_sel_hi:[1,0,1]\n"
    "v_cvt_pk_bf16_f32 v132, v100, v101\n"
    "v_cvt_pk_bf16_f32 v133, v102, v103\n"
    "v_pk_mul_f32 v[108:109], v[108:109], v[154:155] op_sel_hi:[1,0]\n"
    "v_pk_mul_f32 v[110:111], v[110:111], v[154:155] op_sel_hi:[1,0]\n"
    "v_pk_fma_f32 v[108:109], v[236:237], v[158:159], v[108:109] op_sel_hi:[1,0,1]\n"
    "v_pk_fma_f32 v[110:111], v[238:239], v[158:159], v[110:111] op_sel_hi:[1,0,1]\n"
    "v_cvt_pk_bf16_f32 v134, v108, v109\n"
    "v_cvt_pk_bf16_f32 v135, v110, v111\n"
    "buffer_store_dwordx4 v[132:135], v41, s[56:59], s65 offen offset:1024 sc1\n"
    "s_waitcnt lgkmcnt(0)\n"
    "v_pk_mul_f32 v[112:113], v[112:113], v[152:153] op_sel_hi:[1,0]\n"
    "v_pk_mul_f32 v[114:115], v[114:115], v[152:153] op_sel_hi:[1,0]\n"
    "v_pk_fma_f32 v[112:113], v[240:241], v[156:157], v[112:113] op_sel_hi:[1,0,1]\n"
    "v_pk_fma_f32 v[114:115], v[242:243], v[156:157], v[114:115] op_sel_hi:[1,0,1]\n"
    "v_cvt_pk_bf16_f32 v136, v112, v113\n"
    "v_cvt_pk_bf16_f32 v137, v114, v115\n"
    "v_pk_mul_f32 v[120:121], v[120:121], v[152:153] op_sel_hi:[1,0]\n"
    "v_pk_mul_f32 v[122:123], v[122:123], v[152:153] op_sel_hi:[1,0]\n"
    "v_pk_fma_f32 v[120:121], v[248:249], v[156:157], v[120:121] op_sel_hi:[1,0,1]\n"
    "v_pk_fma_f32 v[122:123], v[250:251], v[156:157], v[122:123] op_sel_hi:[1,0,1]\n"
    "v_cvt_pk_bf16_f32 v138, v120, v121\n"
    "v_cvt_pk_bf16_f32 v139, v122, v123\n"
    "buffer_store_dwordx4 v[136:139], v41, s[56:59], s65 offen offset:2048 sc1\n"
    "v_pk_mul_f32 v[116:117], v[116:117], v[154:155] op_sel_hi:[1,0]\n"
    "v_pk_mul_f32 v[118:119], v[118:119], v[154:155] op_sel_hi:[1,0]\n"
    "v_pk_fma_f32 v[116:117], v[244:245], v[158:159], v[116:117] op_sel_hi:[1,0,1]\n"
    "v_pk_fma_f32 v[118:119], v[246:247], v[158:159], v[118:119] op_sel_hi:[1,0,1]\n"
    "v_cvt_pk_bf16_f32 v140, v116, v117\n"
    "v_cvt_pk_bf16_f32 v141, v118, v119\n"
    "v_pk_mul_f32 v[124:125], v[124:125], v[154:155] op_sel_hi:[1,0]\n"
    "v_pk_mul_f32 v[126:127], v[126:127], v[154:155] op_sel_hi:[1,0]\n"
    "v_pk_fma_f32 v[124:125], v[252:253], v[158:159], v[124:125] op_sel_hi:[1,0,1]\n"
    "v_pk_fma_f32 v[126:127], v[254:255], v[158:159], v[126:127] op_sel_hi:[1,0,1]\n"
    "v_cvt_pk_bf16_f32 v142, v124, v125\n"
    "v_cvt_pk_bf16_f32 v143, v126, v127\n"
    "buffer_store_dwordx4 v[140:143], v41, s[56:59], s65 offen offset:3072 sc1\n"
    "s_bfe_u32 s31, s26, 0x4000c\n"
    "s_lshl_b32 s31, s31, 17\n"
    "s_add_u32 m0, s78, 114688\n"
    "s_nop 0\n"
    "buffer_load_dwordx4 v10, s[52:55], s31 offen offset:0 lds\n"
    "buffer_load_dwordx4 v11, s[52:55], s31 offen offset:1024 lds\n"
    "buffer_load_dwordx4 v12, s[52:55], s31 offen offset:2048 lds\n"
    "buffer_load_dwordx4 v13, s[52:55], s31 offen offset:3072 lds\n"
    "s_bfe_u32 s28, s26, 0x40010\n"
    "s_lshl_b32 s28, s28, 17\n"
    "s_add_u32 m0, s78, 16384\n"
    "s_nop 0\n"
    "buffer_load_dwordx4 v10, s[52:55], s28 offen offset:0 lds\n"
    "buffer_load_dwordx4 v11, s[52:55], s28 offen offset:1024 lds\n"
    "buffer_load_dwordx4 v12, s[52:55], s28 offen offset:2048 lds\n"
    "buffer_load_dwordx4 v13, s[52:55], s28 offen offset:3072 lds\n"
    "s_bfe_u32 s29, s26, 0x40014\n"
    "s_lshl_b32 s29, s29, 17\n"
    "s_add_u32 m0, s78, 147456\n"
    "s_nop 0\n"
    "buffer_load_dwordx4 v10, s[52:55], s29 offen offset:0 lds\n"
    "buffer_load_dwordx4 v11, s[52:55], s29 offen offset:1024 lds\n"
    "buffer_load_dwordx4 v12, s[52:55], s29 offen offset:2048 lds\n"
    "buffer_load_dwordx4 v13, s[52:55], s29 offen offset:3072 lds\n"
    "s_bfe_u32 s30, s26, 0x40018\n"
    "s_lshl_b32 s30, s30, 17\n"
    "s_add_u32 m0, s78, 65536\n"
    "s_nop 0\n"
    "buffer_load_dwordx4 v10, s[52:55], s30 offen offset:0 lds\n"
    "buffer_load_dwordx4 v11, s[52:55], s30 offen offset:1024 lds\n"
    "buffer_load_dwordx4 v12, s[52:55], s30 offen offset:2048 lds\n"
    "buffer_load_dwordx4 v13, s[52:55], s30 offen offset:3072 lds\n"
    "ds_read_b64_tr_b16 a[0:1], v21 offset:32768\n"
    "ds_read_b64_tr_b16 a[2:3], v21 offset:36864\n"
    "ds_read_b64_tr_b16 a[4:5], v21 offset:40960\n"
    "ds_read_b64_tr_b16 a[6:7], v21 offset:45056\n"
    "ds_read_b64_tr_b16 a[8:9], v22 offset:32768\n"
    "ds_read_b64_tr_b16 a[10:11], v22 offset:36864\n"
    "ds_read_b64_tr_b16 a[12:13], v22 offset:40960\n"
    "ds_read_b64_tr_b16 a[14:15], v22 offset:45056\n"
    "ds_read_b64_tr_b16 a[16:17], v21 offset:49152\n"
    "ds_read_b64_tr_b16 a[18:19], v21 offset:53248\n"
    "ds_read_b64_tr_b16 a[20:21], v21 offset:57344\n"
    "ds_read_b64_tr_b16 a[22:23], v21 offset:61440\n"
    "ds_read_b64_tr_b16 a[24:25], v22 offset:49152\n"
    "ds_read_b64_tr_b16 a[26:27], v22 offset:53248\n"
    "ds_read_b64_tr_b16 a[28:29], v22 offset:57344\n"
    "ds_read_b64_tr_b16 a[30:31], v22 offset:61440\n"
    "ds_read_b64_tr_b16 a[32:33], v23 offset:32768\n"
    "ds_read_b64_tr_b16 a[34:35], v23 offset:36864\n"
    "ds_read_b64_tr_b16 a[36:37], v23 offset:40960\n"
    "ds_read_b64_tr_b16 a[38:39], v23 offset:45056\n"
    "ds_read_b64_tr_b16 a[40:41], v24 offset:32768\n"
    "ds_read_b64_tr_b16 a[42:43], v24 offset:36864\n"
    "ds_read_b64_tr_b16 a[44:45], v24 offset:40960\n"
    "ds_read_b64_tr_b16 a[46:47], v24 offset:45056\n"
    "s_waitcnt lgkmcnt(0)\n"
    "s_barrier\n"
    "s_waitcnt vmcnt(16)\n"
    "buffer_store_dwordx4 v[56:59], v42, s[56:59], 0 offen sc1\n"
    "s_bfe_u32 s31, s26, 0x4001c\n"
    "s_lshl_b32 s31, s31, 17\n"
    "s_add_u32 m0, s78, 81920\n"
    "s_nop 0\n"
    "buffer_load_dwordx4 v10, s[52:55], s31 offen offset:0 lds\n"
    "buffer_load_dwordx4 v11, s[52:55], s31 offen offset:1024 lds\n"
    "buffer_load_dwordx4 v12, s[52:55], s31 offen offset:2048 lds\n"
    "buffer_load_dwordx4 v13, s[52:55], s31 offen offset:3072 lds\n"
    "s_bfe_u32 s28, s27, 0x40000\n"
    "s_lshl_b32 s28, s28, 17\n"
    "s_add_u32 m0, s78, 131072\n"
    "s_nop 0\n"
    "Lw9_%=:\n"
    "s_cbranch_execz Lw10_%=\n"
    "buffer_load_dwordx4 v10, s[52:55], s28 offen offset:0 lds\n"
    "buffer_load_dwordx4 v11, s[52:55], s28 offen offset:1024 lds\n"
    "buffer_load_dwordx4 v12, s[52:55], s28 offen offset:2048 lds\n"
    "buffer_load_dwordx4 v13, s[52:55], s28 offen offset:3072 lds\n"
    "s_bfe_u32 s29, s27, 0x40004\n"
    "s_lshl_b32 s29, s29, 17\n"
    "s_add_u32 m0, s78, 0\n"
    "s_nop 0\n"
    "buffer_load_dwordx4 v10, s[52:55], s29 offen offset:0 lds\n"
    "buffer_load_dwordx4 v11, s[52:55], s29 offen offset:1024 lds\n"
    "buffer_load_dwordx4 v12, s[52:55], s29 offen offset:2048 lds\n"
    "buffer_load_dwordx4 v13, s[52:55], s29 offen offset:3072 lds\n"
    "s_mov_b32 s93, 1048576\n"
    "s_mov_b64 exec, 255\n"
    "Lpo_%=:\n"
    "buffer_load_dwordx2 v[4:5], v43, s[56:59], 0 offen sc1\n"
    "s_sub_u32 s93, s93, 1\n"
    "s_cbranch_scc1 Lpod_%=\n"
    "s_waitcnt vmcnt(0)\n"
    "v_cmp_ne_u64 vcc, s[24:25], v[4:5]\n"
    "s_cbranch_vccnz Lpo_%=\n"
    "Lpod_%=:\n"
    "s_waitcnt vmcnt(0)\n"
    "s_mov_b64 exec, -1\n"
    "s_waitcnt lgkmcnt(0)\n"
    "s_barrier\n"
    "s_bfe_u32 s30, s26, 0x40000\n"
    "s_lshl_b32 s30, s30, 14\n"
    "s_add_u32 s30, s30, s92\n"
    "s_add_u32 m0, s79, 32768\n"
    "s_nop 0\n"
    "buffer_load_dwordx4 v14, s[56:59], s30 offen offset:0 sc1 lds\n"
    "buffer_load_dwordx4 v15, s[56:59], s30 offen offset:1024 sc1 lds\n"
    "buffer_load_dwordx4 v16, s[56:59], s30 offen offset:2048 sc1 lds\n"
    "s_bfe_u32 s31, s26, 0x40004\n"
    "s_lshl_b32 s31, s31, 14\n"
    "s_add_u32 s31, s31, s92\n"
    "s_add_u32 m0, s79, 49152\n"
    "s_nop 0\n"
    "buffer_load_dwordx4 v14, s[56:59], s31 offen offset:0 sc1 lds\n"
    "buffer_load_dwordx4 v15, s[56:59], s31 offen offset:1024 sc1 lds\n"
    "buffer_load_dwordx4 v16, s[56:59], s31 offen offset:2048 sc1 lds\n"
    "ds_read_b64_tr_b16 a[48:49], v23 offset:49152\n"
    "ds_read_b64_tr_b16 a[50:51], v23 offset:53248\n"
    "ds_read_b64_tr_b16 a[52:53], v23 offset:57344\n"
    "ds_read_b64_tr_b16 a[54:55], v23 offset:61440\n"
    "ds_read_b64_tr_b16 a[56:57], v24 offset:49152\n"
    "ds_read_b64_tr_b16 a[58:59], v24 offset:53248\n"
    "ds_read_b64_tr_b16 a[60:61], v24 offset:57344\n"
    "ds_read_b64_tr_b16 a[62:63], v24 offset:61440\n"
    "ds_read_b64_tr_b16 a[64:65], v21 offset:16384\n"
    "ds_read_b64_tr_b16 a[66:67], v21 offset:20480\n"
    "ds_read_b64_tr_b16 a[68:69], v21 offset:24576\n"
    "ds_read_b64_tr_b16 a[70:71], v21 offset:28672\n"
    "ds_read_b64_tr_b16 a[72:73], v22 offset:16384\n"
    "ds_read_b64_tr_b16 a[74:75], v22 offset:20480\n"
    "ds_read_b64_tr_b16 a[76:77], v22 offset:24576\n"
    "ds_read_b64_tr_b16 a[78:79], v22 offset:28672\n"
    "ds_read_b64_tr_b16 a[80:81], v25 offset:16384\n"
    "ds_read_b64_tr_b16 a[82:83], v25 offset:20480\n"
    "ds_read_b64_tr_b16 a[84:85], v25 offset:24576\n"
    "ds_read_b64_tr_b16 a[86:87], v25 offset:28672\n"
    "ds_read_b64_tr_b16 a[88:89], v26 offset:16384\n"
    "ds_read_b64_tr_b16 a[90:91], v26 offset:20480\n"
    "ds_read_b64_tr_b16 a[92:93], v26 offset:24576\n"
    "ds_read_b64_tr_b16 a[94:95], v26 offset:28672\n"
    "ds_read_b64_tr_b16 a[96:97], v23 offset:0\n"
    "ds_read_b64_tr_b16 a[98:99], v23 offset:4096\n"
    "ds_read_b64_tr_b16 a[100:101], v23 offset:8192\n"
    "ds_read_b64_tr_b16 a[102:103], v23 offset:12288\n"
    "ds_read_b64_tr_b16 a[104:105], v24 offset:0\n"
    "ds_read_b64_tr_b16 a[106:107], v24 offset:4096\n"
    "ds_read_b64_tr_b16 a[108:109], v24 offset:8192\n"
    "ds_read_b64_tr_b16 a[110:111], v24 offset:12288\n"
    "ds_read_b64_tr_b16 a[112:113], v23 offset:16384\n"
    "ds_read_b64_tr_b16 a[114:115], v23 offset:20480\n"
    "ds_read_b64_tr_b16 a[116:117], v23 offset:24576\n"
    "ds_read_b64_tr_b16 a[118:119], v23 offset:28672\n"
    "ds_read_b64_tr_b16 a[120:121], v24 offset:16384\n"
    "ds_read_b64_tr_b16 a[122:123], v24 offset:20480\n"
    "ds_read_b64_tr_b16 a[124:125], v24 offset:24576\n"
    "ds_read_b64_tr_b16 a[126:127], v24 offset:28672\n"
    "ds_read_b64_tr_b16 a[128:129], v25 offset:0\n"
    "ds_read_b64_tr_b16 a[130:131], v25 offset:4096\n"
    "ds_read_b64_tr_b16 a[132:133], v25 offset:8192\n"
    "ds_read_b64_tr_b16 a[134:135], v25 offset:12288\n"
    "ds_read_b64_tr_b16 a[136:137], v26 offset:0\n"
    "ds_read_b64_tr_b16 a[138:139], v26 offset:4096\n"
    "ds_read_b64_tr_b16 a[140:141], v26 offset:8192\n"
    "ds_read_b64_tr_b16 a[142:143], v26 offset:12288\n"
    "ds_read_b64_tr_b16 a[144:145], v21 offset:0\n"
    "ds_read_b64_tr_b16 a[146:147], v21 offset:4096\n"
    "ds_read_b64_tr_b16 a[148:149], v21 offset:8192\n"
    "ds_read_b64_tr_b16 a[150:151], v21 offset:12288\n"
    "ds_read_b64_tr_b16 a[152:153], v22 offset:0\n"
    "ds_read_b64_tr_b16 a[154:155], v22 offset:4096\n"
    "ds_read_b64_tr_b16 a[156:157], v22 offset:8192\n"
    "ds_read_b64_tr_b16 a[158:159], v22 offset:12288\n"
    "s_mul_i32 s17, s11, 6144\n"
    "s_add_u32 s17, s17, s9\n"
    "s_xor_b32 s64, s74, 0\n"
    "s_mul_i32 s64, s64, 24576\n"
    "s_add_u32 s64, s64, s17\n"
    "s_add_u32 s64, s64, 0\n"
    "s_xor_b32 s65, s74, 1\n"
    "s_mul_i32 s65, s65, 24576\n"
    "s_add_u32 s65, s65, s17\n"
    "s_add_u32 s65, s65, 0\n"
    "s_xor_b32 s66, s74, 2\n"
    "s_mul_i32 s66, s66, 24576\n"
    "s_add_u32 s66, s66, s17\n"
    "s_add_u32 s66, s66, 2048\n"
    "s_xor_b32 s67, s74, 3\n"
    "s_mul_i32 s67, s67, 24576\n"
    "s_add_u32 s67, s67, s17\n"
    "s_add_u32 s67, s67, 4096\n"
    "v_lshrrev_b32 v4, 3, v1\n"
    "v_add_u32 v4, 1, v4\n"
    "v_xor_b32 v6, s74, v4\n"
    "v_lshlrev_b32 v6, 11, v6\n"
    "v_lshlrev_b32 v4, 7, v4\n"
    "s_lshl_b32 s17, s11, 9\n"
    "s_add_u32 s17, s17, 1572864\n"
    "s_add_u32 s17, s17, s9\n"
    "v_add3_u32 v42, v6, v4, s17\n"
    "v_and_b32 v6, 7, v1\n"
    "v_lshl_add_u32 v42, v6, 4, v42\n"
    "v_sub_u32 v6, 23, v1\n"
    "v_ashrrev_i32 v6, 31, v6\n"
    "v_lshl_or_b32 v42, v6, 8, v42\n"
    "s_lshl_b32 s18, s74, 11\n"
    "s_add_u32 s17, s17, s18\n"
    "s_add_u32 s17, s17, 128\n"
    "v_lshlrev_b32 v4, 7, v1\n"
    "v_add_u32 v43, s17, v4\n"
    "s_lshl_b32 s21, s15, 17\n"
    "s_lshl_b32 s22, s11, 6\n"
    "s_add_u32 s21, s21, s22\n"
    "v_lshlrev_b32 v4, 12, v8\n"
    "v_lshlrev_b32 v6, 4, v9\n"
    "v_add3_u32 v49, v4, v6, s21\n"
    "s_waitcnt vmcnt(3) lgkmcnt(0)\n"
    "s_barrier\n"
    "ds_read_b128 v[128:131], v27 offset:32768\n"
    "s_bfe_u32 s28, s27, 0x40008\n"
    "ds_read_b128 v[132:135], v27 offset:34816\n"
    "s_lshl_b32 s28, s28, 17\n"
    "ds_read_b128 v[136:139], v27 offset:33792\n"
    "s_add_u32 m0, s78, 98304\n"
    "ds_read_b128 v[140:143], v27 offset:35840\n"
    "buffer_load_dwordx4 v10, s[52:55], s28 offen offset:0 lds\n"
    "ds_read_b128 v[144:147], v27 offset:36864\n"
    "buffer_load_dwordx4 v11, s[52:55], s28 offen offset:1024 lds\n"
    "ds_read_b128 v[148:151], v27 offset:38912\n"
    "buffer_load_dwordx4 v12, s[52:55], s28 offen offset:2048 lds\n"
    "buffer_load_dwordx4 v13, s[52:55], s28 offen offset:3072 lds\n"
    "ds_read_b128 v[152:155], v27 offset:37888\n"
    "s_bfe_u32 s29, s26, 0x40008\n"
    "ds_read_b128 v[156:159], v27 offset:39936\n"
    "s_lshl_b32 s29, s29, 14\n"
    "ds_read_b128 v[160:163], v27 offset:40960\n"
    "s_add_u32 s29, s29, s92\n"
    "ds_read_b128 v[164:167], v27 offset:43008\n"
    "s_add_u32 m0, s79, 114688\n"
    "ds_read_b128 v[168:171], v27 offset:41984\n"
    "buffer_load_dwordx4 v14, s[56:59], s29 offen offset:0 sc1 lds\n"
    "ds_read_b128 v[172:175], v27 offset:44032\n"
    "buffer_load_dwordx4 v15, s[56:59], s29 offen offset:1024 sc1 lds\n"
    "buffer_load_dwordx4 v16, s[56:59], s29 offen offset:2048 sc1 lds\n"
    "s_waitcnt vmcnt(7) lgkmcnt(0)\n"
    "s_barrier\n"
    "v_mfma_f32_16x16x32_bf16 v[80:83], a[0:3], v[128:131], 0\n"
    "ds_read_b128 v[176:179], v27 offset:49152\n"
    "v_mfma_f32_16x16x32_bf16 v[80:83], a[4:7], v[132:135], v[80:83]\n"
    "ds_read_b128 v[180:183], v27 offset:51200\n"
    "s_bfe_u32 s30, s26, 0x4000c\n"
    "v_mfma_f32_16x16x32_bf16 v[84:87], a[12:15], v[132:135], 0\n"
    "ds_read_b128 v[184:187], v27 offset:50176\n"
    "v_mfma_f32_16x16x32_bf16 v[84:87], a[8:11], v[128:131], v[84:87]\n"
    "ds_read_b128 v[188:191], v27 offset:52224\n"
    "s_lshl_b32 s30, s30, 14\n"
    "v_mfma_f32_16x16x32_bf16 v[92:95], a[8:11], v[136:139], 0\n"
    "ds_read_b128 v[192:195], v27 offset:53248\n"
    "v_mfma_f32_16x16x32_bf16 v[92:95], a[12:15], v[140:143], v[92:95]\n"
    "ds_read_b128 v[196:199], v27 offset:55296\n"
    "s_add_u32 s30, s30, s92\n"
    "v_mfma_f32_16x16x32_bf16 v[88:91], a[4:7], v[140:143], 0\n"
    "ds_read_b128 v[200:203], v27 offset:54272\n"
    "s_add_u32 m0, s79, 16384\n"
    "v_mfma_f32_16x16x32_bf16 v[88:91], a[0:3], v[136:139], v[88:91]\n"
    "ds_read_b128 v[204:207], v27 offset:56320\n"
    "v_mfma_f32_16x16x32_bf16 v[96:99], a[0:3], v[144:147], 0\n"
    "ds_read_b128 v[208:211], v27 offset:57344\n"
    "v_mfma_f32_16x16x32_bf16 v[96:99], a[4:7], v[148:151], v[96:99]\n"
    "buffer_load_dwordx4 v14, s[56:59], s30 offen offset:0 sc1 lds\n"
    "v_mfma_f32_16x16x32_bf16 v[100:103], a[12:15], v[148:151], 0\n"
    "ds_read_b128 v[212:215], v27 offset:59392\n"
    "v_mfma_f32_16x16x32_bf16 v[100:103], a[8:11], v[144:147], v[100:103]\n"
    "ds_read_b128 v[216:219], v27 offset:58368\n"
    "v_mfma_f32_16x16x32_bf16 v[108:111], a[8:11], v[152:155], 0\n"
    "buffer_load_dwordx4 v15, s[56:59], s30 offen offset:1024 sc1 lds\n"
    "v_mfma_f32_16x16x32_bf16 v[108:111], a[12:15], v[156:159], v[108:111]\n"
    "ds_read_b128 v[220:223], v27 offset:60416\n"
    "v_mfma_f32_16x16x32_bf16 v[104:107], a[4:7], v[156:159], 0\n"
    "buffer_load_dwordx4 v16, s[56:59], s30 offen offset:2048 sc1 lds\n"
    "v_mfma_f32_16x16x32_bf16 v[104:107], a[0:3], v[152:155], v[104:107]\n"
    "v_mfma_f32_16x16x32_bf16 v[112:115], a[0:3], v[160:163], 0\n"
    "v_mfma_f32_16x16x32_bf16 v[112:115], a[4:7], v[164:167], v[112:115]\n"
    "v_mfma_f32_16x16x32_bf16 v[116:119], a[12:15], v[164:167], 0\n"
    "v_mfma_f32_16x16x32_bf16 v[116:119], a[8:11], v[160:163], v[116:119]\n"
    "v_mfma_f32_16x16x32_bf16 v[124:127], a[8:11], v[168:171], 0\n"
    "v_mfma_f32_16x16x32_bf16 v[124:127], a[12:15], v[172:175], v[124:127]\n"
    "v_mfma_f32_16x16x32_bf16 v[120:123], a[4:7], v[172:175], 0\n"
    "v_mfma_f32_16x16x32_bf16 v[120:123], a[0:3], v[168:171], v[120:123]\n"
    "s_waitcnt vmcnt(3) lgkmcnt(6)\n"
    "s_barrier\n"
    "v_mfma_f32_16x16x32_bf16 v[80:83], a[16:19], v[176:179], v[80:83]\n"
    "ds_read_b64_tr_b16 a[160:161], v23 offset:32768\n"
    "ds_read_b64_tr_b16 a[162:163], v23 offset:36864\n"
    "s_bfe_u32 s31, s27, 0x4000c\n"
    "v_mfma_f32_16x16x32_bf16 v[80:83], a[20:23], v[180:183], v[80:83]\n"
    "ds_read_b64_tr_b16 a[164:165], v23 offset:40960\n"
    "s_lshl_b32 s31, s31, 17\n"
    "v_mfma_f32_16x16x32_bf16 v[84:87], a[28:31], v[180:183], v[84:87]\n"
    "ds_read_b64_tr_b16 a[166:167], v23 offset:45056\n"
    "v_mfma_f32_16x16x32_bf16 v[84:87], a[24:27], v[176:179], v[84:87]\n"
    "ds_read_b128 v[128:131], v29 offset:49152\n"
    "s_add_u32 m0, s78, 147456\n"
    "v_mfma_f32_16x16x32_bf16 v[92:95], a[24:27], v[184:187], v[92:95]\n"
    "ds_read_b128 v[132:135], v29 offset:51200\n"
    "v_mfma_f32_16x16x32_bf16 v[92:95], a[28:31], v[188:191], v[92:95]\n"
    "buffer_load_dwordx4 v10, s[52:55], s31 offen offset:0 lds\n"
    "ds_read_b64_tr_b16 a[168:169], v24 offset:32768\n"
    "v_mfma_f32_16x16x32_bf16 v[88:91], a[20:23], v[188:191], v[88:91]\n"
    "ds_read_b64_tr_b16 a[170:171], v24 offset:36864\n"
    "s_waitcnt lgkmcnt(8)\n"
    "v_mfma_f32_16x16x32_bf16 v[88:91], a[16:19], v[184:187], v[88:91]\n"
    "buffer_load_dwordx4 v11, s[52:55], s31 offen offset:1024 lds\n"
    "v_mfma_f32_16x16x32_bf16 v[96:99], a[16:19], v[192:195], v[96:99]\n"
    "ds_read_b64_tr_b16 a[172:173], v24 offset:40960\n"
    "v_mfma_f32_16x16x32_bf16 v[96:99], a[20:23], v[196:199], v[96:99]\n"
    "buffer_load_dwordx4 v12, s[52:55], s31 offen offset:2048 lds\n"
    "v_mfma_f32_16x16x32_bf16 v[100:103], a[28:31], v[196:199], v[100:103]\n"
    "ds_read_b64_tr_b16 a[174:175], v24 offset:45056\n"
    "v_mfma_f32_16x16x32_bf16 v[100:103], a[24:27], v[192:195], v[100:103]\n"
    "buffer_load_dwordx4 v13, s[52:55], s31 offen offset:3072 lds\n"
    "ds_read_b128 v[136:139], v29 offset:50176\n"
    "v_mfma_f32_16x16x32_bf16 v[108:111], a[24:27], v[200:203], v[108:111]\n"
    "ds_read_b128 v[140:143], v29 offset:52224\n"
    "s_bfe_u32 s28, s26, 0x40010\n"
    "v_mfma_f32_16x16x32_bf16 v[108:111], a[28:31], v[204:207], v[108:111]\n"
    "ds_read_b128 v[144:147], v29 offset:53248\n"
    "s_lshl_b32 s28, s28, 14\n"
    "v_mfma_f32_16x16x32_bf16 v[104:107], a[20:23], v[204:207], v[104:107]\n"
    "ds_read_b128 v[148:151], v29 offset:55296\n"
    "v_mfma_f32_16x16x32_bf16 v[104:107], a[16:19], v[200:203], v[104:107]\n"
    "ds_read_b128 v[152:155], v29 offset:54272\n"
    "s_add_u32 s28, s28, s92\n"
    "v_mfma_f32_16x16x32_bf16 v[112:115], a[16:19], v[208:211], v[112:115]\n"
    "ds_read_b128 v[156:159], v29 offset:56320\n"
    "s_add_u32 m0, s79, 65536\n"
    "v_mfma_f32_16x16x32_bf16 v[112:115], a[20:23], v[212:215], v[112:115]\n"
    "ds_read_b128 v[160:163], v29 offset:57344\n"
    "ds_read_b128 v[164:167], v29 offset:59392\n"
    "v_mfma_f32_16x16x32_bf16 v[116:119], a[28:31], v[212:215], v[116:119]\n"
    "buffer_load_dwordx4 v14, s[56:59], s28 offen offset:0 sc1 lds\n"
    "v_mfma_f32_16x16x32_bf16 v[116:119], a[24:27], v[208:211], v[116:119]\n"
    "ds_read_b128 v[168:171], v29 offset:58368\n"
    "v_mfma_f32_16x16x32_bf16 v[124:127], a[24:27], v[216:219], v[124:127]\n"
    "buffer_load_dwordx4 v15, s[56:59], s28 offen offset:1024 sc1 lds\n"
    "v_mfma_f32_16x16x32_bf16 v[124:127], a[28:31], v[220:223], v[124:127]\n"
    "ds_read_b128 v[172:175], v29 offset:60416\n"
    "v_mfma_f32_16x16x32_bf16 v[120:123], a[20:23], v[220:223], v[120:123]\n"
    "buffer_load_dwordx4 v16, s[56:59], s28 offen offset:2048 sc1 lds\n"
    "v_mfma_f32_16x16x32_bf16 v[120:123], a[16:19], v[216:219], v[120:123]\n"
    "s_waitcnt vmcnt(7) lgkmcnt(6)\n"
    "s_barrier\n"
    "v_mfma_f32_16x16x32_bf16 v[80:83], a[32:35], v[128:131], v[80:83]\n"
    "ds_read_b128 v[176:179], v27 offset:16384\n"
    "v_mfma_f32_16x16x32_bf16 v[80:83], a[36:39], v[132:135], v[80:83]\n"
    "ds_read_b128 v[180:183], v27 offset:18432\n"
    "s_bfe_u32 s29, s26, 0x40014\n"
    "v_mfma_f32_16x16x32_bf16 v[84:87], a[44:47], v[132:135], v[84:87]\n"
    "ds_read_b128 v[184:187], v27 offset:17408\n"
    "v_mfma_f32_16x16x32_bf16 v[84:87], a[40:43], v[128:131], v[84:87]\n"
    "ds_read_b128 v[188:191], v27 offset:19456\n"
    "s_lshl_b32 s29, s29, 14\n"
    "v_mfma_f32_16x16x32_bf16 v[92:95], a[40:43], v[136:139], v[92:95]\n"
    "ds_read_b128 v[192:195], v27 offset:20480\n"
    "v_mfma_f32_16x16x32_bf16 v[92:95], a[44:47], v[140:143], v[92:95]\n"
    "ds_read_b128 v[196:199], v27 offset:22528\n"
    "s_add_u32 s29, s29, s92\n"
    "v_mfma_f32_16x16x32_bf16 v[88:91], a[36:39], v[140:143], v[88:91]\n"
    "ds_read_b128 v[200:203], v27 offset:21504\n"
    "s_add_u32 m0, s79, 81920\n"
    "v_mfma_f32_16x16x32_bf16 v[88:91], a[32:35], v[136:139], v[88:91]\n"
    "ds_read_b128 v[204:207], v27 offset:23552\n"
    "s_waitcnt lgkmcnt(8)\n"
    "v_mfma_f32_16x16x32_bf16 v[96:99], a[32:35], v[144:147], v[96:99]\n"
    "ds_read_b128 v[208:211], v27 offset:24576\n"
    "v_mfma_f32_16x16x32_bf16 v[96:99], a[36:39], v[148:151], v[96:99]\n"
    "buffer_load_dwordx4 v14, s[56:59], s29 offen offset:0 sc1 lds\n"
    "v_mfma_f32_16x16x32_bf16 v[100:103], a[44:47], v[148:151], v[100:103]\n"
    "ds_read_b128 v[212:215], v27 offset:26624\n"
    "v_mfma_f32_16x16x32_bf16 v[100:103], a[40:43], v[144:147], v[100:103]\n"
    "ds_read_b128 v[216:219], v27 offset:25600\n"
    "v_mfma_f32_16x16x32_bf16 v[108:111], a[40:43], v[152:155], v[108:111]\n"
    "buffer_load_dwordx4 v15, s[56:59], s29 offen offset:1024 sc1 lds\n"
    "v_mfma_f32_16x16x32_bf16 v[108:111], a[44:47], v[156:159], v[108:111]\n"
    "ds_read_b128 v[220:223], v27 offset:27648\n"
    "v_mfma_f32_16x16x32_bf16 v[104:107], a[36:39], v[156:159], v[104:107]\n"
    "buffer_load_dwordx4 v16, s[56:59], s29 offen offset:2048 sc1 lds\n"
    "v_mfma_f32_16x16x32_bf16 v[104:107], a[32:35], v[152:155], v[104:107]\n"
    "v_mfma_f32_16x16x32_bf16 v[112:115], a[32:35], v[160:163], v[112:115]\n"
    "v_mfma_f32_16x16x32_bf16 v[112:115], a[36:39], v[164:167], v[112:115]\n"
    "v_mfma_f32_16x16x32_bf16 v[116:119], a[44:47], v[164:167], v[116:119]\n"
    "v_mfma_f32_16x16x32_bf16 v[116:119], a[40:43], v[160:163], v[116:119]\n"
    "v_mfma_f32_16x16x32_bf16 v[124:127], a[40:43], v[168:171], v[124:127]\n"
    "v_mfma_f32_16x16x32_bf16 v[124:127], a[44:47], v[172:175], v[124:127]\n"
    "v_mfma_f32_16x16x32_bf16 v[120:123], a[36:39], v[172:175], v[120:123]\n"
    "v_mfma_f32_16x16x32_bf16 v[120:123], a[32:35], v[168:171], v[120:123]\n"
    "s_waitcnt vmcnt(3) lgkmcnt(6)\n"
    "s_barrier\n"
    "v_mfma_f32_16x16x32_bf16 v[80:83], a[48:51], v[176:179], v[80:83]\n"
    "ds_read_b64_tr_b16 a[176:177], v25 offset:16384\n"
    "ds_read_b64_tr_b16 a[178:179], v25 offset:20480\n"
    "s_bfe_u32 s30, s27, 0x40010\n"
    "v_mfma_f32_16x16x32_bf16 v[80:83], a[52:55], v[180:183], v[80:83]\n"
    "ds_read_b64_tr_b16 a[180:181], v25 offset:24576\n"
    "s_lshl_b32 s30, s30, 17\n"
    "v_mfma_f32_16x16x32_bf16 v[84:87], a[60:63], v[180:183], v[84:87]\n"
    "ds_read_b64_tr_b16 a[182:183], v25 offset:28672\n"
    "v_mfma_f32_16x16x32_bf16 v[84:87], a[56:59], v[176:179], v[84:87]\n"
    "ds_read_b128 v[128:131], v29 offset:0\n"
    "s_add_u32 m0, s78, 131072\n"
    "v_mfma_f32_16x16x32_bf16 v[92:95], a[56:59], v[184:187], v[92:95]\n"
    "ds_read_b128 v[132:135], v29 offset:2048\n"
    "v_mfma_f32_16x16x32_bf16 v[92:95], a[60:63], v[188:191], v[92:95]\n"
    "buffer_load_dwordx4 v10, s[52:55], s30 offen offset:0 lds\n"
    "ds_read_b64_tr_b16 a[184:185], v26 offset:16384\n"
    "v_mfma_f32_16x16x32_bf16 v[88:91], a[52:55], v[188:191], v[88:91]\n"
    "ds_read_b64_tr_b16 a[186:187], v26 offset:20480\n"
    "s_waitcnt lgkmcnt(8)\n"
    "v_mfma_f32_16x16x32_bf16 v[88:91], a[48:51], v[184:187], v[88:91]\n"
    "buffer_load_dwordx4 v11, s[52:55], s30 offen offset:1024 lds\n"
    "v_mfma_f32_16x16x32_bf16 v[96:99], a[48:51], v[192:195], v[96:99]\n"
    "ds_read_b64_tr_b16 a[188:189], v26 offset:24576\n"
    "v_mfma_f32_16x16x32_bf16 v[96:99], a[52:55], v[196:199], v[96:99]\n"
    "buffer_load_dwordx4 v12, s[52:55], s30 offen offset:2048 lds\n"
    "v_mfma_f32_16x16x32_bf16 v[100:103], a[60:63], v[196:199], v[100:103]\n"
    "ds_read_b64_tr_b16 a[190:191], v26 offset:28672\n"
    "v_mfma_f32_16x16x32_bf16 v[100:103], a[56:59], v[192:195], v[100:103]\n"
    "buffer_load_dwordx4 v13, s[52:55], s30 offen offset:3072 lds\n"
    "ds_read_b128 v[136:139], v29 offset:1024\n"
    "v_mfma_f32_16x16x32_bf16 v[108:111], a[56:59], v[200:203], v[108:111]\n"
    "ds_read_b128 v[140:143], v29 offset:3072\n"
    "s_bfe_u32 s31, s26, 0x40018\n"
    "v_mfma_f32_16x16x32_bf16 v[108:111], a[60:63], v[204:207], v[108:111]\n"
    "ds_read_b128 v[144:147], v29 offset:4096\n"
    "s_lshl_b32 s31, s31, 14\n"
    "v_mfma_f32_16x16x32_bf16 v[104:107], a[52:55], v[204:207], v[104:107]\n"
    "ds_read_b128 v[148:151], v29 offset:6144\n"
    "v_mfma_f32_16x16x32_bf16 v[104:107], a[48:51], v[200:203], v[104:107]\n"
    "ds_read_b128 v[152:155], v29 offset:5120\n"
    "s_add_u32 s31, s31, s92\n"
    "v_mfma_f32_16x16x32_bf16 v[112:115], a[48:51], v[208:211], v[112:115]\n"
    "ds_read_b128 v[156:159], v29 offset:7168\n"
    "s_add_u32 m0, s79, 0\n"
    "v_mfma_f32_16x16x32_bf16 v[112:115], a[52:55], v[212:215], v[112:115]\n"
    "ds_read_b128 v[160:163], v29 offset:8192\n"
    "ds_read_b128 v[164:167], v29 offset:10240\n"
    "v_mfma_f32_16x16x32_bf16 v[116:119], a[60:63], v[212:215], v[116:119]\n"
    "buffer_load_dwordx4 v14, s[56:59], s31 offen offset:0 sc1 lds\n"
    "v_mfma_f32_16x16x32_bf16 v[116:119], a[56:59], v[208:211], v[116:119]\n"
    "ds_read_b128 v[168:171], v29 offset:9216\n"
    "v_mfma_f32_16x16x32_bf16 v[124:127], a[56:59], v[216:219], v[124:127]\n"
    "buffer_load_dwordx4 v15, s[56:59], s31 offen offset:1024 sc1 lds\n"
    "v_mfma_f32_16x16x32_bf16 v[124:127], a[60:63], v[220:223], v[124:127]\n"
    "ds_read_b128 v[172:175], v29 offset:11264\n"
    "v_mfma_f32_16x16x32_bf16 v[120:123], a[52:55], v[220:223], v[120:123]\n"
    "buffer_load_dwordx4 v16, s[56:59], s31 offen offset:2048 sc1 lds\n"
    "v_mfma_f32_16x16x32_bf16 v[120:123], a[48:51], v[216:219], v[120:123]\n"
    "s_waitcnt vmcnt(7) lgkmcnt(6)\n"
    "s_barrier\n"
    "v_mfma_f32_16x16x32_bf16 v[80:83], a[64:67], v[128:131], v[80:83]\n"
    "ds_read_b128 v[176:179], v29 offset:16384\n"
    "v_mfma_f32_16x16x32_bf16 v[80:83], a[68:71], v[132:135], v[80:83]\n"
    "ds_read_b128 v[180:183], v29 offset:18432\n"
    "s_bfe_u32 s28, s26, 0x4001c\n"
    "v_mfma_f32_16x16x32_bf16 v[84:87], a[76:79], v[132:135], v[84:87]\n"
    "ds_read_b128 v[184:187], v29 offset:17408\n"
    "v_mfma_f32_16x16x32_bf16 v[84:87], a[72:75], v[128:131], v[84:87]\n"
    "ds_read_b128 v[188:191], v29 offset:19456\n"
    "s_lshl_b32 s28, s28, 14\n"
    "v_mfma_f32_16x16x32_bf16 v[92:95], a[72:75], v[136:139], v[92:95]\n"
    "ds_read_b128 v[192:195], v29 offset:20480\n"
    "v_mfma_f32_16x16x32_bf16 v[92:95], a[76:79], v[140:143], v[92:95]\n"
    "ds_read_b128 v[196:199], v29 offset:22528\n"
    "s_add_u32 s28, s28, s92\n"
    "v_mfma_f32_16x16x32_bf16 v[88:91], a[68:71], v[140:143], v[88:91]\n"
    "ds_read_b128 v[200:203], v29 offset:21504\n"
    "s_add_u32 m0, s79, 32768\n"
    "v_mfma_f32_16x16x32_bf16 v[88:91], a[64:67], v[136:139], v[88:91]\n"
    "ds_read_b128 v[204:207], v29 offset:23552\n"
    "s_waitcnt lgkmcnt(8)\n"
    "v_mfma_f32_16x16x32_bf16 v[96:99], a[64:67], v[144:147], v[96:99]\n"
    "ds_read_b128 v[208:211], v29 offset:24576\n"
    "v_mfma_f32_16x16x32_bf16 v[96:99], a[68:71], v[148:151], v[96:99]\n"
    "buffer_load_dwordx4 v14, s[56:59], s28 offen offset:0 sc1 lds\n"
    "v_mfma_f32_16x16x32_bf16 v[100:103], a[76:79], v[148:151], v[100:103]\n"
    "ds_read_b128 v[212:215], v29 offset:26624\n"
    "v_mfma_f32_16x16x32_bf16 v[100:103], a[72:75], v[144:147], v[100:103]\n"
    "ds_read_b128 v[216:219], v29 offset:25600\n"
    "v_mfma_f32_16x16x32_bf16 v[108:111], a[72:75], v[152:155], v[108:111]\n"
    "buffer_load_dwordx4 v15, s[56:59], s28 offen offset:1024 sc1 lds\n"
    "v_mfma_f32_16x16x32_bf16 v[108:111], a[76:79], v[156:159], v[108:111]\n"
    "ds_read_b128 v[220:223], v29 offset:27648\n"
    "v_mfma_f32_16x16x32_bf16 v[104:107], a[68:71], v[156:159], v[104:107]\n"
    "buffer_load_dwordx4 v16, s[56:59], s28 offen offset:2048 sc1 lds\n"
    "v_mfma_f32_16x16x32_bf16 v[104:107], a[64:67], v[152:155], v[104:107]\n"
    "v_mfma_f32_16x16x32_bf16 v[112:115], a[64:67], v[160:163], v[112:115]\n"
    "v_mfma_f32_16x16x32_bf16 v[112:115], a[68:71], v[164:167], v[112:115]\n"
    "v_mfma_f32_16x16x32_bf16 v[116:119], a[76:79], v[164:167], v[116:119]\n"
    "v_mfma_f32_16x16x32_bf16 v[116:119], a[72:75], v[160:163], v[116:119]\n"
    "v_mfma_f32_16x16x32_bf16 v[124:127], a[72:75], v[168:171], v[124:127]\n"
    "v_mfma_f32_16x16x32_bf16 v[124:127], a[76:79], v[172:175], v[124:127]\n"
    "v_mfma_f32_16x16x32_bf16 v[120:123], a[68:71], v[172:175], v[120:123]\n"
    "v_mfma_f32_16x16x32_bf16 v[120:123], a[64:67], v[168:171], v[120:123]\n"
    "s_waitcnt vmcnt(3) lgkmcnt(6)\n"
    "s_barrier\n"
    "v_mfma_f32_16x16x32_bf16 v[80:83], a[80:83], v[176:179], v[80:83]\n"
    "ds_read_b64_tr_b16 a[192:193], v25 offset:0\n"
    "ds_read_b64_tr_b16 a[194:195], v25 offset:4096\n"
    "s_bfe_u32 s29, s27, 0x40014\n"
    "v_mfma_f32_16x16x32_bf16 v[80:83], a[84:87], v[180:183], v[80:83]\n"
    "ds_read_b64_tr_b16 a[196:197], v25 offset:8192\n"
    "s_lshl_b32 s29, s29, 17\n"
    "v_mfma_f32_16x16x32_bf16 v[84:87], a[92:95], v[180:183], v[84:87]\n"
    "ds_read_b64_tr_b16 a[198:199], v25 offset:12288\n"
    "v_mfma_f32_16x16x32_bf16 v[84:87], a[88:91], v[176:179], v[84:87]\n"
    "ds_read_b128 v[128:131], v27 offset:0\n"
    "s_add_u32 m0, s78, 49152\n"
    "v_mfma_f32_16x16x32_bf16 v[92:95], a[88:91], v[184:187], v[92:95]\n"
    "ds_read_b128 v[132:135], v27 offset:2048\n"
    "v_mfma_f32_16x16x32_bf16 v[92:95], a[92:95], v[188:191], v[92:95]\n"
    "buffer_load_dwordx4 v10, s[52:55], s29 offen offset:0 lds\n"
    "ds_read_b64_tr_b16 a[200:201], v26 offset:0\n"
    "v_mfma_f32_16x16x32_bf16 v[88:91], a[84:87], v[188:191], v[88:91]\n"
    "ds_read_b64_tr_b16 a[202:203], v26 offset:4096\n"
    "s_waitcnt lgkmcnt(8)\n"
    "v_mfma_f32_16x16x32_bf16 v[88:91], a[80:83], v[184:187], v[88:91]\n"
    "buffer_load_dwordx4 v11, s[52:55], s29 offen offset:1024 lds\n"
    "v_mfma_f32_16x16x32_bf16 v[96:99], a[80:83], v[192:195], v[96:99]\n"
    "ds_read_b64_tr_b16 a[204:205], v26 offset:8192\n"
    "v_mfma_f32_16x16x32_bf16 v[96:99], a[84:87], v[196:199], v[96:99]\n"
    "buffer_load_dwordx4 v12, s[52:55], s29 offen offset:2048 lds\n"
    "v_mfma_f32_16x16x32_bf16 v[100:103], a[92:95], v[196:199], v[100:103]\n"
    "ds_read_b64_tr_b16 a[206:207], v26 offset:12288\n"
    "v_mfma_f32_16x16x32_bf16 v[100:103], a[88:91], v[192:195], v[100:103]\n"
    "buffer_load_dwordx4 v13, s[52:55], s29 offen offset:3072 lds\n"
    "ds_read_b128 v[136:139], v27 offset:1024\n"
    "v_mfma_f32_16x16x32_bf16 v[108:111], a[88:91], v[200:203], v[108:111]\n"
    "ds_read_b128 v[140:143], v27 offset:3072\n"
    "s_bfe_u32 s30, s27, 0x40000\n"
    "v_mfma_f32_16x16x32_bf16 v[108:111], a[92:95], v[204:207], v[108:111]\n"
    "ds_read_b128 v[144:147], v27 offset:4096\n"
    "s_lshl_b32 s30, s30, 14\n"
    "v_mfma_f32_16x16x32_bf16 v[104:107], a[84:87], v[204:207], v[104:107]\n"
    "ds_read_b128 v[148:151], v27 offset:6144\n"
    "v_mfma_f32_16x16x32_bf16 v[104:107], a[80:83], v[200:203], v[104:107]\n"
    "ds_read_b128 v[152:155], v27 offset:5120\n"
    "s_add_u32 s30, s30, s92\n"
    "v_mfma_f32_16x16x32_bf16 v[112:115], a[80:83], v[208:211], v[112:115]\n"
    "ds_read_b128 v[156:159], v27 offset:7168\n"
    "s_add_u32 m0, s79, 114688\n"
    "v_mfma_f32_16x16x32_bf16 v[112:115], a[84:87], v[212:215], v[112:115]\n"
    "ds_read_b128 v[160:163], v27 offset:8192\n"
    "ds_read_b128 v[164:167], v27 offset:10240\n"
    "v_mfma_f32_16x16x32_bf16 v[116:119], a[92:95], v[212:215], v[116:119]\n"
    "buffer_load_dwordx4 v14, s[56:59], s30 offen offset:0 sc1 lds\n"
    "v_mfma_f32_16x16x32_bf16 v[116:119], a[88:91], v[208:211], v[116:119]\n"
    "ds_read_b128 v[168:171], v27 offset:9216\n"
    "v_mfma_f32_16x16x32_bf16 v[124:127], a[88:91], v[216:219], v[124:127]\n"
    "buffer_load_dwordx4 v15, s[56:59], s30 offen offset:1024 sc1 lds\n"
    "v_mfma_f32_16x16x32_bf16 v[124:127], a[92:95], v[220:223], v[124:127]\n"
    "ds_read_b128 v[172:175], v27 offset:11264\n"
    "v_mfma_f32_16x16x32_bf16 v[120:123], a[84:87], v[220:223], v[120:123]\n"
    "buffer_load_dwordx4 v16, s[56:59], s30 offen offset:2048 sc1 lds\n"
    "v_mfma_f32_16x16x32_bf16 v[120:123], a[80:83], v[216:219], v[120:123]\n"
    "s_waitcnt vmcnt(7) lgkmcnt(6)\n"
    "s_barrier\n"
    "v_mfma_f32_16x16x32_bf16 v[80:83], a[96:99], v[128:131], v[80:83]\n"
    "ds_read_b128 v[176:179], v27 offset:32768\n"
    "v_mfma_f32_16x16x32_bf16 v[80:83], a[100:103], v[132:135], v[80:83]\n"
    "ds_read_b128 v[180:183], v27 offset:34816\n"
    "s_bfe_u32 s31, s27, 0x40004\n"
    "v_mfma_f32_16x16x32_bf16 v[84:87], a[108:111], v[132:135], v[84:87]\n"
    "ds_read_b128 v[184:187], v27 offset:33792\n"
    "v_mfma_f32_16x16x32_bf16 v[84:87], a[104:107], v[128:131], v[84:87]\n"
    "ds_read_b128 v[188:191], v27 offset:35840\n"
    "s_lshl_b32 s31, s31, 14\n"
    "v_mfma_f32_16x16x32_bf16 v[92:95], a[104:107], v[136:139], v[92:95]\n"
    "ds_read_b128 v[192:195], v27 offset:36864\n"
    "v_mfma_f32_16x16x32_bf16 v[92:95], a[108:111], v[140:143], v[92:95]\n"
    "ds_read_b128 v[196:199], v27 offset:38912\n"
    "s_add_u32 s31, s31, s92\n"
    "v_mfma_f32_16x16x32_bf16 v[88:91], a[100:103], v[140:143], v[88:91]\n"
    "ds_read_b128 v[200:203], v27 offset:37888\n"
    "s_add_u32 m0, s79, 98304\n"
    "v_mfma_f32_16x16x32_bf16 v[88:91], a[96:99], v[136:139], v[88:91]\n"
    "ds_read_b128 v[204:207], v27 offset:39936\n"
    "s_waitcnt lgkmcnt(8)\n"
    "v_mfma_f32_16x16x32_bf16 v[96:99], a[96:99], v[144:147], v[96:99]\n"
    "ds_read_b128 v[208:211], v27 offset:40960\n"
    "v_mfma_f32_16x16x32_bf16 v[96:99], a[100:103], v[148:151], v[96:99]\n"
    "buffer_load_dwordx4 v14, s[56:59], s31 offen offset:0 sc1 lds\n"
    "v_mfma_f32_16x16x32_bf16 v[100:103], a[108:111], v[148:151], v[100:103]\n"
    "ds_read_b128 v[212:215], v27 offset:43008\n"
    "v_mfma_f32_16x16x32_bf16 v[100:103], a[104:107], v[144:147], v[100:103]\n"
    "ds_read_b128 v[216:219], v27 offset:41984\n"
    "v_mfma_f32_16x16x32_bf16 v[108:111], a[104:107], v[152:155], v[108:111]\n"
    "buffer_load_dwordx4 v15, s[56:59], s31 offen offset:1024 sc1 lds\n"
    "v_mfma_f32_16x16x32_bf16 v[108:111], a[108:111], v[156:159], v[108:111]\n"
    "ds_read_b128 v[220:223], v27 offset:44032\n"
    "v_mfma_f32_16x16x32_bf16 v[104:107], a[100:103], v[156:159], v[104:107]\n"
    "buffer_load_dwordx4 v16, s[56:59], s31 offen offset:2048 sc1 lds\n"
    "v_mfma_f32_16x16x32_bf16 v[104:107], a[96:99], v[152:155], v[104:107]\n"
    "v_mfma_f32_16x16x32_bf16 v[112:115], a[96:99], v[160:163], v[112:115]\n"
    "v_mfma_f32_16x16x32_bf16 v[112:115], a[100:103], v[164:167], v[112:115]\n"
    "v_mfma_f32_16x16x32_bf16 v[116:119], a[108:111], v[164:167], v[116:119]\n"
    "v_mfma_f32_16x16x32_bf16 v[116:119], a[104:107], v[160:163], v[116:119]\n"
    "v_mfma_f32_16x16x32_bf16 v[124:127], a[104:107], v[168:171], v[124:127]\n"
    "v_mfma_f32_16x16x32_bf16 v[124:127], a[108:111], v[172:175], v[124:127]\n"
    "v_mfma_f32_16x16x32_bf16 v[120:123], a[100:103], v[172:175], v[120:123]\n"
    "v_mfma_f32_16x16x32_bf16 v[120:123], a[96:99], v[168:171], v[120:123]\n"
    "s_waitcnt vmcnt(3) lgkmcnt(6)\n"
    "s_barrier\n"
    "v_mfma_f32_16x16x32_bf16 v[80:83], a[112:115], v[176:179], v[80:83]\n"
    "ds_read_b64_tr_b16 a[208:209], v21 offset:49152\n"
    "ds_read_b64_tr_b16 a[210:211], v21 offset:53248\n"
    "s_bfe_u32 s28, s27, 0x40018\n"
    "v_mfma_f32_16x16x32_bf16 v[80:83], a[116:119], v[180:183], v[80:83]\n"
    "ds_read_b64_tr_b16 a[212:213], v21 offset:57344\n"
    "s_lshl_b32 s28, s28, 17\n"
    "v_mfma_f32_16x16x32_bf16 v[84:87], a[124:127], v[180:183], v[84:87]\n"
    "ds_read_b64_tr_b16 a[214:215], v21 offset:61440\n"
    "v_mfma_f32_16x16x32_bf16 v[84:87], a[120:123], v[176:179], v[84:87]\n"
    "ds_read_b128 v[128:131], v29 offset:49152\n"
    "s_add_u32 m0, s78, 16384\n"
    "v_mfma_f32_16x16x32_bf16 v[92:95], a[120:123], v[184:187], v[92:95]\n"
    "ds_read_b128 v[132:135], v29 offset:51200\n"
    "v_mfma_f32_16x16x32_bf16 v[92:95], a[124:127], v[188:191], v[92:95]\n"
    "buffer_load_dwordx4 v10, s[52:55], s28 offen offset:0 lds\n"
    "ds_read_b64_tr_b16 a[216:217], v22 offset:49152\n"
    "v_mfma_f32_16x16x32_bf16 v[88:91], a[116:119], v[188:191], v[88:91]\n"
    "ds_read_b64_tr_b16 a[218:219], v22 offset:53248\n"
    "s_waitcnt lgkmcnt(8)\n"
    "v_mfma_f32_16x16x32_bf16 v[88:91], a[112:115], v[184:187], v[88:91]\n"
    "buffer_load_dwordx4 v11, s[52:55], s28 offen offset:1024 lds\n"
    "v_mfma_f32_16x16x32_bf16 v[96:99], a[112:115], v[192:195], v[96:99]\n"
    "ds_read_b64_tr_b16 a[220:221], v22 offset:57344\n"
    "Lw10_%=:\n"
    "s_cbranch_execz Lw11_%=\n"
    "v_mfma_f32_16x16x32_bf16 v[96:99], a[116:119], v[196:199], v[96:99]\n"
    "buffer_load_dwordx4 v12, s[52:55], s28 offen offset:2048 lds\n"
    "v_mfma_f32_16x16x32_bf16 v[100:103], a[124:127], v[196:199], v[100:103]\n"
    "ds_read_b64_tr_b16 a[222:223], v22 offset:61440\n"
    "v_mfma_f32_16x16x32_bf16 v[100:103], a[120:123], v[192:195], v[100:103]\n"
    "buffer_load_dwordx4 v13, s[52:55], s28 offen offset:3072 lds\n"
    "ds_read_b128 v[136:139], v29 offset:50176\n"
    "v_mfma_f32_16x16x32_bf16 v[108:111], a[120:123], v[200:203], v[108:111]\n"
    "ds_read_b128 v[140:143], v29 offset:52224\n"
    "s_bfe_u32 s29, s27, 0x40008\n"
    "v_mfma_f32_16x16x32_bf16 v[108:111], a[124:127], v[204:207], v[108:111]\n"
    "ds_read_b128 v[144:147], v29 offset:53248\n"
    "s_lshl_b32 s29, s29, 14\n"
    "v_mfma_f32_16x16x32_bf16 v[104:107], a[116:119], v[204:207], v[104:107]\n"
    "ds_read_b128 v[148:151], v29 offset:55296\n"
    "v_mfma_f32_16x16x32_bf16 v[104:107], a[112:115], v[200:203], v[104:107]\n"
    "ds_read_b128 v[152:155], v29 offset:54272\n"
    "s_add_u32 s29, s29, s92\n"
    "v_mfma_f32_16x16x32_bf16 v[112:115], a[112:115], v[208:211], v[112:115]\n"
    "ds_read_b128 v[156:159], v29 offset:56320\n"
    "s_add_u32 m0, s79, 65536\n"
    "v_mfma_f32_16x16x32_bf16 v[112:115], a[116:119], v[212:215], v[112:115]\n"
    "ds_read_b128 v[160:163], v29 offset:57344\n"
    "ds_read_b128 v[164:167], v29 offset:59392\n"
    "v_mfma_f32_16x16x32_bf16 v[116:119], a[124:127], v[212:215], v[116:119]\n"
    "buffer_load_dwordx4 v14, s[56:59], s29 offen offset:0 sc1 lds\n"
    "v_mfma_f32_16x16x32_bf16 v[116:119], a[120:123], v[208:211], v[116:119]\n"
    "ds_read_b128 v[168:171], v29 offset:58368\n"
    "v_mfma_f32_16x16x32_bf16 v[124:127], a[120:123], v[216:219], v[124:127]\n"
    "buffer_load_dwordx4 v15, s[56:59], s29 offen offset:1024 sc1 lds\n"
    "v_mfma_f32_16x16x32_bf16 v[124:127], a[124:127], v[220:223], v[124:127]\n"
    "ds_read_b128 v[172:175], v29 offset:60416\n"
    "v_mfma_f32_16x16x32_bf16 v[120:123], a[116:119], v[220:223], v[120:123]\n"
    "buffer_load_dwordx4 v16, s[56:59], s29 offen offset:2048 sc1 lds\n"
    "v_mfma_f32_16x16x32_bf16 v[120:123], a[112:115], v[216:219], v[120:123]\n"
    "s_waitcnt vmcnt(7) lgkmcnt(6)\n"
    "s_barrier\n"
    "v_mfma_f32_16x16x32_bf16 v[80:83], a[128:131], v[128:131], v[80:83]\n"
    "ds_read_b128 v[176:179], v29 offset:32768\n"
    "v_mfma_f32_16x16x32_bf16 v[80:83], a[132:135], v[132:135], v[80:83]\n"
    "ds_read_b128 v[180:183], v29 offset:34816\n"
    "s_bfe_u32 s30, s27, 0x4000c\n"
    "v_mfma_f32_16x16x32_bf16 v[84:87], a[140:143], v[132:135], v[84:87]\n"
    "ds_read_b128 v[184:187], v29 offset:33792\n"
    "v_mfma_f32_16x16x32_bf16 v[84:87], a[136:139], v[128:131], v[84:87]\n"
    "ds_read_b128 v[188:191], v29 offset:35840\n"
    "s_lshl_b32 s30, s30, 14\n"
    "v_mfma_f32_16x16x32_bf16 v[92:95], a[136:139], v[136:139], v[92:95]\n"
    "ds_read_b128 v[192:195], v29 offset:36864\n"
    "v_mfma_f32_16x16x32_bf16 v[92:95], a[140:143], v[140:143], v[92:95]\n"
    "ds_read_b128 v[196:199], v29 offset:38912\n"
    "s_add_u32 s30, s30, s92\n"
    "v_mfma_f32_16x16x32_bf16 v[88:91], a[132:135], v[140:143], v[88:91]\n"
    "ds_read_b128 v[200:203], v29 offset:37888\n"
    "s_add_u32 m0, s79, 147456\n"
    "v_mfma_f32_16x16x32_bf16 v[88:91], a[128:131], v[136:139], v[88:91]\n"
    "ds_read_b128 v[204:207], v29 offset:39936\n"
    "s_waitcnt lgkmcnt(8)\n"
    "v_mfma_f32_16x16x32_bf16 v[96:99], a[128:131], v[144:147], v[96:99]\n"
    "ds_read_b128 v[208:211], v29 offset:40960\n"
    "v_mfma_f32_16x16x32_bf16 v[96:99], a[132:135], v[148:151], v[96:99]\n"
    "buffer_load_dwordx4 v14, s[56:59], s30 offen offset:0 sc1 lds\n"
    "v_mfma_f32_16x16x32_bf16 v[100:103], a[140:143], v[148:151], v[100:103]\n"
    "ds_read_b128 v[212:215], v29 offset:43008\n"
    "v_mfma_f32_16x16x32_bf16 v[100:103], a[136:139], v[144:147], v[100:103]\n"
    "ds_read_b128 v[216:219], v29 offset:41984\n"
    "v_mfma_f32_16x16x32_bf16 v[108:111], a[136:139], v[152:155], v[108:111]\n"
    "buffer_load_dwordx4 v15, s[56:59], s30 offen offset:1024 sc1 lds\n"
    "v_mfma_f32_16x16x32_bf16 v[108:111], a[140:143], v[156:159], v[108:111]\n"
    "ds_read_b128 v[220:223], v29 offset:44032\n"
    "v_mfma_f32_16x16x32_bf16 v[104:107], a[132:135], v[156:159], v[104:107]\n"
    "buffer_load_dwordx4 v16, s[56:59], s30 offen offset:2048 sc1 lds\n"
    "v_mfma_f32_16x16x32_bf16 v[104:107], a[128:131], v[152:155], v[104:107]\n"
    "v_mfma_f32_16x16x32_bf16 v[112:115], a[128:131], v[160:163], v[112:115]\n"
    "v_mfma_f32_16x16x32_bf16 v[112:115], a[132:135], v[164:167], v[112:115]\n"
    "v_mfma_f32_16x16x32_bf16 v[116:119], a[140:143], v[164:167], v[116:119]\n"
    "v_mfma_f32_16x16x32_bf16 v[116:119], a[136:139], v[160:163], v[116:119]\n"
    "v_mfma_f32_16x16x32_bf16 v[124:127], a[136:139], v[168:171], v[124:127]\n"
    "v_mfma_f32_16x16x32_bf16 v[124:127], a[140:143], v[172:175], v[124:127]\n"
    "v_mfma_f32_16x16x32_bf16 v[120:123], a[132:135], v[172:175], v[120:123]\n"
    "v_mfma_f32_16x16x32_bf16 v[120:123], a[128:131], v[168:171], v[120:123]\n"
    "s_waitcnt vmcnt(3) lgkmcnt(6)\n"
    "s_barrier\n"
    "v_mfma_f32_16x16x32_bf16 v[80:83], a[144:147], v[176:179], v[80:83]\n"
    "ds_read_b64_tr_b16 a[224:225], v21 offset:16384\n"
    "ds_read_b64_tr_b16 a[226:227], v21 offset:20480\n"
    "s_bfe_u32 s31, s27, 0x4001c\n"
    "v_mfma_f32_16x16x32_bf16 v[80:83], a[148:151], v[180:183], v[80:83]\n"
    "ds_read_b64_tr_b16 a[228:229], v21 offset:24576\n"
    "s_lshl_b32 s31, s31, 17\n"
    "v_mfma_f32_16x16x32_bf16 v[84:87], a[156:159], v[180:183], v[84:87]\n"
    "ds_read_b64_tr_b16 a[230:231], v21 offset:28672\n"
    "v_mfma_f32_16x16x32_bf16 v[84:87], a[152:155], v[176:179], v[84:87]\n"
    "ds_read_b128 v[128:131], v29 offset:0\n"
    "s_add_u32 m0, s78, 81920\n"
    "v_mfma_f32_16x16x32_bf16 v[92:95], a[152:155], v[184:187], v[92:95]\n"
    "ds_read_b128 v[132:135], v29 offset:2048\n"
    "v_mfma_f32_16x16x32_bf16 v[92:95], a[156:159], v[188:191], v[92:95]\n"
    "buffer_load_dwordx4 v10, s[52:55], s31 offen offset:0 lds\n"
    "ds_read_b64_tr_b16 a[232:233], v22 offset:16384\n"
    "v_mfma_f32_16x16x32_bf16 v[88:91], a[148:151], v[188:191], v[88:91]\n"
    "ds_read_b64_tr_b16 a[234:235], v22 offset:20480\n"
    "s_waitcnt lgkmcnt(8)\n"
    "v_mfma_f32_16x16x32_bf16 v[88:91], a[144:147], v[184:187], v[88:91]\n"
    "buffer_load_dwordx4 v11, s[52:55], s31 offen offset:1024 lds\n"
    "v_mfma_f32_16x16x32_bf16 v[96:99], a[144:147], v[192:195], v[96:99]\n"
    "ds_read_b64_tr_b16 a[236:237], v22 offset:24576\n"
    "v_mfma_f32_16x16x32_bf16 v[96:99], a[148:151], v[196:199], v[96:99]\n"
    "buffer_load_dwordx4 v12, s[52:55], s31 offen offset:2048 lds\n"
    "v_mfma_f32_16x16x32_bf16 v[100:103], a[156:159], v[196:199], v[100:103]\n"
    "ds_read_b64_tr_b16 a[238:239], v22 offset:28672\n"
    "v_mfma_f32_16x16x32_bf16 v[100:103], a[152:155], v[192:195], v[100:103]\n"
    "buffer_load_dwordx4 v13, s[52:55], s31 offen offset:3072 lds\n"
    "ds_read_b128 v[136:139], v29 offset:1024\n"
    "v_mfma_f32_16x16x32_bf16 v[108:111], a[152:155], v[200:203], v[108:111]\n"
    "ds_read_b128 v[140:143], v29 offset:3072\n"
    "s_bfe_u32 s28, s27, 0x40010\n"
    "v_mfma_f32_16x16x32_bf16 v[108:111], a[156:159], v[204:207], v[108:111]\n"
    "ds_read_b128 v[144:147], v29 offset:4096\n"
    "s_lshl_b32 s28, s28, 14\n"
    "v_mfma_f32_16x16x32_bf16 v[104:107], a[148:151], v[204:207], v[104:107]\n"
    "ds_read_b128 v[148:151], v29 offset:6144\n"
    "v_mfma_f32_16x16x32_bf16 v[104:107], a[144:147], v[200:203], v[104:107]\n"
    "ds_read_b128 v[152:155], v29 offset:5120\n"
    "s_add_u32 s28, s28, s92\n"
    "v_mfma_f32_16x16x32_bf16 v[112:115], a[144:147], v[208:211], v[112:115]\n"
    "ds_read_b128 v[156:159], v29 offset:7168\n"
    "s_add_u32 m0, s79, 0\n"
    "v_mfma_f32_16x16x32_bf16 v[112:115], a[148:151], v[212:215], v[112:115]\n"
    "ds_read_b128 v[160:163], v29 offset:8192\n"
    "ds_read_b128 v[164:167], v29 offset:10240\n"
    "v_mfma_f32_16x16x32_bf16 v[116:119], a[156:159], v[212:215], v[116:119]\n"
    "buffer_load_dwordx4 v14, s[56:59], s28 offen offset:0 sc1 lds\n"
    "v_mfma_f32_16x16x32_bf16 v[116:119], a[152:155], v[208:211], v[116:119]\n"
    "ds_read_b128 v[168:171], v29 offset:9216\n"
    "v_mfma_f32_16x16x32_bf16 v[124:127], a[152:155], v[216:219], v[124:127]\n"
    "buffer_load_dwordx4 v15, s[56:59], s28 offen offset:1024 sc1 lds\n"
    "v_mfma_f32_16x16x32_bf16 v[124:127], a[156:159], v[220:223], v[124:127]\n"
    "ds_read_b128 v[172:175], v29 offset:11264\n"
    "v_mfma_f32_16x16x32_bf16 v[120:123], a[148:151], v[220:223], v[120:123]\n"
    "buffer_load_dwordx4 v16, s[56:59], s28 offen offset:2048 sc1 lds\n"
    "v_mfma_f32_16x16x32_bf16 v[120:123], a[144:147], v[216:219], v[120:123]\n"
    "s_waitcnt vmcnt(7) lgkmcnt(6)\n"
    "s_barrier\n"
    "v_mfma_f32_16x16x32_bf16 v[80:83], a[160:163], v[128:131], v[80:83]\n"
    "ds_read_b128 v[176:179], v31 offset:16384\n"
    "s_bfe_u32 s29, s27, 0x40014\n"
    "v_mfma_f32_16x16x32_bf16 v[80:83], a[164:167], v[132:135], v[80:83]\n"
    "ds_read_b128 v[180:183], v31 offset:18432\n"
    "s_lshl_b32 s29, s29, 14\n"
    "v_mfma_f32_16x16x32_bf16 v[84:87], a[172:175], v[132:135], v[84:87]\n"
    "ds_read_b128 v[184:187], v31 offset:17408\n"
    "s_add_u32 s29, s29, s92\n"
    "v_mfma_f32_16x16x32_bf16 v[84:87], a[168:171], v[128:131], v[84:87]\n"
    "ds_read_b128 v[188:191], v31 offset:19456\n"
    "s_add_u32 m0, s79, 131072\n"
    "v_mfma_f32_16x16x32_bf16 v[92:95], a[168:171], v[136:139], v[92:95]\n"
    "buffer_load_dwordx4 v14, s[56:59], s29 offen offset:0 sc1 lds\n"
    "v_mfma_f32_16x16x32_bf16 v[92:95], a[172:175], v[140:143], v[92:95]\n"
    "ds_read_b128 v[192:195], v31 offset:20480\n"
    "v_mfma_f32_16x16x32_bf16 v[88:91], a[164:167], v[140:143], v[88:91]\n"
    "buffer_load_dwordx4 v15, s[56:59], s29 offen offset:1024 sc1 lds\n"
    "s_waitcnt lgkmcnt(5)\n"
    "v_mfma_f32_16x16x32_bf16 v[88:91], a[160:163], v[136:139], v[88:91]\n"
    "ds_read_b128 v[196:199], v31 offset:22528\n"
    "v_mfma_f32_16x16x32_bf16 v[96:99], a[160:163], v[144:147], v[96:99]\n"
    "buffer_load_dwordx4 v16, s[56:59], s29 offen offset:2048 sc1 lds\n"
    "v_mfma_f32_16x16x32_bf16 v[96:99], a[164:167], v[148:151], v[96:99]\n"
    "ds_read_b128 v[200:203], v31 offset:21504\n"
    "s_bfe_u32 s30, s90, 0x40000\n"
    "v_mfma_f32_16x16x32_bf16 v[100:103], a[172:175], v[148:151], v[100:103]\n"
    "ds_read_b128 v[204:207], v31 offset:23552\n"
    "s_lshl_b32 s30, s30, 14\n"
    "s_add_u32 s30, s30, s92\n"
    "v_mfma_f32_16x16x32_bf16 v[100:103], a[168:171], v[144:147], v[100:103]\n"
    "ds_read_b128 v[208:211], v31 offset:24576\n"
    "s_add_u32 m0, s78, 32768\n"
    "v_mfma_f32_16x16x32_bf16 v[108:111], a[168:171], v[152:155], v[108:111]\n"
    "ds_read_b128 v[212:215], v31 offset:26624\n"
    "v_mfma_f32_16x16x32_bf16 v[108:111], a[172:175], v[156:159], v[108:111]\n"
    "buffer_load_dwordx4 v17, s[56:59], s30 offen offset:0 sc1 lds\n"
    "v_mfma_f32_16x16x32_bf16 v[104:107], a[164:167], v[156:159], v[104:107]\n"
    "ds_read_b128 v[216:219], v31 offset:25600\n"
    "v_mfma_f32_16x16x32_bf16 v[104:107], a[160:163], v[152:155], v[104:107]\n"
    "buffer_load_dwordx4 v17, s[56:59], s30 offen offset:1024 sc1 lds\n"
    "v_mfma_f32_16x16x32_bf16 v[112:115], a[160:163], v[160:163], v[112:115]\n"
    "ds_read_b128 v[220:223], v31 offset:27648\n"
    "v_mfma_f32_16x16x32_bf16 v[112:115], a[164:167], v[164:167], v[112:115]\n"
    "buffer_load_dwordx4 v17, s[56:59], s30 offen offset:2048 sc1 lds\n"
    "v_mfma_f32_16x16x32_bf16 v[116:119], a[172:175], v[164:167], v[116:119]\n"
    "buffer_load_dwordx4 v17, s[56:59], s30 offen offset:3072 sc1 lds\n"
    "v_mfma_f32_16x16x32_bf16 v[116:119], a[168:171], v[160:163], v[116:119]\n"
    "v_mfma_f32_16x16x32_bf16 v[124:127], a[168:171], v[168:171], v[124:127]\n"
    "v_mfma_f32_16x16x32_bf16 v[124:127], a[172:175], v[172:175], v[124:127]\n"
    "v_mfma_f32_16x16x32_bf16 v[120:123], a[164:167], v[172:175], v[120:123]\n"
    "v_mfma_f32_16x16x32_bf16 v[120:123], a[160:163], v[168:171], v[120:123]\n"
    "s_waitcnt vmcnt(7) lgkmcnt(6)\n"
    "s_barrier\n"
    "v_mfma_f32_16x16x32_bf16 v[80:83], a[176:179], v[176:179], v[80:83]\n"
    "ds_read_b64_tr_b16 a[240:241], v23 offset:16384\n"
    "ds_read_b64_tr_b16 a[242:243], v23 offset:20480\n"
    "s_bfe_u32 s31, s27, 0x40018\n"
    "v_mfma_f32_16x16x32_bf16 v[80:83], a[180:183], v[180:183], v[80:83]\n"
    "ds_read_b64_tr_b16 a[244:245], v23 offset:24576\n"
    "s_lshl_b32 s31, s31, 14\n"
    "v_mfma_f32_16x16x32_bf16 v[84:87], a[188:191], v[180:183], v[84:87]\n"
    "ds_read_b64_tr_b16 a[246:247], v23 offset:28672\n"
    "s_add_u32 s31, s31, s92\n"
    "v_mfma_f32_16x16x32_bf16 v[84:87], a[184:187], v[176:179], v[84:87]\n"
    "ds_read_b128 v[128:131], v27 offset:0\n"
    "v_mfma_f32_16x16x32_bf16 v[92:95], a[184:187], v[184:187], v[92:95]\n"
    "ds_read_b128 v[132:135], v27 offset:2048\n"
    "s_add_u32 m0, s79, 114688\n"
    "v_mfma_f32_16x16x32_bf16 v[92:95], a[188:191], v[188:191], v[92:95]\n"
    "ds_read_b64_tr_b16 a[248:249], v24 offset:16384\n"
    "buffer_load_dwordx4 v14, s[56:59], s31 offen offset:0 sc1 lds\n"
    "v_mfma_f32_16x16x32_bf16 v[88:91], a[180:183], v[188:191], v[88:91]\n"
    "ds_read_b64_tr_b16 a[250:251], v24 offset:20480\n"
    "s_waitcnt lgkmcnt(8)\n"
    "v_mfma_f32_16x16x32_bf16 v[88:91], a[176:179], v[184:187], v[88:91]\n"
    "buffer_load_dwordx4 v15, s[56:59], s31 offen offset:1024 sc1 lds\n"
    "v_mfma_f32_16x16x32_bf16 v[96:99], a[176:179], v[192:195], v[96:99]\n"
    "ds_read_b64_tr_b16 a[252:253], v24 offset:24576\n"
    "v_mfma_f32_16x16x32_bf16 v[96:99], a[180:183], v[196:199], v[96:99]\n"
    "ds_read_b64_tr_b16 a[254:255], v24 offset:28672\n"
    "v_mfma_f32_16x16x32_bf16 v[100:103], a[188:191], v[196:199], v[100:103]\n"
    "buffer_load_dwordx4 v16, s[56:59], s31 offen offset:2048 sc1 lds\n"
    "v_mfma_f32_16x16x32_bf16 v[100:103], a[184:187], v[192:195], v[100:103]\n"
    "ds_read_b128 v[136:139], v27 offset:1024\n"
    "s_bfe_u32 s28, s90, 0x40010\n"
    "ds_read_b128 v[140:143], v27 offset:3072\n"
    "s_lshl_b32 s28, s28, 14\n"
    "v_mfma_f32_16x16x32_bf16 v[108:111], a[184:187], v[200:203], v[108:111]\n"
    "ds_read_b128 v[144:147], v27 offset:4096\n"
    "v_mfma_f32_16x16x32_bf16 v[108:111], a[188:191], v[204:207], v[108:111]\n"
    "ds_read_b128 v[148:151], v27 offset:6144\n"
    "s_add_u32 s28, s28, s92\n"
    "v_mfma_f32_16x16x32_bf16 v[104:107], a[180:183], v[204:207], v[104:107]\n"
    "ds_read_b128 v[152:155], v27 offset:5120\n"
    "s_add_u32 m0, s78, 49152\n"
    "v_mfma_f32_16x16x32_bf16 v[104:107], a[176:179], v[200:203], v[104:107]\n"
    "ds_read_b128 v[156:159], v27 offset:7168\n"
    "v_mfma_f32_16x16x32_bf16 v[112:115], a[176:179], v[208:211], v[112:115]\n"
    "buffer_load_dwordx4 v17, s[56:59], s28 offen offset:0 sc1 lds\n"
    "v_mfma_f32_16x16x32_bf16 v[112:115], a[180:183], v[212:215], v[112:115]\n"
    "ds_read_b128 v[160:163], v27 offset:8192\n"
    "ds_read_b128 v[164:167], v27 offset:10240\n"
    "v_mfma_f32_16x16x32_bf16 v[116:119], a[188:191], v[212:215], v[116:119]\n"
    "buffer_load_dwordx4 v17, s[56:59], s28 offen offset:1024 sc1 lds\n"
    "v_mfma_f32_16x16x32_bf16 v[116:119], a[184:187], v[208:211], v[116:119]\n"
    "ds_read_b128 v[168:171], v27 offset:9216\n"
    "v_mfma_f32_16x16x32_bf16 v[124:127], a[184:187], v[216:219], v[124:127]\n"
    "buffer_load_dwordx4 v17, s[56:59], s28 offen offset:2048 sc1 lds\n"
    "v_mfma_f32_16x16x32_bf16 v[124:127], a[188:191], v[220:223], v[124:127]\n"
    "ds_read_b128 v[172:175], v27 offset:11264\n"
    "v_mfma_f32_16x16x32_bf16 v[120:123], a[180:183], v[220:223], v[120:123]\n"
    "buffer_load_dwordx4 v17, s[56:59], s28 offen offset:3072 sc1 lds\n"
    "v_mfma_f32_16x16x32_bf16 v[120:123], a[176:179], v[216:219], v[120:123]\n"
    "s_waitcnt vmcnt(11) lgkmcnt(6)\n"
    "s_barrier\n"
    "v_mfma_f32_16x16x32_bf16 v[80:83], a[192:195], v[128:131], v[80:83]\n"
    "ds_read_b128 v[176:179], v31 offset:0\n"
    "s_bfe_u32 s29, s27, 0x4001c\n"
    "v_mfma_f32_16x16x32_bf16 v[80:83], a[196:199], v[132:135], v[80:83]\n"
    "ds_read_b128 v[180:183], v31 offset:2048\n"
    "s_lshl_b32 s29, s29, 14\n"
    "v_mfma_f32_16x16x32_bf16 v[84:87], a[204:207], v[132:135], v[84:87]\n"
    "ds_read_b128 v[184:187], v31 offset:1024\n"
    "s_add_u32 s29, s29, s92\n"
    "v_mfma_f32_16x16x32_bf16 v[84:87], a[200:203], v[128:131], v[84:87]\n"
    "ds_read_b128 v[188:191], v31 offset:3072\n"
    "s_add_u32 m0, s79, 98304\n"
    "v_mfma_f32_16x16x32_bf16 v[92:95], a[200:203], v[136:139], v[92:95]\n"
    "buffer_load_dwordx4 v14, s[56:59], s29 offen offset:0 sc1 lds\n"
    "v_mfma_f32_16x16x32_bf16 v[92:95], a[204:207], v[140:143], v[92:95]\n"
    "ds_read_b128 v[192:195], v31 offset:4096\n"
    "v_mfma_f32_16x16x32_bf16 v[88:91], a[196:199], v[140:143], v[88:91]\n"
    "buffer_load_dwordx4 v15, s[56:59], s29 offen offset:1024 sc1 lds\n"
    "s_waitcnt lgkmcnt(5)\n"
    "v_mfma_f32_16x16x32_bf16 v[88:91], a[192:195], v[136:139], v[88:91]\n"
    "ds_read_b128 v[196:199], v31 offset:6144\n"
    "v_mfma_f32_16x16x32_bf16 v[96:99], a[192:195], v[144:147], v[96:99]\n"
    "buffer_load_dwordx4 v16, s[56:59], s29 offen offset:2048 sc1 lds\n"
    "v_mfma_f32_16x16x32_bf16 v[96:99], a[196:199], v[148:151], v[96:99]\n"
    "ds_read_b128 v[200:203], v31 offset:5120\n"
    "s_bfe_u32 s30, s91, 0x40000\n"
    "v_mfma_f32_16x16x32_bf16 v[100:103], a[204:207], v[148:151], v[100:103]\n"
    "ds_read_b128 v[204:207], v31 offset:7168\n"
    "s_lshl_b32 s30, s30, 14\n"
    "s_add_u32 s30, s30, s92\n"
    "v_mfma_f32_16x16x32_bf16 v[100:103], a[200:203], v[144:147], v[100:103]\n"
    "ds_read_b128 v[208:211], v31 offset:8192\n"
    "s_add_u32 m0, s78, 65536\n"
    "v_mfma_f32_16x16x32_bf16 v[108:111], a[200:203], v[152:155], v[108:111]\n"
    "ds_read_b128 v[212:215], v31 offset:10240\n"
    "v_mfma_f32_16x16x32_bf16 v[108:111], a[204:207], v[156:159], v[108:111]\n"
    "buffer_load_dwordx4 v17, s[56:59], s30 offen offset:0 sc1 lds\n"
    "v_mfma_f32_16x16x32_bf16 v[104:107], a[196:199], v[156:159], v[104:107]\n"
    "ds_read_b128 v[216:219], v31 offset:9216\n"
    "v_mfma_f32_16x16x32_bf16 v[104:107], a[192:195], v[152:155], v[104:107]\n"
    "buffer_load_dwordx4 v17, s[56:59], s30 offen offset:1024 sc1 lds\n"
    "v_mfma_f32_16x16x32_bf16 v[112:115], a[192:195], v[160:163], v[112:115]\n"
    "ds_read_b128 v[220:223], v31 offset:11264\n"
    "v_mfma_f32_16x16x32_bf16 v[112:115], a[196:199], v[164:167], v[112:115]\n"
    "buffer_load_dwordx4 v17, s[56:59], s30 offen offset:2048 sc1 lds\n"
    "v_mfma_f32_16x16x32_bf16 v[116:119], a[204:207], v[164:167], v[116:119]\n"
    "buffer_load_dwordx4 v17, s[56:59], s30 offen offset:3072 sc1 lds\n"
    "v_mfma_f32_16x16x32_bf16 v[116:119], a[200:203], v[160:163], v[116:119]\n"
    "v_mfma_f32_16x16x32_bf16 v[124:127], a[200:203], v[168:171], v[124:127]\n"
    "v_mfma_f32_16x16x32_bf16 v[124:127], a[204:207], v[172:175], v[124:127]\n"
    "v_mfma_f32_16x16x32_bf16 v[120:123], a[196:199], v[172:175], v[120:123]\n"
    "v_mfma_f32_16x16x32_bf16 v[120:123], a[192:195], v[168:171], v[120:123]\n"
    "s_waitcnt vmcnt(11) lgkmcnt(6)\n"
    "s_barrier\n"
    "v_mfma_f32_16x16x32_bf16 v[80:83], a[208:211], v[176:179], v[80:83]\n"
    "ds_read_b128 v[128:131], v29 offset:49152\n"
    "v_mfma_f32_16x16x32_bf16 v[80:83], a[212:215], v[180:183], v[80:83]\n"
    "ds_read_b128 v[132:135], v29 offset:51200\n"
    "s_bfe_u32 s31, s91, 0x40010\n"
    "v_mfma_f32_16x16x32_bf16 v[84:87], a[220:223], v[180:183], v[84:87]\n"
    "ds_read_b128 v[136:139], v29 offset:50176\n"
    "s_lshl_b32 s31, s31, 14\n"
    "v_mfma_f32_16x16x32_bf16 v[84:87], a[216:219], v[176:179], v[84:87]\n"
    "ds_read_b128 v[140:143], v29 offset:52224\n"
    "v_mfma_f32_16x16x32_bf16 v[92:95], a[216:219], v[184:187], v[92:95]\n"
    "ds_read_b128 v[144:147], v29 offset:53248\n"
    "s_add_u32 s31, s31, s92\n"
    "v_mfma_f32_16x16x32_bf16 v[92:95], a[220:223], v[188:191], v[92:95]\n"
    "ds_read_b128 v[148:151], v29 offset:55296\n"
    "s_add_u32 m0, s78, 16384\n"
    "v_mfma_f32_16x16x32_bf16 v[88:91], a[212:215], v[188:191], v[88:91]\n"
    "ds_read_b128 v[152:155], v29 offset:54272\n"
    "s_waitcnt lgkmcnt(7)\n"
    "v_mfma_f32_16x16x32_bf16 v[88:91], a[208:211], v[184:187], v[88:91]\n"
    "ds_read_b128 v[156:159], v29 offset:56320\n"
    "v_mfma_f32_16x16x32_bf16 v[96:99], a[208:211], v[192:195], v[96:99]\n"
    "buffer_load_dwordx4 v17, s[56:59], s31 offen offset:0 sc1 lds\n"
    "v_mfma_f32_16x16x32_bf16 v[96:99], a[212:215], v[196:199], v[96:99]\n"
    "ds_read_b128 v[160:163], v29 offset:57344\n"
    "v_mfma_f32_16x16x32_bf16 v[100:103], a[220:223], v[196:199], v[100:103]\n"
    "buffer_load_dwordx4 v17, s[56:59], s31 offen offset:1024 sc1 lds\n"
    "v_mfma_f32_16x16x32_bf16 v[100:103], a[216:219], v[192:195], v[100:103]\n"
    "ds_read_b128 v[164:167], v29 offset:59392\n"
    "v_mfma_f32_16x16x32_bf16 v[108:111], a[216:219], v[200:203], v[108:111]\n"
    "ds_read_b128 v[168:171], v29 offset:58368\n"
    "v_mfma_f32_16x16x32_bf16 v[108:111], a[220:223], v[204:207], v[108:111]\n"
    "buffer_load_dwordx4 v17, s[56:59], s31 offen offset:2048 sc1 lds\n"
    "v_mfma_f32_16x16x32_bf16 v[104:107], a[212:215], v[204:207], v[104:107]\n"
    "ds_read_b128 v[172:175], v29 offset:60416\n"
    "v_mfma_f32_16x16x32_bf16 v[104:107], a[208:211], v[200:203], v[104:107]\n"
    "buffer_load_dwordx4 v17, s[56:59], s31 offen offset:3072 sc1 lds\n"
    "v_mfma_f32_16x16x32_bf16 v[112:115], a[208:211], v[208:211], v[112:115]\n"
    "v_mfma_f32_16x16x32_bf16 v[112:115], a[212:215], v[212:215], v[112:115]\n"
    "v_mfma_f32_16x16x32_bf16 v[116:119], a[220:223], v[212:215], v[116:119]\n"
    "v_mfma_f32_16x16x32_bf16 v[116:119], a[216:219], v[208:211], v[116:119]\n"
    "v_mfma_f32_16x16x32_bf16 v[124:127], a[216:219], v[216:219], v[124:127]\n"
    "v_mfma_f32_16x16x32_bf16 v[124:127], a[220:223], v[220:223], v[124:127]\n"
    "v_mfma_f32_16x16x32_bf16 v[120:123], a[212:215], v[220:223], v[120:123]\n"
    "v_mfma_f32_16x16x32_bf16 v[120:123], a[208:211], v[216:219], v[120:123]\n"
    "s_waitcnt vmcnt(8) lgkmcnt(6)\n"
    "s_barrier\n"
    "v_mfma_f32_16x16x32_bf16 v[80:83], a[224:227], v[128:131], v[80:83]\n"
    "ds_read_b128 v[176:179], v29 offset:32768\n"
    "v_mfma_f32_16x16x32_bf16 v[80:83], a[228:231], v[132:135], v[80:83]\n"
    "ds_read_b128 v[180:183], v29 offset:34816\n"
    "v_mfma_f32_16x16x32_bf16 v[84:87], a[236:239], v[132:135], v[84:87]\n"
    "ds_read_b128 v[184:187], v29 offset:33792\n"
    "v_mfma_f32_16x16x32_bf16 v[84:87], a[232:235], v[128:131], v[84:87]\n"
    "ds_read_b128 v[188:191], v29 offset:35840\n"
    "v_mfma_f32_16x16x32_bf16 v[92:95], a[232:235], v[136:139], v[92:95]\n"
    "ds_read_b128 v[192:195], v29 offset:36864\n"
    "v_mfma_f32_16x16x32_bf16 v[92:95], a[236:239], v[140:143], v[92:95]\n"
    "ds_read_b128 v[196:199], v29 offset:38912\n"
    "v_mfma_f32_16x16x32_bf16 v[88:91], a[228:231], v[140:143], v[88:91]\n"
    "ds_read_b128 v[200:203], v29 offset:37888\n"
    "s_waitcnt lgkmcnt(7)\n"
    "v_mfma_f32_16x16x32_bf16 v[88:91], a[224:227], v[136:139], v[88:91]\n"
    "ds_read_b128 v[204:207], v29 offset:39936\n"
    "v_mfma_f32_16x16x32_bf16 v[96:99], a[224:227], v[144:147], v[96:99]\n"
    "ds_read_b128 v[208:211], v29 offset:40960\n"
    "v_mfma_f32_16x16x32_bf16 v[96:99], a[228:231], v[148:151], v[96:99]\n"
    "ds_read_b128 v[212:215], v29 offset:43008\n"
    "v_mfma_f32_16x16x32_bf16 v[100:103], a[236:239], v[148:151], v[100:103]\n"
    "ds_read_b128 v[216:219], v29 offset:41984\n"
    "v_mfma_f32_16x16x32_bf16 v[100:103], a[232:235], v[144:147], v[100:103]\n"
    "ds_read_b128 v[220:223], v29 offset:44032\n"
    "v_mfma_f32_16x16x32_bf16 v[108:111], a[232:235], v[152:155], v[108:111]\n"
    "v_mfma_f32_16x16x32_bf16 v[108:111], a[236:239], v[156:159], v[108:111]\n"
    "v_mfma_f32_16x16x32_bf16 v[104:107], a[228:231], v[156:159], v[104:107]\n"
    "v_mfma_f32_16x16x32_bf16 v[104:107], a[224:227], v[152:155], v[104:107]\n"
    "v_mfma_f32_16x16x32_bf16 v[112:115], a[224:227], v[160:163], v[112:115]\n"
    "v_mfma_f32_16x16x32_bf16 v[112:115], a[228:231], v[164:167], v[112:115]\n"
    "v_mfma_f32_16x16x32_bf16 v[116:119], a[236:239], v[164:167], v[116:119]\n"
    "v_mfma_f32_16x16x32_bf16 v[116:119], a[232:235], v[160:163], v[116:119]\n"
    "v_mfma_f32_16x16x32_bf16 v[124:127], a[232:235], v[168:171], v[124:127]\n"
    "v_mfma_f32_16x16x32_bf16 v[124:127], a[236:239], v[172:175], v[124:127]\n"
    "v_mfma_f32_16x16x32_bf16 v[120:123], a[228:231], v[172:175], v[120:123]\n"
    "v_mfma_f32_16x16x32_bf16 v[120:123], a[224:227], v[168:171], v[120:123]\n"
    "s_waitcnt vmcnt(11) lgkmcnt(0)\n"
    "s_barrier\n"
    "v_mfma_f32_16x16x32_bf16 v[80:83], a[240:243], v[176:179], v[80:83]\n"
    "ds_read_b128 v[128:131], v27 offset:32768\n"
    "v_mfma_f32_16x16x32_bf16 v[80:83], a[244:247], v[180:183], v[80:83]\n"
    "ds_read_b128 v[132:135], v27 offset:34816\n"
    "v_mfma_f32_16x16x32_bf16 v[84:87], a[252:255], v[180:183], v[84:87]\n"
    "ds_read_b128 v[136:139], v27 offset:36864\n"
    "v_mfma_f32_16x16x32_bf16 v[84:87], a[248:251], v[176:179], v[84:87]\n"
    "ds_read_b128 v[140:143], v27 offset:38912\n"
    "v_mfma_f32_16x16x32_bf16 v[92:95], a[248:251], v[184:187], v[92:95]\n"
    "ds_read_b128 v[144:147], v27 offset:33792\n"
    "v_mfma_f32_16x16x32_bf16 v[92:95], a[252:255], v[188:191], v[92:95]\n"
    "ds_read_b128 v[148:151], v27 offset:35840\n"
    "v_mfma_f32_16x16x32_bf16 v[88:91], a[244:247], v[188:191], v[88:91]\n"
    "ds_read_b128 v[152:155], v27 offset:37888\n"
    "v_mfma_f32_16x16x32_bf16 v[88:91], a[240:243], v[184:187], v[88:91]\n"
    "ds_read_b128 v[156:159], v27 offset:39936\n"
    "v_mfma_f32_16x16x32_bf16 v[96:99], a[240:243], v[192:195], v[96:99]\n"
    "v_cvt_pk_bf16_f32 v232, v80, v81\n"
    "v_cvt_pk_bf16_f32 v233, v82, v83\n"
    "v_mfma_f32_16x16x32_bf16 v[96:99], a[244:247], v[196:199], v[96:99]\n"
    "v_cvt_pk_bf16_f32 v234, v84, v85\n"
    "v_cvt_pk_bf16_f32 v235, v86, v87\n"
    "v_mfma_f32_16x16x32_bf16 v[100:103], a[252:255], v[196:199], v[100:103]\n"
    "buffer_store_dwordx4 v[232:235], v41, s[56:59], s65 offen offset:0 sc1\n"
    "v_mfma_f32_16x16x32_bf16 v[100:103], a[248:251], v[192:195], v[100:103]\n"
    "v_mfma_f32_16x16x32_bf16 v[108:111], a[248:251], v[200:203], v[108:111]\n"
    "v_cvt_pk_bf16_f32 v236, v88, v89\n"
    "v_cvt_pk_bf16_f32 v237, v90, v91\n"
    "v_mfma_f32_16x16x32_bf16 v[108:111], a[252:255], v[204:207], v[108:111]\n"
    "v_cvt_pk_bf16_f32 v238, v92, v93\n"
    "v_cvt_pk_bf16_f32 v239, v94, v95\n"
    "v_mfma_f32_16x16x32_bf16 v[104:107], a[244:247], v[204:207], v[104:107]\n"
    "buffer_store_dwordx4 v[236:239], v41, s[56:59], s65 offen offset:1024 sc1\n"
    "v_mfma_f32_16x16x32_bf16 v[104:107], a[240:243], v[200:203], v[104:107]\n"
    "v_mfma_f32_16x16x32_bf16 v[112:115], a[240:243], v[208:211], v[112:115]\n"
    "v_cvt_pk_bf16_f32 v240, v96, v97\n"
    "v_cvt_pk_bf16_f32 v241, v98, v99\n"
    "v_mfma_f32_16x16x32_bf16 v[112:115], a[244:247], v[212:215], v[112:115]\n"
    "v_cvt_pk_bf16_f32 v242, v100, v101\n"
    "v_cvt_pk_bf16_f32 v243, v102, v103\n"
    "v_mfma_f32_16x16x32_bf16 v[116:119], a[252:255], v[212:215], v[116:119]\n"
    "buffer_store_dwordx4 v[240:243], v41, s[56:59], s66 offen offset:0 sc1\n"
    "v_mfma_f32_16x16x32_bf16 v[116:119], a[248:251], v[208:211], v[116:119]\n"
    "v_mfma_f32_16x16x32_bf16 v[124:127], a[248:251], v[216:219], v[124:127]\n"
    "v_cvt_pk_bf16_f32 v244, v104, v105\n"
    "v_cvt_pk_bf16_f32 v245, v106, v107\n"
    "v_mfma_f32_16x16x32_bf16 v[124:127], a[252:255], v[220:223], v[124:127]\n"
    "v_cvt_pk_bf16_f32 v246, v108, v109\n"
    "v_cvt_pk_bf16_f32 v247, v110, v111\n"
    "v_mfma_f32_16x16x32_bf16 v[120:123], a[244:247], v[220:223], v[120:123]\n"
    "buffer_store_dwordx4 v[244:247], v41, s[56:59], s66 offen offset:1024 sc1\n"
    "v_mfma_f32_16x16x32_bf16 v[120:123], a[240:243], v[216:219], v[120:123]\n"
    "v_cvt_pk_bf16_f32 v248, v112, v113\n"
    "v_cvt_pk_bf16_f32 v249, v114, v115\n"
    "v_cvt_pk_bf16_f32 v250, v116, v117\n"
    "v_cvt_pk_bf16_f32 v251, v118, v119\n"
    "buffer_store_dwordx4 v[248:251], v41, s[56:59], s67 offen offset:0 sc1\n"
    "s_nop 2\n"
    "v_cvt_pk_bf16_f32 v252, v120, v121\n"
    "v_cvt_pk_bf16_f32 v253, v122, v123\n"
    "v_cvt_pk_bf16_f32 v254, v124, v125\n"
    "v_cvt_pk_bf16_f32 v255, v126, v127\n"
    "buffer_store_dwordx4 v[252:255], v41, s[56:59], s67 offen offset:1024 sc1\n"
    "s_waitcnt lgkmcnt(0)\n"
    "v_mfma_f32_16x16x32_bf16 v[64:67], a[0:3], v[128:131], 0\n"
    "ds_read_b128 v[192:195], v27 offset:40960\n"
    "v_mfma_f32_16x16x32_bf16 v[64:67], a[4:7], v[132:135], v[64:67]\n"
    "ds_read_b128 v[196:199], v27 offset:43008\n"
    "v_mfma_f32_16x16x32_bf16 v[64:67], a[16:19], v[136:139], v[64:67]\n"
    "ds_read_b128 v[200:203], v27 offset:45056\n"
    "v_mfma_f32_16x16x32_bf16 v[64:67], a[20:23], v[140:143], v[64:67]\n"
    "ds_read_b128 v[204:207], v27 offset:47104\n"
    "v_mfma_f32_16x16x32_bf16 v[68:71], a[28:31], v[140:143], 0\n"
    "ds_read_b128 v[208:211], v27 offset:41984\n"
    "v_mfma_f32_16x16x32_bf16 v[68:71], a[24:27], v[136:139], v[68:71]\n"
    "ds_read_b128 v[212:215], v27 offset:44032\n"
    "v_mfma_f32_16x16x32_bf16 v[68:71], a[12:15], v[132:135], v[68:71]\n"
    "ds_read_b128 v[216:219], v27 offset:46080\n"
    "v_mfma_f32_16x16x32_bf16 v[68:71], a[8:11], v[128:131], v[68:71]\n"
    "ds_read_b128 v[220:223], v27 offset:48128\n"
    "v_mfma_f32_16x16x32_bf16 v[76:79], a[8:11], v[144:147], 0\n"
    "v_mfma_f32_16x16x32_bf16 v[76:79], a[12:15], v[148:151], v[76:79]\n"
    "v_mfma_f32_16x16x32_bf16 v[76:79], a[24:27], v[152:155], v[76:79]\n"
    "v_mfma_f32_16x16x32_bf16 v[76:79], a[28:31], v[156:159], v[76:79]\n"
    "v_mfma_f32_16x16x32_bf16 v[72:75], a[20:23], v[156:159], 0\n"
    "v_mfma_f32_16x16x32_bf16 v[72:75], a[16:19], v[152:155], v[72:75]\n"
    "v_mfma_f32_16x16x32_bf16 v[72:75], a[4:7], v[148:151], v[72:75]\n"
    "v_mfma_f32_16x16x32_bf16 v[72:75], a[0:3], v[144:147], v[72:75]\n"
    "s_waitcnt lgkmcnt(0)\n"
    "v_mfma_f32_16x16x32_bf16 v[64:67], a[32:35], v[192:195], v[64:67]\n"
    "ds_read_b128 v[128:131], v27 offset:49152\n"
    "v_mfma_f32_16x16x32_bf16 v[64:67], a[36:39], v[196:199], v[64:67]\n"
    "ds_read_b128 v[132:135], v27 offset:51200\n"
    "v_mfma_f32_16x16x32_bf16 v[64:67], a[48:51], v[200:203], v[64:67]\n"
    "ds_read_b128 v[136:139], v27 offset:53248\n"
    "v_mfma_f32_16x16x32_bf16 v[64:67], a[52:55], v[204:207], v[64:67]\n"
    "ds_read_b128 v[140:143], v27 offset:55296\n"
    "v_mfma_f32_16x16x32_bf16 v[68:71], a[60:63], v[204:207], v[68:71]\n"
    "ds_read_b128 v[144:147], v27 offset:50176\n"
    "v_mfma_f32_16x16x32_bf16 v[68:71], a[56:59], v[200:203], v[68:71]\n"
    "ds_read_b128 v[148:151], v27 offset:52224\n"
    "v_mfma_f32_16x16x32_bf16 v[68:71], a[44:47], v[196:199], v[68:71]\n"
    "ds_read_b128 v[152:155], v27 offset:54272\n"
    "v_mfma_f32_16x16x32_bf16 v[68:71], a[40:43], v[192:195], v[68:71]\n"
    "ds_read_b128 v[156:159], v27 offset:56320\n"
    "v_mfma_f32_16x16x32_bf16 v[76:79], a[40:43], v[208:211], v[76:79]\n"
    "v_mfma_f32_16x16x32_bf16 v[76:79], a[44:47], v[212:215], v[76:79]\n"
    "v_mfma_f32_16x16x32_bf16 v[76:79], a[56:59], v[216:219], v[76:79]\n"
    "v_mfma_f32_16x16x32_bf16 v[76:79], a[60:63], v[220:223], v[76:79]\n"
    "v_mfma_f32_16x16x32_bf16 v[72:75], a[52:55], v[220:223], v[72:75]\n"
    "v_mfma_f32_16x16x32_bf16 v[72:75], a[48:51], v[216:219], v[72:75]\n"
    "v_mfma_f32_16x16x32_bf16 v[72:75], a[36:39], v[212:215], v[72:75]\n"
    "v_mfma_f32_16x16x32_bf16 v[72:75], a[32:35], v[208:211], v[72:75]\n"
    "s_waitcnt lgkmcnt(0)\n"
    "v_mfma_f32_16x16x32_bf16 v[64:67], a[64:67], v[128:131], v[64:67]\n"
    "ds_read_b128 v[192:195], v27 offset:57344\n"
    "v_mfma_f32_16x16x32_bf16 v[64:67], a[68:71], v[132:135], v[64:67]\n"
    "ds_read_b128 v[196:199], v27 offset:59392\n"
    "v_mfma_f32_16x16x32_bf16 v[64:67], a[80:83], v[136:139], v[64:67]\n"
    "ds_read_b128 v[200:203], v27 offset:61440\n"
    "v_mfma_f32_16x16x32_bf16 v[64:67], a[84:87], v[140:143], v[64:67]\n"
    "ds_read_b128 v[204:207], v27 offset:63488\n"
    "v_mfma_f32_16x16x32_bf16 v[68:71], a[92:95], v[140:143], v[68:71]\n"
    "ds_read_b128 v[208:211], v27 offset:58368\n"
    "v_mfma_f32_16x16x32_bf16 v[68:71], a[88:91], v[136:139], v[68:71]\n"
    "ds_read_b128 v[212:215], v27 offset:60416\n"
    "v_mfma_f32_16x16x32_bf16 v[68:71], a[76:79], v[132:135], v[68:71]\n"
    "ds_read_b128 v[216:219], v27 offset:62464\n"
    "v_mfma_f32_16x16x32_bf16 v[68:71], a[72:75], v[128:131], v[68:71]\n"
    "ds_read_b128 v[220:223], v27 offset:64512\n"
    "v_mfma_f32_16x16x32_bf16 v[76:79], a[72:75], v[144:147], v[76:79]\n"
    "s_waitcnt vmcnt(0)\n"
    "buffer_store_dwordx4 v[56:59], v42, s[56:59], 0 offen sc1\n"
    "v_mfma_f32_16x16x32_bf16 v[76:79], a[76:79], v[148:151], v[76:79]\n"
    "v_mfma_f32_16x16x32_bf16 v[76:79], a[88:91], v[152:155], v[76:79]\n"
    "v_mfma_f32_16x16x32_bf16 v[76:79], a[92:95], v[156:159], v[76:79]\n"
    "v_mfma_f32_16x16x32_bf16 v[72:75], a[84:87], v[156:159], v[72:75]\n"
    "Lw11_%=:\n"
    "s_cbranch_execz Lret0_%=\n"
    "v_mfma_f32_16x16x32_bf16 v[72:75], a[80:83], v[152:155], v[72:75]\n"
    "v_mfma_f32_16x16x32_bf16 v[72:75], a[68:71], v[148:151], v[72:75]\n"
    "v_mfma_f32_16x16x32_bf16 v[72:75], a[64:67], v[144:147], v[72:75]\n"
    "s_waitcnt vmcnt(7) lgkmcnt(0)\n"
    "s_barrier\n"
    "v_mfma_f32_16x16x32_bf16 v[64:67], a[96:99], v[192:195], v[64:67]\n"
    "ds_read_b128 v[128:131], v29 offset:0\n"
    "v_mfma_f32_16x16x32_bf16 v[64:67], a[100:103], v[196:199], v[64:67]\n"
    "ds_read_b128 v[132:135], v29 offset:2048\n"
    "v_mfma_f32_16x16x32_bf16 v[64:67], a[112:115], v[200:203], v[64:67]\n"
    "ds_read_b128 v[136:139], v29 offset:4096\n"
    "v_mfma_f32_16x16x32_bf16 v[64:67], a[116:119], v[204:207], v[64:67]\n"
    "ds_read_b128 v[140:143], v29 offset:6144\n"
    "v_mfma_f32_16x16x32_bf16 v[68:71], a[124:127], v[204:207], v[68:71]\n"
    "ds_read_b128 v[144:147], v29 offset:1024\n"
    "v_mfma_f32_16x16x32_bf16 v[68:71], a[120:123], v[200:203], v[68:71]\n"
    "ds_read_b128 v[148:151], v29 offset:3072\n"
    "v_mfma_f32_16x16x32_bf16 v[68:71], a[108:111], v[196:199], v[68:71]\n"
    "ds_read_b128 v[152:155], v29 offset:5120\n"
    "v_mfma_f32_16x16x32_bf16 v[68:71], a[104:107], v[192:195], v[68:71]\n"
    "ds_read_b128 v[156:159], v29 offset:7168\n"
    "v_mfma_f32_16x16x32_bf16 v[76:79], a[104:107], v[208:211], v[76:79]\n"
    "v_mfma_f32_16x16x32_bf16 v[76:79], a[108:111], v[212:215], v[76:79]\n"
    "v_mfma_f32_16x16x32_bf16 v[76:79], a[120:123], v[216:219], v[76:79]\n"
    "v_mfma_f32_16x16x32_bf16 v[76:79], a[124:127], v[220:223], v[76:79]\n"
    "v_mfma_f32_16x16x32_bf16 v[72:75], a[116:119], v[220:223], v[72:75]\n"
    "v_mfma_f32_16x16x32_bf16 v[72:75], a[112:115], v[216:219], v[72:75]\n"
    "v_mfma_f32_16x16x32_bf16 v[72:75], a[100:103], v[212:215], v[72:75]\n"
    "v_mfma_f32_16x16x32_bf16 v[72:75], a[96:99], v[208:211], v[72:75]\n"
    "s_waitcnt lgkmcnt(0)\n"
    "v_mfma_f32_16x16x32_bf16 v[64:67], a[128:131], v[128:131], v[64:67]\n"
    "ds_read_b128 v[192:195], v29 offset:8192\n"
    "v_mfma_f32_16x16x32_bf16 v[64:67], a[132:135], v[132:135], v[64:67]\n"
    "ds_read_b128 v[196:199], v29 offset:10240\n"
    "v_mfma_f32_16x16x32_bf16 v[64:67], a[144:147], v[136:139], v[64:67]\n"
    "ds_read_b128 v[200:203], v29 offset:12288\n"
    "v_mfma_f32_16x16x32_bf16 v[64:67], a[148:151], v[140:143], v[64:67]\n"
    "ds_read_b128 v[204:207], v29 offset:14336\n"
    "v_mfma_f32_16x16x32_bf16 v[68:71], a[156:159], v[140:143], v[68:71]\n"
    "ds_read_b128 v[208:211], v29 offset:9216\n"
    "v_mfma_f32_16x16x32_bf16 v[68:71], a[152:155], v[136:139], v[68:71]\n"
    "ds_read_b128 v[212:215], v29 offset:11264\n"
    "v_mfma_f32_16x16x32_bf16 v[68:71], a[140:143], v[132:135], v[68:71]\n"
    "ds_read_b128 v[216:219], v29 offset:13312\n"
    "v_mfma_f32_16x16x32_bf16 v[68:71], a[136:139], v[128:131], v[68:71]\n"
    "ds_read_b128 v[220:223], v29 offset:15360\n"
    "v_mfma_f32_16x16x32_bf16 v[76:79], a[136:139], v[144:147], v[76:79]\n"
    "v_mfma_f32_16x16x32_bf16 v[76:79], a[140:143], v[148:151], v[76:79]\n"
    "v_mfma_f32_16x16x32_bf16 v[76:79], a[152:155], v[152:155], v[76:79]\n"
    "v_mfma_f32_16x16x32_bf16 v[76:79], a[156:159], v[156:159], v[76:79]\n"
    "v_mfma_f32_16x16x32_bf16 v[72:75], a[148:151], v[156:159], v[72:75]\n"
    "v_mfma_f32_16x16x32_bf16 v[72:75], a[144:147], v[152:155], v[72:75]\n"
    "v_mfma_f32_16x16x32_bf16 v[72:75], a[132:135], v[148:151], v[72:75]\n"
    "v_mfma_f32_16x16x32_bf16 v[72:75], a[128:131], v[144:147], v[72:75]\n"
    "s_waitcnt lgkmcnt(0)\n"
    "v_mfma_f32_16x16x32_bf16 v[64:67], a[160:163], v[192:195], v[64:67]\n"
    "ds_read_b128 v[128:131], v27 offset:16384\n"
    "v_mfma_f32_16x16x32_bf16 v[64:67], a[164:167], v[196:199], v[64:67]\n"
    "ds_read_b128 v[132:135], v27 offset:18432\n"
    "v_mfma_f32_16x16x32_bf16 v[64:67], a[176:179], v[200:203], v[64:67]\n"
    "ds_read_b128 v[136:139], v27 offset:20480\n"
    "v_mfma_f32_16x16x32_bf16 v[64:67], a[180:183], v[204:207], v[64:67]\n"
    "ds_read_b128 v[140:143], v27 offset:22528\n"
    "v_mfma_f32_16x16x32_bf16 v[68:71], a[188:191], v[204:207], v[68:71]\n"
    "ds_read_b128 v[144:147], v27 offset:17408\n"
    "v_mfma_f32_16x16x32_bf16 v[68:71], a[184:187], v[200:203], v[68:71]\n"
    "ds_read_b128 v[148:151], v27 offset:19456\n"
    "v_mfma_f32_16x16x32_bf16 v[68:71], a[172:175], v[196:199], v[68:71]\n"
    "ds_read_b128 v[152:155], v27 offset:21504\n"
    "v_mfma_f32_16x16x32_bf16 v[68:71], a[168:171], v[192:195], v[68:71]\n"
    "ds_read_b128 v[156:159], v27 offset:23552\n"
    "v_mfma_f32_16x16x32_bf16 v[76:79], a[168:171], v[208:211], v[76:79]\n"
    "v_mfma_f32_16x16x32_bf16 v[76:79], a[172:175], v[212:215], v[76:79]\n"
    "v_mfma_f32_16x16x32_bf16 v[76:79], a[184:187], v[216:219], v[76:79]\n"
    "v_mfma_f32_16x16x32_bf16 v[76:79], a[188:191], v[220:223], v[76:79]\n"
    "v_mfma_f32_16x16x32_bf16 v[72:75], a[180:183], v[220:223], v[72:75]\n"
    "v_mfma_f32_16x16x32_bf16 v[72:75], a[176:179], v[216:219], v[72:75]\n"
    "v_mfma_f32_16x16x32_bf16 v[72:75], a[164:167], v[212:215], v[72:75]\n"
    "v_mfma_f32_16x16x32_bf16 v[72:75], a[160:163], v[208:211], v[72:75]\n"
    "s_waitcnt lgkmcnt(0)\n"
    "s_mov_b32 s93, 1048576\n"
    "s_mov_b64 exec, 7\n"
    "Lpr_%=:\n"
    "buffer_load_dwordx2 v[4:5], v43, s[56:59], 0 offen sc1\n"
    "s_sub_u32 s93, s93, 1\n"
    "s_cbranch_scc1 Lprd_%=\n"
    "s_waitcnt vmcnt(0)\n"
    "v_cmp_ne_u64 vcc, s[24:25], v[4:5]\n"
    "s_cbranch_vccnz Lpr_%=\n"
    "Lprd_%=:\n"
    "s_waitcnt vmcnt(0)\n"
    "s_mov_b64 exec, -1\n"
    "s_add_u32 s18, s64, 4096\n"
    "buffer_load_dwordx4 v[160:163], v41, s[56:59], s64 offen offset:0 sc1\n"
    "buffer_load_dwordx4 v[164:167], v41, s[56:59], s64 offen offset:1024 sc1\n"
    "buffer_load_dwordx4 v[168:171], v41, s[56:59], s64 offen offset:2048 sc1\n"
    "buffer_load_dwordx4 v[172:175], v41, s[56:59], s64 offen offset:3072 sc1\n"
    "buffer_load_dwordx4 v[176:179], v41, s[56:59], s18 offen offset:0 sc1\n"
    "buffer_load_dwordx4 v[180:183], v41, s[56:59], s18 offen offset:1024 sc1\n"
    "v_mfma_f32_16x16x32_bf16 v[64:67], a[192:195], v[128:131], v[64:67]\n"
    "ds_read_b128 v[192:195], v27 offset:24576\n"
    "v_mfma_f32_16x16x32_bf16 v[64:67], a[196:199], v[132:135], v[64:67]\n"
    "ds_read_b128 v[196:199], v27 offset:26624\n"
    "v_mfma_f32_16x16x32_bf16 v[64:67], a[208:211], v[136:139], v[64:67]\n"
    "ds_read_b128 v[200:203], v27 offset:28672\n"
    "v_mfma_f32_16x16x32_bf16 v[64:67], a[212:215], v[140:143], v[64:67]\n"
    "ds_read_b128 v[204:207], v27 offset:30720\n"
    "v_mfma_f32_16x16x32_bf16 v[68:71], a[220:223], v[140:143], v[68:71]\n"
    "ds_read_b128 v[208:211], v27 offset:25600\n"
    "v_mfma_f32_16x16x32_bf16 v[68:71], a[216:219], v[136:139], v[68:71]\n"
    "ds_read_b128 v[212:215], v27 offset:27648\n"
    "v_mfma_f32_16x16x32_bf16 v[68:71], a[204:207], v[132:135], v[68:71]\n"
    "ds_read_b128 v[216:219], v27 offset:29696\n"
    "v_mfma_f32_16x16x32_bf16 v[68:71], a[200:203], v[128:131], v[68:71]\n"
    "ds_read_b128 v[220:223], v27 offset:31744\n"
    "v_mfma_f32_16x16x32_bf16 v[76:79], a[200:203], v[144:147], v[76:79]\n"
    "v_mfma_f32_16x16x32_bf16 v[76:79], a[204:207], v[148:151], v[76:79]\n"
    "v_mfma_f32_16x16x32_bf16 v[76:79], a[216:219], v[152:155], v[76:79]\n"
    "v_mfma_f32_16x16x32_bf16 v[76:79], a[220:223], v[156:159], v[76:79]\n"
    "v_mfma_f32_16x16x32_bf16 v[72:75], a[212:215], v[156:159], v[72:75]\n"
    "v_mfma_f32_16x16x32_bf16 v[72:75], a[208:211], v[152:155], v[72:75]\n"
    "v_mfma_f32_16x16x32_bf16 v[72:75], a[196:199], v[148:151], v[72:75]\n"
    "v_mfma_f32_16x16x32_bf16 v[72:75], a[192:195], v[144:147], v[72:75]\n"
    "s_waitcnt lgkmcnt(0)\n"
    "v_mfma_f32_16x16x32_bf16 v[64:67], a[224:227], v[192:195], v[64:67]\n"
    "v_mfma_f32_16x16x32_bf16 v[64:67], a[228:231], v[196:199], v[64:67]\n"
    "v_mfma_f32_16x16x32_bf16 v[64:67], a[240:243], v[200:203], v[64:67]\n"
    "v_mfma_f32_16x16x32_bf16 v[64:67], a[244:247], v[204:207], v[64:67]\n"
    "v_mfma_f32_16x16x32_bf16 v[68:71], a[252:255], v[204:207], v[68:71]\n"
    "v_mfma_f32_16x16x32_bf16 v[68:71], a[248:251], v[200:203], v[68:71]\n"
    "v_mfma_f32_16x16x32_bf16 v[68:71], a[236:239], v[196:199], v[68:71]\n"
    "v_mfma_f32_16x16x32_bf16 v[68:71], a[232:235], v[192:195], v[68:71]\n"
    "v_mfma_f32_16x16x32_bf16 v[76:79], a[232:235], v[208:211], v[76:79]\n"
    "v_mfma_f32_16x16x32_bf16 v[76:79], a[236:239], v[212:215], v[76:79]\n"
    "v_mfma_f32_16x16x32_bf16 v[76:79], a[248:251], v[216:219], v[76:79]\n"
    "v_mfma_f32_16x16x32_bf16 v[76:79], a[252:255], v[220:223], v[76:79]\n"
    "v_mfma_f32_16x16x32_bf16 v[72:75], a[244:247], v[220:223], v[72:75]\n"
    "v_mfma_f32_16x16x32_bf16 v[72:75], a[240:243], v[216:219], v[72:75]\n"
    "v_mfma_f32_16x16x32_bf16 v[72:75], a[228:231], v[212:215], v[72:75]\n"
    "v_mfma_f32_16x16x32_bf16 v[72:75], a[224:227], v[208:211], v[72:75]\n"
    "s_mov_b32 s21, 0xffff0000\n"
    "s_mov_b32 s22, 65536\n"
    "s_mov_b32 s19, 0x3f80\n"
    "s_mov_b32 s20, 0x3f800000\n"
    "s_waitcnt vmcnt(5)\n"
    "v_lshlrev_b32 v4, 16, v160\n"
    "v_and_b32 v5, s21, v160\n"
    "v_pk_add_f32 v[64:65], v[64:65], v[4:5]\n"
    "v_lshlrev_b32 v6, 16, v161\n"
    "v_and_b32 v7, s21, v161\n"
    "v_pk_add_f32 v[66:67], v[66:67], v[6:7]\n"
    "v_lshlrev_b32 v4, 16, v162\n"
    "v_and_b32 v5, s21, v162\n"
    "v_pk_add_f32 v[68:69], v[68:69], v[4:5]\n"
    "v_lshlrev_b32 v6, 16, v163\n"
    "v_and_b32 v7, s21, v163\n"
    "v_pk_add_f32 v[70:71], v[70:71], v[6:7]\n"
    "s_waitcnt vmcnt(4)\n"
    "v_lshlrev_b32 v4, 16, v164\n"
    "v_and_b32 v5, s21, v164\n"
    "v_pk_add_f32 v[72:73], v[72:73], v[4:5]\n"
    "v_lshlrev_b32 v6, 16, v165\n"
    "v_and_b32 v7, s21, v165\n"
    "v_pk_add_f32 v[74:75], v[74:75], v[6:7]\n"
    "v_lshlrev_b32 v4, 16, v166\n"
    "v_and_b32 v5, s21, v166\n"
    "v_pk_add_f32 v[76:77], v[76:77], v[4:5]\n"
    "v_lshlrev_b32 v6, 16, v167\n"
    "v_and_b32 v7, s21, v167\n"
    "v_pk_add_f32 v[78:79], v[78:79], v[6:7]\n"
    "s_waitcnt vmcnt(3)\n"
    "v_lshlrev_b32 v4, 16, v168\n"
    "v_and_b32 v5, s21, v168\n"
    "v_pk_add_f32 v[64:65], v[64:65], v[4:5]\n"
    "v_lshlrev_b32 v6, 16, v169\n"
    "v_and_b32 v7, s21, v169\n"
    "v_pk_add_f32 v[66:67], v[66:67], v[6:7]\n"
    "v_lshlrev_b32 v4, 16, v170\n"
    "v_and_b32 v5, s21, v170\n"
    "v_pk_add_f32 v[68:69], v[68:69], v[4:5]\n"
    "v_lshlrev_b32 v6, 16, v171\n"
    "v_and_b32 v7, s21, v171\n"
    "v_pk_add_f32 v[70:71], v[70:71], v[6:7]\n"
    "s_waitcnt vmcnt(2)\n"
    "v_lshlrev_b32 v4, 16, v172\n"
    "v_and_b32 v5, s21, v172\n"
    "v_pk_add_f32 v[72:73], v[72:73], v[4:5]\n"
    "v_lshlrev_b32 v6, 16, v173\n"
    "v_and_b32 v7, s21, v173\n"
    "v_pk_add_f32 v[74:75], v[74:75], v[6:7]\n"
    "v_lshlrev_b32 v4, 16, v174\n"
    "v_and_b32 v5, s21, v174\n"
    "v_pk_add_f32 v[76:77], v[76:77], v[4:5]\n"
    "v_lshlrev_b32 v6, 16, v175\n"
    "v_and_b32 v7, s21, v175\n"
    "v_pk_add_f32 v[78:79], v[78:79], v[6:7]\n"
    "s_waitcnt vmcnt(1)\n"
    "v_lshlrev_b32 v4, 16, v176\n"
    "v_and_b32 v5, s21, v176\n"
    "v_pk_add_f32 v[64:65], v[64:65], v[4:5]\n"
    "v_lshlrev_b32 v6, 16, v177\n"
    "v_and_b32 v7, s21, v177\n"
    "v_pk_add_f32 v[66:67], v[66:67], v[6:7]\n"
    "v_lshlrev_b32 v4, 16, v178\n"
    "v_and_b32 v5, s21, v178\n"
    "v_pk_add_f32 v[68:69], v[68:69], v[4:5]\n"
    "v_lshlrev_b32 v6, 16, v179\n"
    "v_and_b32 v7, s21, v179\n"
    "v_pk_add_f32 v[70:71], v[70:71], v[6:7]\n"
    "v_cvt_pk_bf16_f32 v224, v64, v65\n"
    "v_cvt_pk_bf16_f32 v225, v66, v67\n"
    "v_cvt_pk_bf16_f32 v226, v68, v69\n"
    "v_cvt_pk_bf16_f32 v227, v70, v71\n"
    "s_waitcnt vmcnt(0)\n"
    "v_lshlrev_b32 v4, 16, v180\n"
    "v_and_b32 v5, s21, v180\n"
    "v_pk_add_f32 v[72:73], v[72:73], v[4:5]\n"
    "v_lshlrev_b32 v6, 16, v181\n"
    "v_and_b32 v7, s21, v181\n"
    "v_pk_add_f32 v[74:75], v[74:75], v[6:7]\n"
    "v_lshlrev_b32 v4, 16, v182\n"
    "v_and_b32 v5, s21, v182\n"
    "v_pk_add_f32 v[76:77], v[76:77], v[4:5]\n"
    "v_lshlrev_b32 v6, 16, v183\n"
    "v_and_b32 v7, s21, v183\n"
    "v_pk_add_f32 v[78:79], v[78:79], v[6:7]\n"
    "v_cvt_pk_bf16_f32 v228, v72, v73\n"
    "v_cvt_pk_bf16_f32 v229, v74, v75\n"
    "v_cvt_pk_bf16_f32 v230, v76, v77\n"
    "v_cvt_pk_bf16_f32 v231, v78, v79\n"
    "v_permlane16_swap_b32 v224, v226\n"
    "v_permlane16_swap_b32 v225, v227\n"
    "buffer_store_dwordx4 v[224:227], v49, s[60:63], 0 offen sc0 sc1\n"
    "v_permlane16_swap_b32 v228, v230\n"
    "v_permlane16_swap_b32 v229, v231\n"
    "buffer_store_dwordx4 v[228:231], v49, s[60:63], s22 offen sc0 sc1\n"
    "s_and_b32 s21, s10, 0x3b\n"
    "s_or_b32 s21, s21, s11\n"
    "s_cmp_lg_u32 s21, 0\n"
    "s_cbranch_scc1 Lnoep_%=\n"
    "s_lshl_b32 s21, s16, 2\n"
    "s_add_u32 s21, s21, s13\n"
    "s_lshl_b32 s21, s21, 8\n"
    "v_mov_b32 v42, s21\n"
    "v_and_b32 v6, 7, v1\n"
    "v_lshl_add_u32 v42, v6, 4, v42\n"
    "v_sub_u32 v6, 7, v1\n"
    "v_ashrrev_i32 v6, 31, v6\n"
    "v_lshl_or_b32 v42, v6, 8, v42\n"
    "buffer_store_dwordx4 v[56:59], v42, s[56:59], 0 offen sc1\n"
    "Lnoep_%=:\n"
    "Lend_%=:\n"
    "s_endpgm\n"
    "Lredo_%=:\n"
    "s_waitcnt vmcnt(0) lgkmcnt(0)\n"
    "s_nop 7\n"
    "s_barrier\n"
    "s_mov_b32 s94, -1\n"
    "s_mov_b32 s17, 0\n"
    "v_mov_b32 v52, 0xff7fffff\n"
    "v_mov_b32 v53, 0xff7fffff\n"
    "v_mov_b32 v54, 0xff7fffff\n"
    "v_mov_b32 v55, 0xff7fffff\n"
    "Lmaxp_%=:\n"
    "s_mov_b32 m0, s78\n"
    "s_nop 0\n"
    "buffer_load_dwordx4 v33, s[44:47], s17 offen offset:0 lds\n"
    "buffer_load_dwordx4 v34, s[44:47], s17 offen offset:1024 lds\n"
    "buffer_load_dwordx4 v35, s[44:47], s17 offen offset:2048 lds\n"
    "buffer_load_dwordx4 v36, s[44:47], s17 offen offset:3072 lds\n"
    "s_waitcnt vmcnt(0)\n"
    "s_barrier\n"
    "ds_read_b128 a[64:67], v160 offset:0\n"
    "ds_read_b128 a[68:71], v161 offset:0\n"
    "ds_read_b128 a[72:75], v162 offset:0\n"
    "ds_read_b128 a[76:79], v163 offset:0\n"
    "ds_read_b128 a[80:83], v160 offset:4096\n"
    "ds_read_b128 a[84:87], v161 offset:4096\n"
    "ds_read_b128 a[88:91], v162 offset:4096\n"
    "ds_read_b128 a[92:95], v163 offset:4096\n"
    "s_waitcnt lgkmcnt(0)\n"
    "s_barrier\n"
    "v_mfma_f32_16x16x32_bf16 v[128:131], a[64:67], a[0:3], 0\n"
    "v_mfma_f32_16x16x32_bf16 v[128:131], a[68:71], a[4:7], v[128:131]\n"
    "v_mfma_f32_16x16x32_bf16 v[128:131], a[72:75], a[8:11], v[128:131]\n"
    "v_mfma_f32_16x16x32_bf16 v[128:131], a[76:79], a[12:15], v[128:131]\n"
    "v_mfma_f32_16x16x32_bf16 v[132:135], a[64:67], a[16:19], 0\n"
    "v_mfma_f32_16x16x32_bf16 v[132:135], a[68:71], a[20:23], v[132:135]\n"
    "v_mfma_f32_16x16x32_bf16 v[132:135], a[72:75], a[24:27], v[132:135]\n"
    "v_mfma_f32_16x16x32_bf16 v[132:135], a[76:79], a[28:31], v[132:135]\n"
    "v_mfma_f32_16x16x32_bf16 v[136:139], a[64:67], a[32:35], 0\n"
    "v_mfma_f32_16x16x32_bf16 v[136:139], a[68:71], a[36:39], v[136:139]\n"
    "v_mfma_f32_16x16x32_bf16 v[136:139], a[72:75], a[40:43], v[136:139]\n"
    "v_mfma_f32_16x16x32_bf16 v[136:139], a[76:79], a[44:47], v[136:139]\n"
    "v_mfma_f32_16x16x32_bf16 v[140:143], a[64:67], a[48:51], 0\n"
    "v_mfma_f32_16x16x32_bf16 v[140:143], a[68:71], a[52:55], v[140:143]\n"
    "v_mfma_f32_16x16x32_bf16 v[140:143], a[72:75], a[56:59], v[140:143]\n"
    "v_mfma_f32_16x16x32_bf16 v[140:143], a[76:79], a[60:63], v[140:143]\n"
    "s_nop 7\n"
    "v_max3_f32 v52, v52, v128, v129\n"
    "v_max3_f32 v52, v52, v130, v131\n"
    "v_max3_f32 v53, v53, v132, v133\n"
    "v_max3_f32 v53, v53, v134, v135\n"
    "v_max3_f32 v54, v54, v136, v137\n"
    "v_max3_f32 v54, v54, v138, v139\n"
    "v_max3_f32 v55, v55, v140, v141\n"
    "v_max3_f32 v55, v55, v142, v143\n"
    "v_mfma_f32_16x16x32_bf16 v[128:131], a[80:83], a[0:3], 0\n"
    "v_mfma_f32_16x16x32_bf16 v[128:131], a[84:87], a[4:7], v[128:131]\n"
    "v_mfma_f32_16x16x32_bf16 v[128:131], a[88:91], a[8:11], v[128:131]\n"
    "v_mfma_f32_16x16x32_bf16 v[128:131], a[92:95], a[12:15], v[128:131]\n"
    "v_mfma_f32_16x16x32_bf16 v[132:135], a[80:83], a[16:19], 0\n"
    "v_mfma_f32_16x16x32_bf16 v[132:135], a[84:87], a[20:23], v[132:135]\n"
    "v_mfma_f32_16x16x32_bf16 v[132:135], a[88:91], a[24:27], v[132:135]\n"
    "v_mfma_f32_16x16x32_bf16 v[132:135], a[92:95], a[28:31], v[132:135]\n"
    "v_mfma_f32_16x16x32_bf16 v[136:139], a[80:83], a[32:35], 0\n"
    "v_mfma_f32_16x16x32_bf16 v[136:139], a[84:87], a[36:39], v[136:139]\n"
    "v_mfma_f32_16x16x32_bf16 v[136:139], a[88:91], a[40:43], v[136:139]\n"
    "v_mfma_f32_16x16x32_bf16 v[136:139], a[92:95], a[44:47], v[136:139]\n"
    "v_mfma_f32_16x16x32_bf16 v[140:143], a[80:83], a[48:51], 0\n"
    "v_mfma_f32_16x16x32_bf16 v[140:143], a[84:87], a[52:55], v[140:143]\n"
    "v_mfma_f32_16x16x32_bf16 v[140:143], a[88:91], a[56:59], v[140:143]\n"
    "v_mfma_f32_16x16x32_bf16 v[140:143], a[92:95], a[60:63], v[140:143]\n"
    "s_nop 7\n"
    "v_max3_f32 v52, v52, v128, v129\n"
    "v_max3_f32 v52, v52, v130, v131\n"
    "v_max3_f32 v53, v53, v132, v133\n"
    "v_max3_f32 v53, v53, v134, v135\n"
    "v_max3_f32 v54, v54, v136, v137\n"
    "v_max3_f32 v54, v54, v138, v139\n"
    "v_max3_f32 v55, v55, v140, v141\n"
    "v_max3_f32 v55, v55, v142, v143\n"
    "s_add_u32 s17, s17, 8192\n"
    "s_cmp_lt_u32 s17, 0x20000\n"
    "s_cbranch_scc1 Lmaxp_%=\n"
    "v_mov_b32 v56, v52\n"
    "v_mov_b32 v57, v53\n"
    "v_mov_b32 v58, v54\n"
    "v_mov_b32 v59, v55\n"
    "v_permlane16_swap_b32 v52, v56\n"
    "v_permlane16_swap_b32 v53, v57\n"
    "v_permlane16_swap_b32 v54, v58\n"
    "v_permlane16_swap_b32 v55, v59\n"
    "v_max_f32 v52, v52, v56\n"
    "v_max_f32 v53, v53, v57\n"
    "v_max_f32 v54, v54, v58\n"
    "v_max_f32 v55, v55, v59\n"
    "v_mov_b32 v56, v52\n"
    "v_mov_b32 v57, v53\n"
    "v_mov_b32 v58, v54\n"
    "v_mov_b32 v59, v55\n"
    "v_permlane32_swap_b32 v52, v56\n"
    "v_permlane32_swap_b32 v53, v57\n"
    "v_permlane32_swap_b32 v54, v58\n"
    "v_permlane32_swap_b32 v55, v59\n"
    "v_max_f32 v52, v52, v56\n"
    "v_max_f32 v53, v53, v57\n"
    "v_max_f32 v54, v54, v58\n"
    "v_max_f32 v55, v55, v59\n"
    "s_add_u32 s28, s82, 0\n"
    "s_add_u32 m0, s78, 0\n"
    "s_nop 0\n"
    "buffer_load_dwordx4 v33, s[44:47], s28 offen offset:0 lds\n"
    "buffer_load_dwordx4 v34, s[44:47], s28 offen offset:1024 lds\n"
    "buffer_load_dwordx4 v35, s[44:47], s28 offen offset:2048 lds\n"
    "buffer_load_dwordx4 v36, s[44:47], s28 offen offset:3072 lds\n"
    "s_add_u32 s29, s83, 0\n"
    "s_add_u32 m0, s78, 16384\n"
    "s_nop 0\n"
    "buffer_load_dwordx4 v33, s[44:47], s29 offen offset:0 lds\n"
    "buffer_load_dwordx4 v34, s[44:47], s29 offen offset:1024 lds\n"
    "buffer_load_dwordx4 v35, s[44:47], s29 offen offset:2048 lds\n"
    "buffer_load_dwordx4 v36, s[44:47], s29 offen offset:3072 lds\n"
    "s_add_u32 s30, s82, 16384\n"
    "s_add_u32 m0, s78, 32768\n"
    "s_nop 0\n"
    "buffer_load_dwordx4 v33, s[44:47], s30 offen offset:0 lds\n"
    "buffer_load_dwordx4 v34, s[44:47], s30 offen offset:1024 lds\n"
    "buffer_load_dwordx4 v35, s[44:47], s30 offen offset:2048 lds\n"
    "buffer_load_dwordx4 v36, s[44:47], s30 offen offset:3072 lds\n"
    "s_add_u32 s31, s84, 0\n"
    "s_add_u32 m0, s78, 49152\n"
    "s_nop 0\n"
    "buffer_load_dwordx4 v37, s[48:51], s31 offen offset:0 lds\n"
    "buffer_load_dwordx4 v38, s[48:51], s31 offen offset:1024 lds\n"
    "buffer_load_dwordx4 v39, s[48:51], s31 offen offset:2048 lds\n"
    "buffer_load_dwordx4 v40, s[48:51], s31 offen offset:3072 lds\n"
    "s_add_u32 s28, s83, 16384\n"
    "s_add_u32 m0, s78, 65536\n"
    "s_nop 0\n"
    "buffer_load_dwordx4 v33, s[44:47], s28 offen offset:0 lds\n"
    "buffer_load_dwordx4 v34, s[44:47], s28 offen offset:1024 lds\n"
    "buffer_load_dwordx4 v35, s[44:47], s28 offen offset:2048 lds\n"
    "buffer_load_dwordx4 v36, s[44:47], s28 offen offset:3072 lds\n"
    "s_waitcnt vmcnt(0)\n"
    "s_barrier\n"
    "ds_read_b128 a[64:67], v160 offset:0\n"
    "ds_read_b128 a[68:71], v161 offset:0\n"
    "ds_read_b128 a[72:75], v162 offset:0\n"
    "ds_read_b128 a[76:79], v163 offset:0\n"
    "ds_read_b128 a[80:83], v160 offset:4096\n"
    "ds_read_b128 a[84:87], v161 offset:4096\n"
    "ds_read_b128 a[88:91], v162 offset:4096\n"
    "ds_read_b128 a[92:95], v163 offset:4096\n"
    "s_waitcnt lgkmcnt(0)\n"
    "s_branch Lattn_%=\n"
    ".p2align 12\n"
    :
    : "s"(__builtin_amdgcn_kernarg_segment_ptr()), "s"(blockIdx.x), "v"(threadIdx.x), "s"(ldsbase)
    : "v1", "v2", "v3", "v4", "v5", "v6", "v7", "v8", "v9", "v10", "v11", "v12", "v13", "v14", "v15", "v16", "v17", "v18", "v19", "v20", "v21", "v22", "v23", "v24", "v25", "v26", "v27", "v28", "v29", "v30", "v31", "v32", "v33", "v34", "v35", "v36", "v37", "v38", "v39", "v40", "v41", "v42", "v43", "v44", "v45", "v46", "v47", "v48", "v49", "v50", "v51", "v52", "v53", "v54", "v55", "v56", "v57", "v58", "v59", "v60", "v61", "v62", "v63", "v64", "v65", "v66", "v67", "v68", "v69", "v70", "v71", "v72", "v73", "v74", "v75", "v76", "v77", "v78", "v79", "v80", "v81", "v82", "v83", "v84", "v85", "v86", "v87", "v88", "v89", "v90", "v91", "v92", "v93", "v94", "v95", "v96", "v97", "v98", "v99", "v100", "v101", "v102", "v103", "v104", "v105", "v106", "v107", "v108", "v109", "v110", "v111", "v112", "v113", "v114", "v115", "v116", "v117", "v118", "v119", "v120", "v121", "v122", "v123", "v124", "v125", "v126", "v127", "v128", "v129", "v130", "v131", "v132", "v133", "v134", "v135", "v136", "v137", "v138", "v139", "v140", "v141", "v142", "v143", "v144", "v145", "v146", "v147", "v148", "v149", "v150", "v151", "v152", "v153", "v154", "v155", "v156", "v157", "v158", "v159", "v160", "v161", "v162", "v163", "v164", "v165", "v166", "v167", "v168", "v169", "v170", "v171", "v172", "v173", "v174", "v175", "v176", "v177", "v178", "v179", "v180", "v181", "v182", "v183", "v184", "v185", "v186", "v187", "v188", "v189", "v190", "v191", "v192", "v193", "v194", "v195", "v196", "v197", "v198", "v199", "v200", "v201", "v202", "v203", "v204", "v205", "v206", "v207", "v208", "v209", "v210", "v211", "v212", "v213", "v214", "v215", "v216", "v217", "v218", "v219", "v220", "v221", "v222", "v223", "v224", "v225", "v226", "v227", "v228", "v229", "v230", "v231", "v232", "v233", "v234", "v235", "v236", "v237", "v238", "v239", "v240", "v241", "v242", "v243", "v244", "v245", "v246", "v247", "v248", "v249", "v250", "v251", "v252", "v253", "v254", "v255", "a0", "a1", "a2", "a3", "a4", "a5", "a6", "a7", "a8", "a9", "a10", "a11", "a12", "a13", "a14", "a15", "a16", "a17", "a18", "a19", "a20", "a21", "a22", "a23", "a24", "a25", "a26", "a27", "a28", "a29", "a30", "a31", "a32", "a33", "a34", "a35", "a36", "a37", "a38", "a39", "a40", "a41", "a42", "a43", "a44", "a45", "a46", "a47", "a48", "a49", "a50", "a51", "a52", "a53", "a54", "a55", "a56", "a57", "a58", "a59", "a60", "a61", "a62", "a63", "a64", "a65", "a66", "a67", "a68", "a69", "a70", "a71", "a72", "a73", "a74", "a75", "a76", "a77", "a78", "a79", "a80", "a81", "a82", "a83", "a84", "a85", "a86", "a87", "a88", "a89", "a90", "a91", "a92", "a93", "a94", "a95", "a96", "a97", "a98", "a99", "a100", "a101", "a102", "a103", "a104", "a105", "a106", "a107", "a108", "a109", "a110", "a111", "a112", "a113", "a114", "a115", "a116", "a117", "a118", "a119", "a120", "a121", "a122", "a123", "a124", "a125", "a126", "a127", "a128", "a129", "a130", "a131", "a132", "a133", "a134", "a135", "a136", "a137", "a138", "a139", "a140", "a141", "a142", "a143", "a144", "a145", "a146", "a147", "a148", "a149", "a150", "a151", "a152", "a153", "a154", "a155", "a156", "a157", "a158", "a159", "a160", "a161", "a162", "a163", "a164", "a165", "a166", "a167", "a168", "a169", "a170", "a171", "a172", "a173", "a174", "a175", "a176", "a177", "a178", "a179", "a180", "a181", "a182", "a183", "a184", "a185", "a186", "a187", "a188", "a189", "a190", "a191", "a192", "a193", "a194", "a195", "a196", "a197", "a198", "a199", "a200", "a201", "a202", "a203", "a204", "a205", "a206", "a207", "a208", "a209", "a210", "a211", "a212", "a213", "a214", "a215", "a216", "a217", "a218", "a219", "a220", "a221", "a222", "a223", "a224", "a225", "a226", "a227", "a228", "a229", "a230", "a231", "a232", "a233", "a234", "a235", "a236", "a237", "a238", "a239", "a240", "a241", "a242", "a243", "a244", "a245", "a246", "a247", "a248", "a249", "a250", "a251", "a252", "a253", "a254", "a255", "s4", "s5", "s6", "s7", "s8", "s9", "s10", "s11", "s12", "s13", "s14", "s15", "s16", "s17", "s18", "s19", "s20", "s21", "s22", "s23", "s24", "s25", "s26", "s27", "s28", "s29", "s30", "s31", "s36", "s37", "s38", "s39", "s40", "s41", "s42", "s43", "s44", "s45", "s46", "s47", "s48", "s49", "s50", "s51", "s52", "s53", "s54", "s55", "s56", "s57", "s58", "s59", "s60", "s61", "s62", "s63", "s64", "s65", "s66", "s67", "s68", "s69", "s70", "s71", "s72", "s73", "s74", "s75", "s76", "s77", "s78", "s79", "s80", "s81", "s82", "s83", "s84", "s85", "s86", "s87", "s88", "s89", "s90", "s91", "s92", "s93", "s94", "s95", "s96", "s97", "s98", "s99", "vcc", "scc", "memory");
}

extern "C" void kernel_launch(void* const* d_in, const int* in_sizes, int n_in, void* d_out, int out_size, void* d_ws, size_t ws_size,
                              hipStream_t stream) {
  gqa2_fused<<<256, 256, 0, stream>>>(d_in[0], d_in[1], d_in[2], d_in[3], d_in[4], d_in[5], d_in[6], d_in[7], d_in[8], d_in[9], d_out, d_ws);
}
```
